```python
import torch

def _softmax(x, axis=-1):
    return torch.softmax(x, dim=axis)
def _gelu(x):
    return torch.nn.functional.gelu(x, approximate="tanh")
def _silu(x):
    return torch.nn.functional.silu(x)
def _sigmoid(x):
    return torch.sigmoid(x)
def _layernorm(x, axis=-1, eps=1e-5):
    m = x.mean(dim=axis, keepdim=True)
    v = x.var(dim=axis, keepdim=True, unbiased=False)
    return (x - m) / torch.sqrt(v + eps)
def _scatter_add(tgt, idx, src, axis=0):
    return tgt.clone().index_add_(axis, idx, src)
def _segment_sum(src, seg, n):
    out = torch.zeros((n,) + src.shape[1:], dtype=src.dtype)
    return out.index_add_(0, seg, src)
def _pad(x, pw):
    flat = tuple(v for pair in reversed(pw) for v in pair)
    return torch.nn.functional.pad(x, flat)

kernel_name = 'interleave_layers__sliding_block__glu_gated_bilinear__prompt'

def setup_inputs():

    _w = (lambda a: (lambda t: t.to(torch.bfloat16) if t.is_floating_point() else t)(torch.as_tensor(a)))
    def _A_setup():
        B, S, D = 1, 2048, 640
        return {
            "x": _w((lambda *s: torch.randn(*s, dtype=torch.bfloat16) * 0.05)(B, S, D)),
            "wqkv": _w((lambda *s: torch.randn(*s, dtype=torch.bfloat16) * 0.05)(D, 3 * D)),
            "wo": _w((lambda *s: torch.randn(*s, dtype=torch.bfloat16) * 0.05)(D, D)),
        }
    _A_in = _A_setup()

    def _B_setup():
        B, S, D, F = 1, 2048, 640, 1536
        return {
            "x": _w((lambda *s: torch.randn(*s, dtype=torch.bfloat16) * 0.05)(B, S, D)),
            "w_gate": _w((lambda *s: torch.randn(*s, dtype=torch.bfloat16) * 0.8)(D, F) * (D ** -0.5)),
            "w_up": _w((lambda *s: torch.randn(*s, dtype=torch.bfloat16) * 0.8)(D, F) * (D ** -0.5)),
            "w_down": _w((lambda *s: torch.randn(*s, dtype=torch.bfloat16) * 0.8)(F, D) * (F ** -0.5)),
        }
    _B_in = _B_setup()

    return {
        "A_x": _A_in["x"],
        "A_wqkv": _A_in["wqkv"],
        "A_wo": _A_in["wo"],
        "B_x": _B_in["x"],
        "B_w_gate": _B_in["w_gate"],
        "B_w_up": _B_in["w_up"],
        "B_w_down": _B_in["w_down"],
    }

def reference(A_x, A_wqkv, A_wo, B_x, B_w_gate, B_w_up, B_w_down):
    def _A_ref(x, wqkv, wo):
        B, S, D = x.shape
        H = 32; Dh = D // H; W = 256
        qkv = (lambda x, *d: x.permute(*d))((x @ wqkv).reshape(B, S, 3, H, Dh), 2, 0, 3, 1, 4)
        q, k, v = qkv[0], qkv[1], qkv[2]
        scores = torch.einsum("bhqd,bhkd->bhqk", q, k) * (Dh ** -0.5)
        idx = torch.arange(S)
        mask = (idx[:, None] - idx[None, :] >= 0) & (idx[:, None] - idx[None, :] < W)
        scores = torch.where(mask[None, None], scores, -1e9)
        p = _softmax(scores, axis=-1)
        o = torch.einsum("bhqk,bhkd->bhqd", p, v)
        return (lambda x, *d: x.permute(*d))(o, 0, 2, 1, 3).reshape(B, S, D) @ wo

    def _B_ref(x, w_gate, w_up, w_down):
        g = torch.einsum("bsd,df->bsf", x, w_gate)
        u = torch.einsum("bsd,df->bsf", x, w_up)
        h = g * u
        return torch.einsum("bsf,fd->bsd", h, w_down)

    x = A_x
    x = x + _A_ref(_layernorm(x, axis=-1), A_wqkv, A_wo)
    x = x + _B_ref(_layernorm(x, axis=-1), B_w_gate, B_w_up, B_w_down)
    x = x + _A_ref(_layernorm(x, axis=-1), A_wqkv, A_wo)
    x = x + _B_ref(_layernorm(x, axis=-1), B_w_gate, B_w_up, B_w_down)
    return x
```

```cpp
#include <hip/hip_runtime.h>
#include <hip/hip_bf16.h>
typedef __hip_bfloat16 bf16;
#define S_ 2048
#define D_ 640
#define F_ 1536
#define H_ 32
#define DH_ 20
#define W_ 256

__device__ __forceinline__ float b2f(bf16 v){ return __bfloat162float(v); }
__device__ __forceinline__ bf16 f2b(float v){ return __float2bfloat16(v); }

__global__ void k_ln(const bf16* x, bf16* y){
  int row = blockIdx.x; int l = threadIdx.x;
  float v[10]; float s=0;
  for(int i=0;i<10;i++){ v[i]=b2f(x[row*D_+l+64*i]); s+=v[i]; }
  for(int m=32;m>=1;m>>=1) s+=__shfl_xor(s,m);
  float mean=s/D_; float q=0;
  for(int i=0;i<10;i++){ float d=v[i]-mean; q+=d*d; }
  for(int m=32;m>=1;m>>=1) q+=__shfl_xor(q,m);
  float r=rsqrtf(q/D_+1e-5f);
  for(int i=0;i<10;i++) y[row*D_+l+64*i]=f2b((v[i]-mean)*r);
}
__global__ void k_gemm(const bf16* A,const bf16* B,const bf16* B2,const bf16* R,bf16* C,int M,int N,int K,int mode){
  __shared__ float As[16][17], Bs[16][17], B2s[16][17];
  int tx=threadIdx.x&15, ty=threadIdx.x>>4;
  int row=blockIdx.y*16+ty, col=blockIdx.x*16+tx;
  float acc=0, acc2=0;
  for(int k0=0;k0<K;k0+=16){
    As[ty][tx]=b2f(A[row*K+k0+tx]);
    Bs[ty][tx]=b2f(B[(k0+ty)*N+col]);
    if(mode==2) B2s[ty][tx]=b2f(B2[(k0+ty)*N+col]);
    __syncthreads();
    for(int k=0;k<16;k++){ acc+=As[ty][k]*Bs[k][tx]; if(mode==2) acc2+=As[ty][k]*B2s[k][tx]; }
    __syncthreads();
  }
  if(mode==0) C[row*N+col]=f2b(acc);
  else if(mode==1) C[row*N+col]=f2b(b2f(R[row*N+col])+b2f(f2b(acc)));
  else C[row*N+col]=f2b(b2f(f2b(acc))*b2f(f2b(acc2)));
}
__global__ void k_attn(const bf16* qkv, bf16* o){
  int row=blockIdx.x, h=blockIdx.y, l=threadIdx.x;
  float q[DH_];
  for(int d=0;d<DH_;d++) q[d]=b2f(qkv[row*1920+h*DH_+d]);
  float sc[4]; float mx=-1e30f;
  for(int i=0;i<4;i++){ int j=row-(l+64*i); float s=-1e30f;
    if(j>=0){ s=0; for(int d=0;d<DH_;d++) s+=q[d]*b2f(qkv[j*1920+640+h*DH_+d]); s*=0.22360679774997896f; }
    sc[i]=s; mx=fmaxf(mx,s);}
  for(int m=32;m>=1;m>>=1) mx=fmaxf(mx,__shfl_xor(mx,m));
  float sum=0; for(int i=0;i<4;i++){ int j=row-(l+64*i); sc[i]= j>=0? __expf(sc[i]-mx):0.f; sum+=sc[i]; }
  for(int m=32;m>=1;m>>=1) sum+=__shfl_xor(sum,m);
  float inv=1.f/sum;
  for(int d=0;d<DH_;d++){ float a=0; for(int i=0;i<4;i++){ int j=row-(l+64*i); if(j>=0) a+=sc[i]*b2f(qkv[j*1920+1280+h*DH_+d]); }
    for(int m=32;m>=1;m>>=1) a+=__shfl_xor(a,m);
    if(l==0) o[row*D_+h*DH_+d]=f2b(a*inv); }
}

extern "C" void kernel_launch(void* const* d_in, const int* in_sizes, int n_in,
                              void* d_out, int out_size, void* d_ws, size_t ws_size,
                              hipStream_t stream) {
  const bf16* x=(const bf16*)d_in[0]; const bf16* wqkv=(const bf16*)d_in[1]; const bf16* wo=(const bf16*)d_in[2];
  const bf16* wg=(const bf16*)d_in[4]; const bf16* wu=(const bf16*)d_in[5]; const bf16* wd=(const bf16*)d_in[6];
  char* ws=(char*)d_ws;
  bf16* xn=(bf16*)ws; bf16* qkv=(bf16*)(ws+(4<<20)); bf16* o=(bf16*)(ws+(16<<20)); bf16* x1=(bf16*)(ws+(20<<20)); bf16* hh=(bf16*)(ws+(24<<20)); bf16* x2=(bf16*)(ws+(32<<20));
  const bf16* cur=x;
  for(int L=0;L<2;L++){
    k_ln<<<S_,64,0,stream>>>(cur,xn);
    k_gemm<<<dim3(1920/16,S_/16),256,0,stream>>>(xn,wqkv,nullptr,nullptr,qkv,S_,1920,D_,0);
    k_attn<<<dim3(S_,H_),64,0,stream>>>(qkv,o);
    k_gemm<<<dim3(D_/16,S_/16),256,0,stream>>>(o,wo,nullptr,cur,x1,S_,D_,D_,1);
    k_ln<<<S_,64,0,stream>>>(x1,xn);
    k_gemm<<<dim3(F_/16,S_/16),256,0,stream>>>(xn,wg,wu,nullptr,hh,S_,F_,D_,2);
    bf16* dst = L==0? x2 : (bf16*)d_out;
    k_gemm<<<dim3(D_/16,S_/16),256,0,stream>>>(hh,wd,nullptr,x1,dst,S_,D_,F_,1);
    cur=x2;
  }
}
```

```cpp
#include <hip/hip_runtime.h>
#include <hip/hip_bf16.h>
#include <stdint.h>
#pragma clang diagnostic ignored "-Wint-to-pointer-cast"

typedef unsigned v4u __attribute__((ext_vector_type(4)));
typedef unsigned v2u __attribute__((ext_vector_type(2)));
typedef short s16x8 __attribute__((ext_vector_type(8)));
typedef short s16x4 __attribute__((ext_vector_type(4)));
typedef float f32x16 __attribute__((ext_vector_type(16)));
typedef float f32x4 __attribute__((ext_vector_type(4)));
typedef float f32x2 __attribute__((ext_vector_type(2)));
typedef __bf16 bf2 __attribute__((ext_vector_type(2)));
typedef unsigned short u16;
#define LAS __attribute__((address_space(3)))
#define GAS __attribute__((address_space(1)))
#define DI __device__ __forceinline__
#define INL __attribute__((always_inline))
#define RLX_AGENT __ATOMIC_RELAXED, __HIP_MEMORY_SCOPE_AGENT
#define RLX_WG __ATOMIC_RELAXED, __HIP_MEMORY_SCOPE_WORKGROUP
#define CBAR() asm volatile("" ::: "memory")
#define SCHED() __builtin_amdgcn_sched_barrier(0)

constexpr unsigned OFF_CTR = 0;
constexpr unsigned OFF_HALOF = 7 * 64 * 1024;
constexpr unsigned CTR_END = OFF_HALOF + 2 * 8 * 32 * 512;
constexpr unsigned OFF_STAMP = 64u << 20;
constexpr unsigned OFF_WQ = 1u << 20;
constexpr unsigned OFF_WO = OFF_WQ + 32 * 81920;
constexpr unsigned OFF_WG = OFF_WO + 16 * 51200;
constexpr unsigned OFF_WD = OFF_WG + 32 * 122880;
constexpr unsigned XA_T = 40960, XA_SZ = 64 * XA_T;
constexpr unsigned OFF_XA = OFF_WD + 16 * 122880;
constexpr unsigned ST_SZ = 64 * 4096;
constexpr unsigned OFF_ST = OFF_XA + 4 * XA_SZ;
constexpr unsigned OA_T = 20480, OA_SZ = 128 * OA_T;
constexpr unsigned OFF_OA = OFF_ST + 4 * ST_SZ;
constexpr unsigned HA_T = 49152, HA_SZ = 128 * HA_T;
constexpr unsigned OFF_HA = OFF_OA + 2 * OA_SZ;
constexpr unsigned KVH_T = 3072, KVH_SZ = 8 * 32 * 8 * KVH_T;
constexpr unsigned OFF_KVH = OFF_HA + 2 * HA_SZ;
constexpr unsigned OFF_CS = OFF_KVH + 2 * KVH_SZ;
constexpr unsigned WS_USED = OFF_CS + 32 * 20 * 640;
static_assert(CTR_END == 256 * 11 * 256 && CTR_END <= OFF_WQ, "");

constexpr int UNIT = 10240;
constexpr int MISC = 0, ARENA = 2048;
constexpr int KV_OFF = ARENA + 8 * UNIT, KV_T = 3072;
constexpr int L_PROG = MISC, L_LANDED = MISC + 64, L_RDY = MISC + 68, L_CSUM = MISC + 76;
constexpr int L_CQ = MISC + 256, L_CG = MISC + 768, L_STAMP = ARENA + 15 * UNIT;
constexpr int LDS_BYTES = ARENA + 15 * UNIT + 4096;
#define LDEPTH 2
#define WQ_ 16
#define WG_ 12
#define WD_ 16
#define ANN 0
#define TURN0 3
constexpr int NFILL = 37, NSTAGE = 38;
constexpr unsigned SPIN_CAP = 1u << 18;

struct TabQ { static constexpr int u(int i) { return i; } };
struct TabO { static constexpr int u(int i) { return i + 1; } };
struct TabG { static constexpr int u(int i) { constexpr int t[12] = {13, 14, 0, 6, 7, 8, 9, 10, 11, 12, 1, 2}; return t[i]; } };
struct TabD { static constexpr int u(int i) { constexpr int t[12] = {3, 4, 5, 13, 14, 6, 7, 8, 9, 10, 11, 12}; return t[i]; } };

template <int V> struct IC { static constexpr int v = V; };
template <int I, int N, class F> DI void sfor(F&& f) { if constexpr (I < N) { f(IC<I>{}); sfor<I + 1, N>(f); } }
DI int fresh_lane() { int l; asm volatile("v_mbcnt_lo_u32_b32 %0, -1, 0\n\tv_mbcnt_hi_u32_b32 %0, -1, %0" : "=v"(l)); return l; }
DI int opq(int v) { asm volatile("" : "+s"(v)); return v; }
DI float asf(unsigned u) { return __uint_as_float(u); }
DI unsigned asu(float f) { return __float_as_uint(f); }
DI unsigned pk(float a, float b) { f32x2 t = {a, b}; bf2 p = __builtin_convertvector(t, bf2); return __builtin_bit_cast(unsigned, p); }
DI float rbf(float a) { return asf(pk(a, a) << 16); }
DI float ex2(float a) { return __builtin_amdgcn_exp2f(a); }
DI float dot2(unsigned a, unsigned b, float c) { return __builtin_amdgcn_fdot2_f32_bf16(__builtin_bit_cast(bf2, a), __builtin_bit_cast(bf2, b), c, false); }
DI f32x16 mfma32(v4u a, v4u b, f32x16 c) { return __builtin_amdgcn_mfma_f32_32x32x16_bf16(__builtin_bit_cast(s16x8, a), __builtin_bit_cast(s16x8, b), c, 0, 0, 0); }
DI f32x16 mfma32k8(v2u a, v2u b, f32x16 c) { return __builtin_amdgcn_mfma_f32_32x32x8bf16_1k(__builtin_bit_cast(s16x4, a), __builtin_bit_cast(s16x4, b), c, 0, 0, 0); }
DI f32x4 mfma16(v4u a, v4u b, f32x4 c) { return __builtin_amdgcn_mfma_f32_16x16x32_bf16(__builtin_bit_cast(s16x8, a), __builtin_bit_cast(s16x8, b), c, 0, 0, 0); }
DI unsigned ufl(unsigned v) { return __builtin_amdgcn_readfirstlane(v); }
DI unsigned lds_ld(unsigned a) { return __hip_atomic_load((LAS unsigned*)a, RLX_WG); }
DI void lds_st(unsigned a, unsigned v) { __hip_atomic_store((LAS unsigned*)a, v, RLX_WG); }
DI void lds_or1(unsigned a, unsigned v) { asm volatile("s_mov_b64 exec, 1\n\tds_or_b32 %0, %1\n\ts_mov_b64 exec, -1" :: "v"(a), "v"(v) : "memory"); }
DI v4u lds128(unsigned a) { return *(LAS v4u*)a; }
DI v2u lds64(unsigned a) { return *(LAS v2u*)a; }
DI v2u ldstr(unsigned a) { return __builtin_bit_cast(v2u, __builtin_amdgcn_ds_read_tr16_b64_v4i16((LAS s16x4*)a)); }
DI float sum_halves(float x) { auto r = __builtin_amdgcn_permlane32_swap(asu(x), asu(x), false, false); unsigned a = r[0], b = r[1]; return asf(a) + asf(b); }
DI float max_halves(float x) { auto r = __builtin_amdgcn_permlane32_swap(asu(x), asu(x), false, false); unsigned a = r[0], b = r[1]; return fmaxf(asf(a), asf(b)); }
DI float sum_rows16(float x) { auto r = __builtin_amdgcn_permlane16_swap(asu(x), asu(x), false, false); unsigned a = r[0], b = r[1]; return asf(a) + asf(b); }
DI float max3(float a, float b, float c) { return __builtin_elementwise_maximum(__builtin_elementwise_maximum(a, b), c); }

typedef __amdgpu_buffer_rsrc_t rsrc_t;
constexpr int SC1 = 16;
DI v4u gld128(rsrc_t r, unsigned v, unsigned s) { return __builtin_amdgcn_raw_buffer_load_b128(r, v, s, SC1); }
DI v2u gld64(rsrc_t r, unsigned v, unsigned s) { return __builtin_amdgcn_raw_buffer_load_b64(r, v, s, SC1); }
DI void gst128(v4u x, rsrc_t r, unsigned v, unsigned s) { __builtin_amdgcn_raw_buffer_store_b128(x, r, v + s, 0, SC1); }
DI void gst64(v2u x, rsrc_t r, unsigned v, unsigned s) { __builtin_amdgcn_raw_buffer_store_b64(x, r, v, s, SC1); }
template <int S> DI v4u lda(rsrc_t r, unsigned voff, unsigned base) { return gld128(r, voff + (S & 3) * 1024, base + (S >> 2) * 4096); }

#define STAMP_HI (SPIN_CAP - rg.bud)
#define STAMP(i) do {} while (0)
#define INNER 1
#define STAMP1(i) do { if (INNER == 1) STAMP(i); } while (0)
#define STAMP2(i) do { if (INNER == 2) STAMP(i); } while (0)

#define SPEND(bud) { if ((bud) >= SPIN_CAP) break; ++(bud); }
DI unsigned lds_wait_gt(unsigned a, unsigned f, unsigned& bud) {
  unsigned v;
  for (;;) {
    v = lds_ld(a);
    if (ufl(v) > f) break;
    SPEND(bud);
    __builtin_amdgcn_s_sleep(1);
  }
  CBAR();
  return v;
}
DI void lds_wait_bits(unsigned a, unsigned bits, unsigned& cache, unsigned& bud) {
  if (__builtin_expect((cache & bits) == bits, 1)) return;
  for (;;) {
    cache = ufl(lds_ld(a));
    if ((cache & bits) == bits) break;
    SPEND(bud);
    __builtin_amdgcn_s_sleep(1);
  }
  CBAR();
}
DI void prog_wait(unsigned need, unsigned& bud) {
  const int lane = fresh_lane();
  for (;;) {
    unsigned v = lds_ld(L_PROG + (lane & 7) * 4);
    if (__all(v >= need)) break;
    SPEND(bud);
    __builtin_amdgcn_s_sleep(1);
  }
  CBAR();
}
DI unsigned rmw_poll(GAS unsigned* p) {
  unsigned f;
  asm volatile("s_mov_b64 exec, 1\n\ts_nop 3\n\tglobal_atomic_or %0, %1, %1, %2 sc0\n\ts_waitcnt vmcnt(0)\n\ts_mov_b64 exec, -1" : "=&v"(f) : "v"(0u), "s"(p) : "memory");
  return ufl(f);
}
DI void ctr_wait(unsigned char* ws, int kind, int t32, int rep, unsigned& bud) {
  GAS unsigned* p = (GAS unsigned*)(ws + OFF_CTR + (kind * 64 + t32) * 1024 + rep * 256);
  for (;;) {
    if (rmw_poll(p) >= 32u) break;
    SPEND(bud);
  }
  __builtin_amdgcn_fence(__ATOMIC_ACQUIRE, "wavefront");
  CBAR();
}
DI void ctr_arrive(unsigned char* ws, int kind, int t32) {
  const int lane = fresh_lane();
  GAS unsigned* p = (GAS unsigned*)(ws + OFF_CTR + (kind * 64 + t32) * 1024);
  asm volatile("s_waitcnt vmcnt(0)\n\ts_mov_b64 exec, 15\n\ts_nop 3\n\tglobal_atomic_add %0, %1, %2\n\ts_mov_b64 exec, -1" :: "v"(lane * 256), "v"(1u), "s"(p) : "memory");
}

DI void dma_fill(v4u srd, unsigned voff, unsigned soff, unsigned ldst) {
  unsigned keep, so;
  asm volatile(
      "s_mov_b32 %0, m0\n\ts_mov_b32 m0, %5\n\ts_add_u32 %1, %4, 0x1000\n\t"
      "buffer_load_dwordx4 %2, %3, %4 offen lds\n\tbuffer_load_dwordx4 %2, %3, %4 offen offset:1024 lds\n\t"
      "buffer_load_dwordx4 %2, %3, %4 offen offset:2048 lds\n\tbuffer_load_dwordx4 %2, %3, %4 offen offset:3072 lds\n\t"
      "s_add_u32 m0, m0, 0x1000\n\ts_nop 0\n\t"
      "buffer_load_dwordx4 %2, %3, %1 offen lds\n\tbuffer_load_dwordx4 %2, %3, %1 offen offset:1024 lds\n\t"
      "buffer_load_dwordx4 %2, %3, %1 offen offset:2048 lds\n\tbuffer_load_dwordx4 %2, %3, %1 offen offset:3072 lds\n\t"
      "s_add_u32 m0, m0, 0x1000\n\ts_add_u32 %1, %1, 0x1000\n\t"
      "buffer_load_dwordx4 %2, %3, %1 offen lds\n\tbuffer_load_dwordx4 %2, %3, %1 offen offset:1024 lds\n\t"
      "s_mov_b32 m0, %0"
      : "=&s"(keep), "=&s"(so) : "v"(voff), "s"(srd), "s"(soff), "s"(ldst) : "memory", "scc");
}

struct Ring {
  unsigned lv;
  unsigned lf, ls;
  unsigned prog;
  unsigned bud;
};
DI void ring_check(Ring& r, int f) {
  if (__builtin_expect(ufl(r.lv) <= r.lf + f, 0)) r.lv = lds_wait_gt(L_LANDED, r.lf + f, r.bud);
  CBAR();
}
DI void ring_peek(Ring& r) { r.lv = lds_ld(L_LANDED); }
constexpr int BSTEP = 61440;

template <int NB, int W, int DIST, int F0, int S0, class Tab, int CS>
DI void gemm32(f32x16 (&acc)[NB], float& rs, Ring& rg, rsrc_t ws, unsigned abase, unsigned stbase) {
  const int lane = fresh_lane();
  const unsigned voff = lane * 16;
  v4u a[W], b[DIST + 1][NB];
  sfor<0, W>([&](auto S) INL { a[S.v] = lda<S.v>(ws, voff, abase); });
  SCHED();
  unsigned lb[3];
#pragma unroll
  for (int i = 0; i < 3; ++i) { lb[i] = voff + i * BSTEP; asm volatile("" : "+v"(lb[i])); }
  unsigned pa = rg.prog, pv = rg.ls + S0;
  asm volatile("" : "+v"(pa), "+v"(pv));
  v2u st[8], cf[NB], xm;
  float s_ = 0.f, q_ = 0.f;
  unsigned csv = 0;
  auto readB = [&](auto P) INL {
    constexpr int ph = P.v;
    if constexpr (ph < 40 * NB) {
      constexpr int ad = ARENA + Tab::u(ph / 10) * UNIT + (ph % 10) * 1024;
      b[(ph / NB) % (DIST + 1)][ph % NB] = lds128(lb[ad / BSTEP] + ad % BSTEP);
    }
    if constexpr (ph % 10 == 3 && ph + 7 < 40 * NB) ring_peek(rg);
    if constexpr (ph % 10 == 8 && ph + 2 < 40 * NB) ring_check(rg, F0 + ph / 10 + 1);
    if constexpr (ph % 10 == 0 && ph > 0 && ph <= 40 * NB) { CBAR(); ++pv; lds_st(pa, pv); CBAR(); }
  };
  ring_peek(rg);
  ring_check(rg, F0);
  sfor<0, DIST * NB>(readB);
  SCHED();
  constexpr int X1 = 40 * NB - 14;
  constexpr int X0 = X1 - 28;
  static_assert(X1 - X0 >= 8, "");
  sfor<0, 40 * NB>([&](auto SL) INL {
    constexpr int sl = SL.v, s = sl / NB, t = sl % NB;
    if constexpr (s == 0) { f32x16 z; _Pragma("unroll") for (int r = 0; r < 16; ++r) z[r] = 0.f; acc[t] = mfma32(b[0][t], a[0], z); }
    else acc[t] = mfma32(b[s % (DIST + 1)][t], a[s % W], acc[t]);
    readB(IC<sl + DIST * NB>{});
    if constexpr (t == NB - 1 && s + W < 40) a[s % W] = lda<s + W>(ws, voff, abase);
    if constexpr (sl >= X0 && sl < X0 + 4) {
      st[2 * (sl - X0)] = gld64(ws, lane * 8 + (2 * (sl - X0)) * 512, stbase);
      st[2 * (sl - X0) + 1] = gld64(ws, lane * 8 + (2 * (sl - X0) + 1) * 512, stbase);
    }
    if constexpr (sl >= X1 && sl < X1 + 4) {
      constexpr int i = 2 * (sl - X1);
      unsigned a0 = st[i][0], b0 = st[i][1], a1 = st[i + 1][0], b1 = st[i + 1][1];
      if constexpr (i == 0) { s_ = asf(a0) + asf(a1); q_ = asf(b0) + asf(b1); }
      else { s_ += asf(a0); q_ += asf(b0); s_ += asf(a1); q_ += asf(b1); }
    }
    if constexpr (NB == 2 && sl == X1 - 8) csv = lds_ld(L_CSUM);
    if constexpr (NB == 2 && sl == X1 - 3) { if (__builtin_expect(ufl(csv) == 0u, 0)) lds_wait_gt(L_CSUM, 0u, rg.bud); CBAR(); }
    if constexpr (sl == X1 + 4) s_ = sum_halves(s_) * (1.f / 640.f);
    if constexpr (sl == X1 + 5) q_ = sum_halves(q_) * (1.f / 640.f);
    if constexpr (sl == X1 + 6) {
      q_ = fmaxf(q_ - s_ * s_, 0.f);
      const unsigned nm = pk(-s_, -s_);
      xm[0] = lane < 32 ? nm : 0u; xm[1] = lane < 32 ? (nm & 0xffffu) : 0u;
    }
    if constexpr (sl == X1 + 7) {
      q_ = rbf(rbf(q_) + 1e-5f);
      sfor<0, NB>([&](auto T) INL { cf[T.v] = lds64(CS + T.v * 256 + (lane & 31) * 8); });
    }
    if constexpr (sl == X1 + 8) rs = __builtin_amdgcn_rcpf(rbf(__builtin_amdgcn_sqrtf(q_)));
    SCHED();
  });
  sfor<0, NB>([&](auto T) INL { acc[T.v] = mfma32k8(cf[T.v], xm, acc[T.v]); });
}

template <int NS, int W, int DIST, int F0, int S0, class Tab>
DI void gemm16(Ring& rg, rsrc_t ws, unsigned abase, unsigned resbase, unsigned xbase, unsigned stbase, bool last, void* out, int t16i, int ct) {
  const int lane = fresh_lane();
  const unsigned voff = lane * 16;
  v4u a[W], b[DIST + 1][3];
  sfor<0, W>([&](auto S) INL { a[S.v] = lda<S.v>(ws, voff, abase); });
  SCHED();
  unsigned lb[3], lc[3];
  const int g4 = lane >> 4;
#pragma unroll
  for (int i = 0; i < 3; ++i) {
    lb[i] = voff + i * BSTEP; asm volatile("" : "+v"(lb[i]));
    lc[i] = g4 * 128 + (lane & 7) * 16 + i * BSTEP; asm volatile("" : "+v"(lc[i]));
  }
  unsigned pa = rg.prog, pv = rg.ls + S0;
  asm volatile("" : "+v"(pa), "+v"(pv));
  v2u res[3];
  f32x4 acc[3];
  auto readB = [&](auto S) INL {
    constexpr int s = S.v;
    if constexpr (s < NS) {
      constexpr int ad = ARENA + Tab::u(s / 4) * UNIT + (s % 4) * 2560;
      b[s % (DIST + 1)][0] = lds128(lb[ad / BSTEP] + ad % BSTEP);
      b[s % (DIST + 1)][1] = lds128(lb[ad / BSTEP] + ad % BSTEP + 1024);
      b[s % (DIST + 1)][2] = lds128(lc[ad / BSTEP] + ad % BSTEP + 2048);
    }
    if constexpr (s % 4 == 1 && s + 3 < NS) ring_peek(rg);
    if constexpr (s % 4 == 3 && s + 1 < NS) ring_check(rg, F0 + s / 4 + 1);
    if constexpr (s % 4 == 0 && s > 0 && s <= NS) { CBAR(); ++pv; lds_st(pa, pv); CBAR(); }
  };
  ring_peek(rg);
  ring_check(rg, F0);
  sfor<0, DIST>(readB);
  SCHED();
  const unsigned t32 = (t16i & 1) * 16 + (lane & 15);
  if constexpr (NS == 48) { STAMP1(27); }
  sfor<0, NS>([&](auto S) INL {
    constexpr int s = S.v;
    sfor<0, 3>([&](auto T) INL {
      constexpr int t = 2 - T.v;
      if constexpr (s == 0) acc[t] = mfma16(b[0][t], a[0], f32x4{0.f, 0.f, 0.f, 0.f});
      else acc[t] = mfma16(b[s % (DIST + 1)][t], a[s % W], acc[t]);
    });
    readB(IC<s + DIST>{});
    if constexpr (s + W < NS) a[s % W] = lda<s + W>(ws, voff, abase);
    if constexpr (s == NS - W + 2) {
      const unsigned ro = (g4 >> 1) * 512 + t32 * 16 + (g4 & 1) * 8;
      res[0] = gld64(ws, ro, resbase + (5 * ct) * 512);
      res[1] = gld64(ws, ro + 1024, resbase + (5 * ct) * 512);
      res[2] = gld64(ws, t32 * 16 + (g4 & 1) * 8 + 2048, resbase + (5 * ct) * 512);
    }
    SCHED();
  });
  if constexpr (NS == 48) { STAMP1(28); }
  unsigned p[3][2];
#pragma unroll
  for (int t = 0; t < 3; ++t)
#pragma unroll
    for (int y = 0; y < 2; ++y) {
      unsigned r = res[t][y];
      p[t][y] = pk(acc[t][2 * y] + asf(r << 16), acc[t][2 * y + 1] + asf(r & 0xffff0000u));
    }
  auto s0 = __builtin_amdgcn_permlane16_swap(p[0][0], p[1][0], false, false);
  auto s1 = __builtin_amdgcn_permlane16_swap(p[0][1], p[1][1], false, false);
  auto s2 = __builtin_amdgcn_permlane16_swap(p[2][0], p[2][0], false, false);
  auto s3 = __builtin_amdgcn_permlane16_swap(p[2][1], p[2][1], false, false);
  const v4u o01 = {s0[0], s1[0], s0[1], s1[1]};
  const v4u o2 = {s2[0], s3[0], s2[1], s3[1]};
  const unsigned pi = 2 * (g4 & 1) + (g4 >> 1);
  if (last) {
    GAS char* ob = (GAS char*)out + (size_t)(t16i * 16 + (lane & 15)) * 1280 + ct * 80;
    *(GAS v4u*)(ob + pi * 16) = o01;
    if (lane < 16) *(GAS v4u*)(ob + 64) = o2;
  } else {
    gst128(o01, ws, pi * 512 + t32 * 16, xbase + (5 * ct) * 512);
    const unsigned dead = lane < 16 ? 0u : WS_USED;
    gst128(o2, ws, t32 * 16 + 2048 + dead, xbase + (5 * ct) * 512);
    if (g4 >= 2) { p[2][0] = 0u; p[2][1] = 0u; }
    float s = 0.f, q = 0.f;
#pragma unroll
    for (int t = 0; t < 3; ++t)
#pragma unroll
      for (int y = 0; y < 2; ++y) { s = dot2(p[t][y], 0x3f803f80u, s); q = dot2(p[t][y], p[t][y], q); }
    s = sum_halves(sum_rows16(s));
    q = sum_halves(sum_rows16(q));
    gst64(v2u{asu(s), asu(q)}, ws, t32 * 8 + dead, stbase + ct * 256);
  }
}

constexpr float QSCALE = 0.22360679774997896f * 1.4426950408889634f;
constexpr float NEG = -1e30f;

DI float max16(const f32x16& s) {
  const float a = max3(s[0], s[1], s[2]), b = max3(s[3], s[4], s[5]), c = max3(s[6], s[7], s[8]);
  const float d = max3(s[9], s[10], s[11]), e = max3(s[12], s[13], s[14]);
  return __builtin_elementwise_maximum(max3(a, b, c), max3(d, e, s[15]));
}
DI void kv_write(unsigned kb, int lane, v4u k1, v4u ta, v4u tb) {
  *(LAS v4u*)(kb + lane * 16) = k1;
  *(LAS v2u*)(kb + 1024 + (lane ^ 32) * 8) = v2u{ta[0], ta[1]};
  const unsigned vb = kb + 1536 + (lane & 31) * 48 + (lane >> 5) * 8;
  *(LAS v2u*)(vb) = v2u{ta[2], ta[3]};
  *(LAS v2u*)(vb + 16) = v2u{tb[0], tb[1]};
  *(LAS v2u*)(vb + 32) = v2u{tb[2], tb[3]};
}
constexpr float MB = 16.f;

__global__ void __launch_bounds__(704) mega_kernel(unsigned char* wsp, void* out) {
  __shared__ __attribute__((aligned(16))) char smem[LDS_BYTES];
  const int wave = ufl(threadIdx.x >> 6);
  asm volatile("" :: "v"((LAS char*)smem) : "memory");
  const int bx = blockIdx.x & 7, br = blockIdx.x >> 3;
  const int band = (bx & 6) + (br >> 4), j = (bx & 1) * 16 + (br & 15);
  const int ct = (j >> 4) * 8 + (j & 7), rsel = (j >> 3) & 1;
  const rsrc_t ws = __builtin_amdgcn_make_buffer_rsrc(wsp, 0, WS_USED, 0x00020000);

  if (wave == 0) {
    const int lane = fresh_lane();
    __builtin_amdgcn_s_setprio(3);
    const v4u wsd = {(unsigned)(uintptr_t)wsp, (unsigned)((uintptr_t)wsp >> 32) & 0xffffu, WS_USED, 0x00020000u};
#pragma unroll 1
    for (int f = 0; f < 6; ++f) dma_fill(wsd, lane * 16, OFF_WQ + j * 81920 + f * UNIT, ARENA + f * UNIT);
    if (lane < 32) lds_st(MISC + lane * 4, 0u);
    asm volatile("s_waitcnt lgkmcnt(0)" ::: "memory");
    __builtin_amdgcn_s_barrier();
    asm volatile("s_waitcnt vmcnt(50)" ::: "memory"); lds_st(L_LANDED, 1u);
    dma_fill(wsd, lane * 16, OFF_WQ + j * 81920 + 6 * UNIT, ARENA + 6 * UNIT);
    asm volatile("s_waitcnt vmcnt(50)" ::: "memory"); lds_st(L_LANDED, 2u);
    dma_fill(wsd, lane * 16, OFF_WQ + j * 81920 + 7 * UNIT, ARENA + 7 * UNIT);
    asm volatile("s_waitcnt vmcnt(50)" ::: "memory"); lds_st(L_LANDED, 3u);
    asm volatile("s_waitcnt vmcnt(40)" ::: "memory"); lds_st(L_LANDED, 4u);
    asm volatile("s_waitcnt vmcnt(30)" ::: "memory"); lds_st(L_LANDED, 5u);
    asm volatile("s_waitcnt vmcnt(20)" ::: "memory"); lds_st(L_LANDED, 6u);
    unsigned issued = 8, published = 6, bud = 0;
#pragma unroll 1
    for (int lp = 1; lp < 8; ++lp) {
      const int L = lp >> 2, ph = lp & 3;
      unsigned long long units, needs_lo, needs_hi; int n; unsigned src;
      if (ph == 0) { units = 0x76543210ull; n = 8; src = OFF_WQ + j * 81920;
        needs_lo = 17ull | 25ull << 8 | 26ull << 16 | 27ull << 24 | 28ull << 32 | 29ull << 40 | 32ull << 48 | 33ull << 56; needs_hi = 0; }
      else if (ph == 1) { units = 0x54321ull; n = 5; src = OFF_WO + ct * 51200;
        needs_lo = 40ull | 41ull << 8 | 42ull << 16 | 43ull << 24 | 44ull << 32; needs_hi = 0; }
      else if (ph == 2) { units = 0x21CBA98760EDull; n = 12; src = OFF_WG + j * 122880;
        needs_lo = 30ull | 31ull << 8 | 39ull << 16 | 45ull << 24 | 46ull << 32 | 47ull << 40 | 47ull << 48 | 47ull << 56;
        needs_hi = 47ull | 47ull << 8 | 48ull << 16 | 49ull << 24; }
      else { units = 0xCBA9876ED543ull; n = 12; src = OFF_WD + ct * 122880;
        needs_lo = 50ull | 51ull << 8 | 52ull << 16 | 53ull << 24 | 54ull << 32 | 56ull << 40 | 57ull << 48 | 58ull << 56;
        needs_hi = 59ull | 60ull << 8 | 61ull << 16 | 62ull << 24; }
#pragma unroll 1
      for (int f = 0; f < n; ++f) {
        const int unit = (int)((units >> (4 * f)) & 15ull);
        const int nd = (int)(((f < 8 ? needs_lo >> (8 * f) : needs_hi >> (8 * (f - 8)))) & 255ull) + (L - 1) * NSTAGE;
        if (nd > 0) {
          unsigned v = lds_ld(L_PROG + (lane & 7) * 4);
          if (!__all((int)v >= nd)) {
            if (published != issued) {
              if (issued - published >= 2) { asm volatile("s_waitcnt vmcnt(10)" ::: "memory"); lds_st(L_LANDED, issued - 1); }
              asm volatile("s_waitcnt vmcnt(0)" ::: "memory");
              published = issued; lds_st(L_LANDED, published);
            }
            prog_wait((unsigned)nd, bud);
          }
        }
        dma_fill(wsd, lane * 16, src + f * UNIT, ARENA + unit * UNIT);
        ++issued;
        if (issued - published > LDEPTH) {
          asm volatile("s_waitcnt vmcnt(%0)" :: "i"(10 * LDEPTH) : "memory");
          published = issued - LDEPTH; lds_st(L_LANDED, published);
        }
      }
    }
    asm volatile("s_waitcnt vmcnt(10)" ::: "memory");
    lds_st(L_LANDED, issued - 1);
    asm volatile("s_waitcnt vmcnt(0)" ::: "memory");
    lds_st(L_LANDED, issued);
    return;
  }
  __builtin_amdgcn_s_barrier();
  CBAR();

  if (wave <= 8) {
    const int cw = wave - 1, grp = cw >> 2, wi = cw & 3;
    const int qt = (1 - grp) * 4 + wi;
    const int t32_ = band * 8 + qt;
    const int t16_ = band * 16 + (1 - grp) * 8 + rsel * 4 + wi;
    const int rep = j & 3;
    Ring rg;
    rg.prog = L_PROG + cw * 4;
    rg.bud = 0;
#define PRIO_E 2
#define PRIO_L 1
    if (grp == 0) __builtin_amdgcn_s_setprio(PRIO_E); else __builtin_amdgcn_s_setprio(PRIO_L);
    const int dslot = qt + (qt < 4 ? 4 : 8);
    STAMP(30);
    STAMP(0);
    if (TURN0 > 0 && grp == 1) lds_wait_gt(rg.prog - 16, TURN0 - 1, rg.bud);
#pragma unroll 1
    for (int L = 0; L < 2; ++L) {
      rg.lf = L * NFILL; rg.ls = L * NSTAGE;
      const int t32i = opq(t32_), t16i = opq(t16_);
      if (L > 0) ctr_wait(wsp, 3, t32i, rep, rg.bud);
      STAMP(1 + L * 12);
      v4u q1; v2u q2;
      {
        f32x16 acc[2]; float rs;
        gemm32<2, WQ_, 2, 0, 0, TabQ, L_CQ>(acc, rs, rg, ws, OFF_XA + (2 * L) * XA_SZ + t32i * XA_T, OFF_ST + (2 * L) * ST_SZ + t32i * 4096);
        STAMP(2 + L * 12);
        const int lane = fresh_lane(), h = lane >> 5;
        v4u k1, ta, tb;
        const float rq = rs * QSCALE, r0 = h ? rs : rq;
#pragma unroll
        for (int i = 0; i < 4; ++i) k1[i] = pk(acc[0][8 + 2 * i] * rs, acc[0][9 + 2 * i] * rs);
        ta[0] = pk(acc[1][0] * r0, acc[1][1] * r0); ta[1] = pk(acc[1][2] * r0, acc[1][3] * r0);
        ta[2] = pk(acc[1][4] * rs, acc[1][5] * rs); ta[3] = pk(acc[1][6] * rs, acc[1][7] * rs);
#pragma unroll
        for (int i = 0; i < 4; ++i) tb[i] = pk(acc[1][8 + 2 * i] * rs, acc[1][9 + 2 * i] * rs);
        if (h) { tb[2] = 0x3f803f80u; tb[3] = 0x3f803f80u; }
        q2[0] = h ? 0x3f80u : ta[0]; q2[1] = h ? 0u : ta[1];
        if (!h) { ta[0] = 0u; ta[1] = 0u; }
        if (band < 7) {
          const unsigned gb = OFF_KVH + L * KVH_SZ + ((band * 32 + j) * 8 + qt) * KVH_T;
          gst128(k1, ws, lane * 16, gb); gst128(ta, ws, lane * 16 + 1024, gb); gst128(tb, ws, lane * 16 + 2048, gb);
        }
        if (L > 0) prog_wait(L * NSTAGE, rg.bud);
        kv_write(KV_OFF + dslot * KV_T, lane, k1, ta, tb);
        CBAR();
        lds_or1(L_RDY + L * 4, 1u << dslot);
#pragma unroll
        for (int i = 0; i < 4; ++i) q1[i] = pk(acc[0][2 * i] * rq, acc[0][2 * i + 1] * rq);
      }
      STAMP(3 + L * 12);
      {
        const int lane = fresh_lane(), h = lane >> 5;
        f32x16 o, negm, sc[2];
        const int topA = dslot, topB = grp == 0 ? 8 : 12;
        const int top0 = grp == 0 ? topA : topB, top1 = grp == 0 ? topB : topA;
        const unsigned vlo = 1536 + (4 * h + ((lane & 15) >> 2)) * 48 + (((lane >> 4) & 1) == 0 ? 8 * (lane & 3) : ((lane & 3) == 0 ? 32 : 40));
        unsigned ka[2], kb[2], va[2], vd;
        ka[0] = KV_OFF + (top0 - 4) * KV_T + lane * 16; ka[1] = KV_OFF + (top1 - 4) * KV_T + lane * 16;
        kb[0] = KV_OFF + (top0 - 4) * KV_T + 1024 + lane * 8; kb[1] = KV_OFF + (top1 - 4) * KV_T + 1024 + lane * 8;
        va[0] = KV_OFF + (top0 - 4) * KV_T + vlo; va[1] = KV_OFF + (top1 - 4) * KV_T + vlo;
        vd = KV_OFF + dslot * KV_T + vlo;
        asm volatile("" : "+v"(ka[0]), "+v"(ka[1]), "+v"(kb[0]), "+v"(kb[1]), "+v"(va[0]), "+v"(va[1]), "+v"(vd));
        const int ql = (lane & 31) - 4 * h;
        unsigned rdyv = lds_ld(L_RDY + L * 4);
        {
          f32x16 z;
#pragma unroll
          for (int r = 0; r < 16; ++r) { z[r] = 0.f; o[r] = 0.f; }
          sc[0] = mfma32(lds128(KV_OFF + dslot * KV_T + lane * 16), q1, z);
          sc[0] = mfma32k8(lds64(KV_OFF + dslot * KV_T + 1024 + lane * 8), q2, sc[0]);
#pragma unroll
          for (int r = 0; r < 16; ++r) sc[0][r] = ((r & 3) + 8 * (r >> 2)) <= ql ? sc[0][r] : NEG;
          const float m = max_halves(max16(sc[0])) + MB;
#pragma unroll
          for (int r = 0; r < 16; ++r) { sc[0][r] -= m; negm[r] = -m; }
        }
        STAMP2(24);
        unsigned rdy = ufl(rdyv);
        sfor<0, 9>([&](auto N) INL {
          constexpr int n = N.v;
          f32x16& s = sc[n & 1];
          f32x16& sn = sc[(n + 1) & 1];
          v4u k1; v2u k2, v0, v1, v2, v3;
          if constexpr (n == ANN) {
            if (band < 7) {
              GAS unsigned* fl = (GAS unsigned*)(wsp + OFF_HALOF + (((L * 8 + band) * 32 + j) * 2 + (qt >> 2)) * 256);
              asm volatile("s_waitcnt vmcnt(0)\n\ts_mov_b64 exec, 1\n\ts_nop 3\n\tglobal_atomic_or %0, %1, %2\n\ts_mov_b64 exec, -1" :: "v"(0u), "v"(1u << (qt & 3)), "s"(fl) : "memory");
            }
            STAMP2(25);
          }
          {
            const unsigned a = n == 0 ? vd : va[(n - 1) >> 2] + (3 - ((n - 1) & 3)) * KV_T;
            v0 = ldstr(a); v1 = ldstr(a + 384); v2 = ldstr(a + 768); v3 = ldstr(a + 1152);
          }
          if constexpr (n == 8) { CBAR(); lds_st(rg.prog, rg.ls + 9); CBAR(); }
          if constexpr (n == 4 || n == 8) {
            if (grp == (n == 4 ? 0 : 1)) {
#pragma unroll
              for (int r = 0; r < 16; ++r) s[r] = ((r & 3) + 8 * (r >> 2)) > ql ? s[r] : NEG;
            }
          }
          v4u p, p2;
          p[0] = pk(ex2(s[0]), ex2(s[1])); p[1] = pk(ex2(s[2]), ex2(s[3]));
          if constexpr (n < 8) {
            lds_wait_bits(L_RDY + L * 4, 1u << ((n < 4 ? top0 : top1) - 1 - (n & 3)), rdy, rg.bud);
            k1 = lds128(ka[n >> 2] + (3 - (n & 3)) * KV_T); k2 = lds64(kb[n >> 2] + (3 - (n & 3)) * KV_T);
          }
          p[2] = pk(ex2(s[4]), ex2(s[5])); p[3] = pk(ex2(s[6]), ex2(s[7]));
          p2[0] = pk(ex2(s[8]), ex2(s[9]));
          SCHED();
          if constexpr (n < 8) sn = mfma32(k1, q1, negm);
          p2[1] = pk(ex2(s[10]), ex2(s[11]));
          float e12 = ex2(s[12]);
          SCHED();
          if constexpr (n < 8) sn = mfma32k8(k2, q2, sn);
          p2[2] = pk(e12, ex2(s[13])); p2[3] = pk(ex2(s[14]), ex2(s[15]));
          const unsigned any = ((p[0] | p[1] | p[2]) | (p[3] | p2[0] | p2[1]) | (p2[2] | p2[3])) & 0x40004000u;
          if (__builtin_expect(__any(any != 0u), 0)) {
            const float d = fmaxf(max_halves(max16(s)) + MB, 0.f);
            const float f = ex2(-d);
#pragma unroll
            for (int r = 0; r < 16; ++r) { s[r] -= d; negm[r] -= d; o[r] *= f; if (n < 8) sn[r] -= d; }
#pragma unroll
            for (int i = 0; i < 4; ++i) { p[i] = pk(ex2(s[2 * i]), ex2(s[2 * i + 1])); p2[i] = pk(ex2(s[8 + 2 * i]), ex2(s[9 + 2 * i])); }
          }
          o = mfma32(v4u{v0[0], v0[1], v1[0], v1[1]}, p, o);
          o = mfma32(v4u{v2[0], v2[1], v3[0], v3[1]}, p2, o);
          SCHED();
          if constexpr (n == 3) STAMP2(26);
          if constexpr (n == 5) STAMP2(27);
          if constexpr (n == 7) STAMP2(28);
        });
        const float inv = __builtin_amdgcn_rcpf(o[12]);
        v4u o1;
#pragma unroll
        for (int i = 0; i < 4; ++i) o1[i] = pk(o[2 * i] * inv, o[2 * i + 1] * inv);
        const v2u o2 = {pk(o[8] * inv, o[9] * inv), pk(o[10] * inv, o[11] * inv)};
        const unsigned ob = OFF_OA + L * OA_SZ + (2 * t32i) * OA_T + (5 * (j >> 1)) * 256;
        const unsigned tv = ((lane >> 4) & 1) * OA_T + (lane & 15) * 16;
        gst128(o1, ws, tv + h * 256, ob + (3 * (j & 1)) * 256);
        gst64(o2, ws, tv + (h ? WS_USED : 0u), ob + 2 * 256 + (j & 1) * 8);
        STAMP(4 + L * 12);
        ctr_arrive(wsp, 4 * L, t32i);
      }
      STAMP(5 + L * 12);
      ctr_wait(wsp, 4 * L, t16i >> 1, rep, rg.bud);
      STAMP(6 + L * 12);
      gemm16<20, 20, 3, 8, 9, TabO>(rg, ws, OFF_OA + L * OA_SZ + t16i * OA_T, OFF_XA + (2 * L) * XA_SZ + (t16i >> 1) * XA_T,
                                    OFF_XA + (2 * L + 1) * XA_SZ + (t16i >> 1) * XA_T, OFF_ST + (2 * L + 1) * ST_SZ + (t16i >> 1) * 4096,
                                    false, out, t16i, ct);
      STAMP(7 + L * 12);
      ctr_arrive(wsp, 4 * L + 1, t16i >> 1);
      ctr_wait(wsp, 4 * L + 1, t32i, rep, rg.bud);
      STAMP(8 + L * 12);
      {
        f32x16 acc[3]; float rs;
        STAMP1(24);
        gemm32<3, WG_, 2, 13, 14, TabG, L_CG>(acc, rs, rg, ws, OFF_XA + (2 * L + 1) * XA_SZ + t32i * XA_T, OFF_ST + (2 * L + 1) * ST_SZ + t32i * 4096);
        STAMP1(25); STAMP1(26);
        const int lane = fresh_lane(), h = lane >> 5;
        const float r2 = rs * rs;
        const unsigned hb = OFF_HA + L * HA_SZ + (2 * t32i) * HA_T + (6 * j) * 256;
        const unsigned tv = ((lane >> 4) & 1) * HA_T + (lane & 15) * 16 + h * 256;
        v4u hv;
#pragma unroll
        for (int i = 0; i < 4; ++i) hv[i] = pk(acc[0][2 * i] * r2 * acc[1][2 * i], acc[0][2 * i + 1] * r2 * acc[1][2 * i + 1]);
        gst128(hv, ws, tv, hb);
#pragma unroll
        for (int i = 0; i < 4; ++i) hv[i] = pk(acc[0][8 + 2 * i] * r2 * acc[1][8 + 2 * i], acc[0][9 + 2 * i] * r2 * acc[1][9 + 2 * i]);
        gst128(hv, ws, tv + 512, hb);
#pragma unroll
        for (int i = 0; i < 4; ++i) hv[i] = pk(acc[2][2 * i] * r2 * acc[2][8 + 2 * i], acc[2][2 * i + 1] * r2 * acc[2][9 + 2 * i]);
        gst128(hv, ws, tv + 1024, hb);
      }
      STAMP(9 + L * 12);
      ctr_arrive(wsp, 4 * L + 2, t32i);
      ctr_wait(wsp, 4 * L + 2, t16i >> 1, rep, rg.bud);
      STAMP(10 + L * 12);
      gemm16<48, WD_, 3, 25, 26, TabD>(rg, ws, OFF_HA + L * HA_SZ + t16i * HA_T, OFF_XA + (2 * L + 1) * XA_SZ + (t16i >> 1) * XA_T,
                                       OFF_XA + 2 * XA_SZ + (t16i >> 1) * XA_T, OFF_ST + 2 * ST_SZ + (t16i >> 1) * 4096,
                                       L == 1, out, t16i, ct);
      STAMP(11 + L * 12);
      if (L == 0) ctr_arrive(wsp, 3, t16i >> 1);
    }
  } else {
    const int lane = fresh_lane();
    const int half = 10 - wave;
    __builtin_amdgcn_s_setprio(3);
    if (half == 0) {
      f32x4 c = {0.f, 0.f, 0.f, 0.f};
      v4u pc[20];
#pragma unroll
      for (int i = 0; i < 20; ++i) pc[i] = __builtin_amdgcn_raw_buffer_load_b128(ws, min(lane, 39) * 16, OFF_CS + (j * 20 + i) * 640, 0);
#pragma unroll
      for (int i = 0; i < 20; ++i) c += __builtin_bit_cast(f32x4, pc[i]);
      unsigned o[8];
#pragma unroll
      for (int i = 0; i < 4; ++i) {
        const float v = c[i], hi = rbf(v), mid = rbf(v - hi), lo = rbf(v - hi - mid);
        o[2 * i] = pk(hi, mid); o[2 * i + 1] = pk(lo, 0.f);
      }
      if (lane < 40) {
        *(LAS v4u*)(unsigned)(L_CQ + lane * 32) = v4u{o[0], o[1], o[2], o[3]};
        *(LAS v4u*)(unsigned)(L_CQ + lane * 32 + 16) = v4u{o[4], o[5], o[6], o[7]};
      }
      CBAR();
      lds_st(L_CSUM, 1u);
      CBAR();
    }
    if (band == 0) {
      unsigned bud = 0;
#pragma unroll 1
      for (int L = 0; L < 2; ++L) {
        if (L > 0) prog_wait(L * NSTAGE, bud);
        const v4u z = {0u, 0u, 0u, 0u};
#pragma unroll 1
        for (int sl = 0; sl < 4; ++sl) kv_write(KV_OFF + (8 * half + sl) * KV_T, lane, z, v4u{lane < 32 ? 0xf149u : 0u, 0u, 0u, 0u}, z);
        CBAR();
        lds_or1(L_RDY + L * 4, 0xfu << (8 * half));
      }
    } else {
      unsigned bud = 0;
#pragma unroll 1
      for (int L = 0; L < 2; ++L) {
        lds_wait_gt(L_PROG + (1 - half) * 16, L * NSTAGE + 5, bud);
        if (L > 0) prog_wait(L * NSTAGE, bud);
        unsigned pend = 0xfu;
        GAS unsigned* fl = (GAS unsigned*)(wsp + OFF_HALOF + (((L * 8 + band - 1) * 32 + j) * 2 + half) * 256);
        const unsigned gb = OFF_KVH + L * KVH_SZ + (((band - 1) * 32 + j) * 8 + 4 * half) * KVH_T;
        while (pend) {
          const unsigned nw = rmw_poll(fl) & pend;
          __builtin_amdgcn_fence(__ATOMIC_ACQUIRE, "wavefront");
          CBAR();
          if (!nw) {
            SPEND(bud);
            continue;
          }
          pend &= ~nw;
          v4u x[4][3];
#pragma unroll
          for (int sl = 3; sl >= 0; --sl) {
            const unsigned vo = lane * 16 + ((nw >> sl & 1) ? 0u : WS_USED);
#pragma unroll
            for (int i = 0; i < 3; ++i) x[sl][i] = gld128(ws, vo + i * 1024, gb + sl * KVH_T);
          }
#pragma unroll
          for (int sl = 3; sl >= 0; --sl)
            if (nw >> sl & 1) {
              kv_write(KV_OFF + (8 * half + sl) * KV_T, lane, x[sl][0], x[sl][1], x[sl][2]);
              CBAR();
              lds_or1(L_RDY + L * 4, 1u << (8 * half + sl));
            }
        }
      }
    }
  }
}

DI int kmapO4(int P, int t) {
  const int p = P / 5, r = P - 5 * p;
  if (r == 2) return 20 * (2 * p + t) + 16;
  return 20 * (2 * p + (r > 2)) + 8 * t + 4 * (r > 2 ? r - 3 : r);
}
DI v4u tr_frag(unsigned a0, unsigned a1) { const v2u x = ldstr(a0), y = ldstr(a1); return v4u{x[0], x[1], y[0], y[1]}; }
DI float sum8(v4u f, float s) {
#pragma unroll
  for (int i = 0; i < 4; ++i) { const unsigned u = f[i]; s = dot2(u, 0x3f803f80u, s); }
  return s;
}
constexpr int PREP_LDS = 51200 + 512;

__global__ void __launch_bounds__(512) prep_kernel(const unsigned char* x, const unsigned char* wqkv, const unsigned char* wo, const unsigned char* wg,
                                                   const unsigned char* wu, const unsigned char* wd, unsigned char* ws) {
  __shared__ __attribute__((aligned(16))) char smem[PREP_LDS];
  asm volatile("" :: "v"((LAS char*)smem) : "memory");
  const int bid = blockIdx.x, tid = threadIdx.x, lane = tid & 63, w = ufl(tid >> 6);
  const v4u x0 = *(const GAS v4u*)(x + (size_t)(bid * 8 + (tid & 7)) * 1280 + (tid >> 3) * 16);
  v4u x1 = {0u, 0u, 0u, 0u};
  if (tid < 128) x1 = *(const GAS v4u*)(x + (size_t)(bid * 8 + (tid & 7)) * 1280 + (64 + (tid >> 3)) * 16);
  int type, cb, kb, nch;
  if (bid < 80) { type = 0; cb = bid & 3; kb = bid >> 2; nch = 1920; }
  else if (bid < 160) { type = 1; cb = (bid - 80) & 7; kb = (bid - 80) >> 3; nch = 3072; }
  else if (bid < 192) { type = 2; cb = (bid - 160) & 7; kb = (bid - 160) >> 3; nch = 1600; }
  else { type = 3; cb = (bid - 192) & 3; kb = (bid - 192) >> 2; nch = 1920; }
  v4u t[6];
#pragma unroll
  for (int i = 0; i < 6; ++i) {
    const int c = min(tid + 512 * i, nch - 1);
    const unsigned char* src;
    if (type == 0) { const int row = c / 60, r = c - 60 * row, sec = r / 20; src = wqkv + (size_t)(32 * kb + row) * 3840 + sec * 1280 + cb * 320 + (r - 20 * sec) * 16; }
    else if (type == 1) { const int row = c / 48, r = c - 48 * row; src = (r < 24 ? wg : wu) + (size_t)(64 * kb + row) * 3072 + cb * 384 + (r < 24 ? r : r - 24) * 16; }
    else if (type == 2) { const int row = c / 10; src = wo + (size_t)(160 * kb + row) * 1280 + cb * 160 + (c - 10 * row) * 16; }
    else { const int row = c / 20; src = wd + (size_t)(96 * kb + row) * 1280 + cb * 320 + (c - 20 * row) * 16; }
    if (i < 4 || type == 1) t[i] = *(const GAS v4u*)src;
  }
  if (tid < 11) *(GAS unsigned*)(ws + OFF_CTR + (bid * 11 + tid) * 256) = 0u;
  {
    unsigned char* xa = ws + OFF_XA + (bid >> 2) * XA_T + ((bid & 3) * 8 + (tid & 7)) * 16;
    *(GAS v4u*)(xa + (tid >> 3) * 512) = x0;
    if (tid < 128) *(GAS v4u*)(xa + (64 + (tid >> 3)) * 512) = x1;
    float s = sum8(x1, sum8(x0, 0.f)), q = 0.f;
#pragma unroll
    for (int i = 0; i < 4; ++i) { const unsigned u = x0[i], v = x1[i]; q = dot2(u, u, q); q = dot2(v, v, q); }
    s += __shfl_xor(s, 8); q += __shfl_xor(q, 8);
    s = sum_halves(sum_rows16(s)); q = sum_halves(sum_rows16(q));
    if (lane < 16) *(GAS v2u*)(ws + OFF_ST + (bid >> 2) * 4096 + (w + (lane & 8)) * 256 + ((bid & 3) * 8 + (lane & 7)) * 8) = lane < 8 ? v2u{asu(s), asu(q)} : v2u{0u, 0u};
  }
#pragma unroll
  for (int i = 0; i < 6; ++i)
    if ((i < 4 || type == 1) && tid + 512 * i < nch) *(LAS v4u*)(unsigned)((tid + 512 * i) * 16) = t[i];
  __syncthreads();
  const int gam = lane >> 4, q = (lane & 15) >> 2, p = lane & 3;
  if (type == 0) {
    const int head = 8 * cb + w, hh = gam >> 1, g = (gam & 1) * 4 + p;
    const unsigned c0 = (g < 4 ? 0 : 320) + w * 40 + (g & 3) * 8;
    const unsigned c1 = (g == 0 ? 32 : g == 1 ? 320 + 32 : 640 + ((g < 7 ? g : 6) - 2) * 8) + w * 40;
    float s0 = 0.f, s1 = 0.f;
#pragma unroll
    for (int ks = 0; ks < 2; ++ks) {
      const unsigned r = (16 * ks + 8 * hh + q) * 960;
      const v4u f0 = tr_frag(r + c0, r + 4 * 960 + c0);
      v4u f1 = tr_frag(r + c1, r + 4 * 960 + c1);
      if ((lane & 31) >= 28) f1 = v4u{0u, 0u, 0u, 0u};
      unsigned char* dst = ws + OFF_WQ + head * 81920 + (2 * kb + ks) * 2048 + lane * 16;
      *(GAS v4u*)dst = f0; *(GAS v4u*)(dst + 1024) = f1;
      s0 = sum8(f0, s0); s1 = sum8(f1, s1);
    }
    s0 = sum_halves(s0); s1 = sum_halves(s1);
    *(GAS float*)(ws + OFF_CS + (head * 20 + kb) * 640 + lane * 4) = lane < 32 ? s0 : s1;
  } else if (type == 1) {
    const int jj = 4 * cb + (w >> 1), hh = gam >> 1, g = (gam & 1) * 4 + p;
    const unsigned cg = (w >> 1) * 96 + g * 8, c2 = (w >> 1) * 96 + 64 + (g & 3) * 8 + (g < 4 ? 0 : 384);
    float s0 = 0.f, s1 = 0.f, s2 = 0.f;
#pragma unroll
    for (int k2 = 0; k2 < 2; ++k2) {
      const int ks = 2 * (w & 1) + k2;
      const unsigned r = (16 * ks + 8 * hh + q) * 768;
      const v4u f0 = tr_frag(r + cg, r + 4 * 768 + cg), f1 = tr_frag(r + 384 + cg, r + 4 * 768 + 384 + cg), f2 = tr_frag(r + c2, r + 4 * 768 + c2);
      unsigned char* dst = ws + OFF_WG + jj * 122880 + (4 * kb + ks) * 3072 + lane * 16;
      *(GAS v4u*)dst = f0; *(GAS v4u*)(dst + 1024) = f1; *(GAS v4u*)(dst + 2048) = f2;
      s0 = sum8(f0, s0); s1 = sum8(f1, s1); s2 = sum8(f2, s2);
    }
    s0 = sum_halves(s0); s1 = sum_halves(s1); s2 = sum_halves(s2);
    unsigned char* cs = ws + OFF_CS + (jj * 20 + 2 * kb + (w & 1)) * 640 + 256;
    *(GAS float*)(cs + lane * 4) = lane < 32 ? s0 : s1;
    if (lane < 32) *(GAS float*)(cs + 256 + lane * 4) = s2;
  } else {
    const int np = type == 2 ? 10 : 12, nks = type == 2 ? 5 : 3, rb = type == 2 ? 160 : 320;
    for (int u = w; u < np; u += 8) {
      const int cl = u / nks, ks = u - cl * nks;
      const int P = 4 * ks + gam;
      const int r0 = type == 2 ? kmapO4(P, 0) : 16 * (P >> 1) + 4 * (P & 1), r1 = type == 2 ? kmapO4(P, 1) : r0 + 8;
      const unsigned a0 = (r0 + q) * rb + cl * 80, a1 = (r1 + q) * rb + cl * 80;
      const v4u f0 = tr_frag(a0 + p * 8, a1 + p * 8), f1 = tr_frag(a0 + 32 + p * 8, a1 + 32 + p * 8), f2 = tr_frag(a0 + 64 + (p & 1) * 8, a1 + 64 + (p & 1) * 8);
      unsigned char* dst = type == 2 ? ws + OFF_WO + (2 * cb + cl) * 51200 + (5 * kb + ks) * 2560 : ws + OFF_WD + (4 * cb + cl) * 122880 + (3 * kb + ks) * 2560;
      *(GAS v4u*)(dst + lane * 16) = f0; *(GAS v4u*)(dst + 1024 + lane * 16) = f1;
      if ((lane & 15) < 8) *(GAS v4u*)(dst + 2048 + gam * 128 + (lane & 7) * 16) = f2;
    }
  }
}

extern "C" void kernel_launch(void* const* d_in, const int* in_sizes, int n_in, void* d_out, int out_size, void* d_ws, size_t ws_size,
                              hipStream_t stream) {
  typedef const unsigned char* cp;
  prep_kernel<<<256, 512, 0, stream>>>((cp)d_in[0], (cp)d_in[1], (cp)d_in[2], (cp)d_in[4], (cp)d_in[5], (cp)d_in[6], (unsigned char*)d_ws);
  mega_kernel<<<256, 704, 0, stream>>>((unsigned char*)d_ws, d_out);
}
```

```cpp
#include <hip/hip_runtime.h>
#include <hip/hip_bf16.h>
#include <stdint.h>
#pragma clang diagnostic ignored "-Wint-to-pointer-cast"

typedef unsigned v4u __attribute__((ext_vector_type(4)));
typedef unsigned v2u __attribute__((ext_vector_type(2)));
typedef short s16x8 __attribute__((ext_vector_type(8)));
typedef short s16x4 __attribute__((ext_vector_type(4)));
typedef float f32x16 __attribute__((ext_vector_type(16)));
typedef float f32x4 __attribute__((ext_vector_type(4)));
typedef float f32x2 __attribute__((ext_vector_type(2)));
typedef __bf16 bf2 __attribute__((ext_vector_type(2)));
typedef unsigned short u16;
#define LAS __attribute__((address_space(3)))
#define GAS __attribute__((address_space(1)))
#define DI __device__ __forceinline__
#define INL __attribute__((always_inline))
#define RLX_AGENT __ATOMIC_RELAXED, __HIP_MEMORY_SCOPE_AGENT
#define RLX_WG __ATOMIC_RELAXED, __HIP_MEMORY_SCOPE_WORKGROUP
#define CBAR() asm volatile("" ::: "memory")
#define SCHED() __builtin_amdgcn_sched_barrier(0)

constexpr unsigned OFF_CTR = 0;
constexpr unsigned OFF_HALOF = 7 * 64 * 1024;
constexpr unsigned OFF_IN = OFF_HALOF + 2 * 8 * 32 * 512;
constexpr unsigned OFF_F2 = OFF_IN + 256 * 512;
constexpr unsigned OFF_EP = OFF_F2 + 1024;
constexpr unsigned OFF_STAMP = 64u << 20;
constexpr unsigned OFF_WQ = 1u << 20;
constexpr unsigned OFF_WO = OFF_WQ + 32 * 81920;
constexpr unsigned OFF_WG = OFF_WO + 16 * 51200;
constexpr unsigned OFF_WD = OFF_WG + 32 * 122880;
constexpr unsigned XA_T = 40960, XA_SZ = 64 * XA_T;
constexpr unsigned OFF_XA = OFF_WD + 16 * 122880;
constexpr unsigned ST_SZ = 64 * 4096;
constexpr unsigned OFF_ST = OFF_XA + 4 * XA_SZ;
constexpr unsigned OA_T = 20480, OA_SZ = 128 * OA_T;
constexpr unsigned OFF_OA = OFF_ST + 4 * ST_SZ;
constexpr unsigned HA_T = 49152, HA_SZ = 128 * HA_T;
constexpr unsigned OFF_HA = OFF_OA + 2 * OA_SZ;
constexpr unsigned KVH_T = 3072, KVH_SZ = 8 * 32 * 8 * KVH_T;
constexpr unsigned OFF_KVH = OFF_HA + 2 * HA_SZ;
constexpr unsigned OFF_CSQ = OFF_KVH + 2 * KVH_SZ;
constexpr unsigned OFF_CSG = OFF_CSQ + 32 * 40 * 256;
constexpr unsigned WS_USED = OFF_CSG + 32 * 40 * 512;
static_assert(OFF_EP + 256 <= OFF_WQ, "");

constexpr int UNIT = 10240;
constexpr int MISC = 0, ARENA = 2048;
constexpr int KV_OFF = ARENA + 8 * UNIT, KV_T = 3072;
constexpr int L_PROG = MISC, L_LANDED = MISC + 64, L_RDY = MISC + 68, L_CSUM = MISC + 76;
constexpr int L_TAG = MISC + 80, L_TAGV = MISC + 84, L_XRDY = MISC + 88, L_STG = MISC + 92, L_WQOK = MISC + 96, L_W2OK = MISC + 100, L_CRIT = MISC + 104;
constexpr int STG = ARENA + 8 * UNIT;
constexpr int STG_X = STG + 61440;
constexpr int L_CQ = MISC + 256, L_CG = MISC + 768, L_STAMP = ARENA + 15 * UNIT;
constexpr int LDS_BYTES = ARENA + 15 * UNIT + 4096;
#define LDEPTH 2
#define WQ_ 12
#define WG_ 10
#define WO_ 10
#define WD_ 10
#define PRIO_H 0
#define PHPRIO 438
#define PP(p) do { if (PHPRIO) __builtin_amdgcn_s_setprio(p); } while (0)
#define PRETOUCH 0
#define L2GATE 0
#define DRYPASS 0
#define S2GATE 4
#define F2GATE 3u
#define ANN 0
#define TURN0 3
constexpr int NFILL = 37, NSTAGE = 38;
constexpr unsigned SPIN_CAP = 1u << 18;

struct TabQ { static constexpr int u(int i) { return i; } };
struct TabO { static constexpr int u(int i) { return i + 1; } };
struct TabG { static constexpr int u(int i) { constexpr int t[12] = {13, 14, 0, 6, 7, 8, 9, 10, 11, 12, 1, 2}; return t[i]; } };
struct TabD { static constexpr int u(int i) { constexpr int t[12] = {3, 4, 5, 13, 14, 6, 7, 8, 9, 10, 11, 12}; return t[i]; } };

template <int V> struct IC { static constexpr int v = V; };
template <int I, int N, class F> DI void sfor(F&& f) { if constexpr (I < N) { f(IC<I>{}); sfor<I + 1, N>(f); } }
DI int fresh_lane() { int l; asm volatile("v_mbcnt_lo_u32_b32 %0, -1, 0\n\tv_mbcnt_hi_u32_b32 %0, -1, %0" : "=v"(l)); return l; }
DI int opq(int v) { asm volatile("" : "+s"(v)); return v; }
DI float asf(unsigned u) { return __uint_as_float(u); }
DI unsigned asu(float f) { return __float_as_uint(f); }
DI unsigned pk(float a, float b) { f32x2 t = {a, b}; bf2 p = __builtin_convertvector(t, bf2); return __builtin_bit_cast(unsigned, p); }
DI float rbf(float a) { return asf(pk(a, a) << 16); }
DI float ex2(float a) { return __builtin_amdgcn_exp2f(a); }
DI float dot2(unsigned a, unsigned b, float c) { return __builtin_amdgcn_fdot2_f32_bf16(__builtin_bit_cast(bf2, a), __builtin_bit_cast(bf2, b), c, false); }
DI f32x16 mfma32(v4u a, v4u b, f32x16 c) { return __builtin_amdgcn_mfma_f32_32x32x16_bf16(__builtin_bit_cast(s16x8, a), __builtin_bit_cast(s16x8, b), c, 0, 0, 0); }
DI f32x16 mfma32k8(v2u a, v2u b, f32x16 c) { return __builtin_amdgcn_mfma_f32_32x32x8bf16_1k(__builtin_bit_cast(s16x4, a), __builtin_bit_cast(s16x4, b), c, 0, 0, 0); }
DI f32x4 mfma16(v4u a, v4u b, f32x4 c) { return __builtin_amdgcn_mfma_f32_16x16x32_bf16(__builtin_bit_cast(s16x8, a), __builtin_bit_cast(s16x8, b), c, 0, 0, 0); }
DI void mfma16i(f32x4& c, v4u a, v4u b) { asm volatile("v_mfma_f32_16x16x32_bf16 %0, %1, %2, %0" : "+v"(c) : "v"(a), "v"(b)); }
DI void mfma_fence(f32x4& a, f32x4& b, f32x4& c) { asm volatile("s_nop 7" : "+v"(a), "+v"(b), "+v"(c)); }
DI unsigned ufl(unsigned v) { return __builtin_amdgcn_readfirstlane(v); }
DI unsigned lds_ld(unsigned a) { return __hip_atomic_load((LAS unsigned*)a, RLX_WG); }
DI void lds_st(unsigned a, unsigned v) { __hip_atomic_store((LAS unsigned*)a, v, RLX_WG); }
DI void lds_or1(unsigned a, unsigned v) { asm volatile("s_mov_b64 exec, 1\n\tds_or_b32 %0, %1\n\ts_mov_b64 exec, -1" :: "v"(a), "v"(v) : "memory"); }
DI v4u lds128(unsigned a) { return *(LAS v4u*)a; }
DI v2u lds64(unsigned a) { return *(LAS v2u*)a; }
DI v2u ldstr(unsigned a) { return __builtin_bit_cast(v2u, __builtin_amdgcn_ds_read_tr16_b64_v4i16((LAS s16x4*)a)); }
DI float sum_halves(float x) { auto r = __builtin_amdgcn_permlane32_swap(asu(x), asu(x), false, false); unsigned a = r[0], b = r[1]; return asf(a) + asf(b); }
DI float max_halves(float x) { auto r = __builtin_amdgcn_permlane32_swap(asu(x), asu(x), false, false); unsigned a = r[0], b = r[1]; return fmaxf(asf(a), asf(b)); }
DI float sum_rows16(float x) { auto r = __builtin_amdgcn_permlane16_swap(asu(x), asu(x), false, false); unsigned a = r[0], b = r[1]; return asf(a) + asf(b); }
DI float ror8(float x) { return asf(__builtin_amdgcn_update_dpp(0u, asu(x), 0x128, 0xf, 0xf, false)); }
DI unsigned st_line(int c, int hf) { return ((c >> 2) * 8 + ((c >> 1) & 1) * 4 + hf * 2 + (c & 1)) * 128; }
DI float max3(float a, float b, float c) { return __builtin_elementwise_maximum(__builtin_elementwise_maximum(a, b), c); }

typedef __amdgpu_buffer_rsrc_t rsrc_t;
constexpr int SC1 = 16;
DI v4u gld128(rsrc_t r, unsigned v, unsigned s) { return __builtin_amdgcn_raw_buffer_load_b128(r, v, s, SC1); }
DI v2u gld64(rsrc_t r, unsigned v, unsigned s) { return __builtin_amdgcn_raw_buffer_load_b64(r, v, s, SC1); }
DI void gst128(v4u x, rsrc_t r, unsigned v, unsigned s) { __builtin_amdgcn_raw_buffer_store_b128(x, r, v + s, 0, SC1); }
DI void gst64(v2u x, rsrc_t r, unsigned v, unsigned s) { __builtin_amdgcn_raw_buffer_store_b64(x, r, v, s, SC1); }
template <int S> DI v4u lda(rsrc_t r, unsigned voff, unsigned base) { return gld128(r, voff + (S & 3) * 1024, base + (S >> 2) * 4096); }

#define STAMP_HI (SPIN_CAP - rg.bud)
#define STAMP(i) do {} while (0)
#define INNER 1
#define STAMP1(i) do { if (INNER == 1) STAMP(i); } while (0)
#define STAMP2(i) do { if (INNER == 2) STAMP(i); } while (0)
#define STAMP4(i) do { if (INNER == 4) STAMP(i); } while (0)
#define STAMP3(i) do { if (INNER == 3 && rg.ls == 0) STAMP(i); } while (0)

#define SPEND(bud) { if ((bud) >= SPIN_CAP) break; ++(bud); }
DI unsigned lds_wait_gt(unsigned a, unsigned f, unsigned& bud) {
  unsigned v;
  for (;;) {
    v = lds_ld(a);
    if (ufl(v) > f) break;
    SPEND(bud);
    __builtin_amdgcn_s_sleep(1);
  }
  CBAR();
  return v;
}
DI void lds_wait_bits(unsigned a, unsigned bits, unsigned& cache, unsigned& bud) {
  if (__builtin_expect((cache & bits) == bits, 1)) return;
  for (;;) {
    cache = ufl(lds_ld(a));
    if ((cache & bits) == bits) break;
    SPEND(bud);
    __builtin_amdgcn_s_sleep(1);
  }
  CBAR();
}
DI void prog_wait(unsigned need, unsigned& bud) {
  const int lane = fresh_lane();
  for (;;) {
    unsigned v = lds_ld(L_PROG + (lane & 7) * 4);
    if (__all(v >= need)) break;
    SPEND(bud);
    __builtin_amdgcn_s_sleep(1);
  }
  CBAR();
}
DI unsigned rmw_poll(GAS unsigned* p) {
  unsigned f;
  asm volatile("s_mov_b64 exec, 1\n\ts_nop 3\n\tglobal_atomic_or %0, %1, %1, %2 sc0\n\ts_waitcnt vmcnt(0)\n\ts_mov_b64 exec, -1" : "=&v"(f) : "v"(0u), "s"(p) : "memory");
  return ufl(f);
}
DI unsigned rmw_poll_lanes(GAS unsigned* p, unsigned voff, unsigned mlo, unsigned mhi, unsigned init) {
  asm volatile("s_mov_b32 exec_lo, %3\n\ts_mov_b32 exec_hi, %4\n\ts_nop 3\n\tglobal_atomic_or %0, %1, %5, %2 sc0\n\ts_waitcnt vmcnt(0)\n\ts_mov_b64 exec, -1"
               : "+v"(init) : "v"(voff), "s"(p), "s"(mlo), "s"(mhi), "v"(0u) : "memory");
  return init;
}
DI bool rmw_poll_pairs(GAS unsigned* p, unsigned voff, unsigned mlo, unsigned tag) {
  v2u f = {0u, 0u};
  asm volatile("s_mov_b32 exec_lo, %3\n\ts_mov_b32 exec_hi, 0\n\ts_nop 3\n\tglobal_atomic_or_x2 %0, %1, %4, %2 sc0\n\ts_waitcnt vmcnt(0)\n\ts_mov_b64 exec, -1"
               : "+v"(f) : "v"(voff), "s"(p), "s"(mlo), "v"(v2u{0u, 0u}) : "memory");
  return f[0] == tag && f[1] == tag;
}
DI void gst32(unsigned x, rsrc_t r, unsigned v) { __builtin_amdgcn_raw_buffer_store_b32(x, r, v, 0, SC1); }
#define DRAIN() asm volatile("s_waitcnt vmcnt(0)" ::: "memory")
#define WG_BARRIER() do { asm volatile("s_waitcnt lgkmcnt(0)" ::: "memory"); __builtin_amdgcn_s_barrier(); CBAR(); } while (0)
DI void ctr_wait(unsigned char* ws, int kind, int t32, int rep, unsigned& bud) {
  GAS unsigned* p = (GAS unsigned*)(ws + OFF_CTR + (kind * 64 + t32) * 1024 + rep * 256);
  for (;;) {
    if (rmw_poll(p) >= 32u) break;
    SPEND(bud);
  }
  __builtin_amdgcn_fence(__ATOMIC_ACQUIRE, "wavefront");
  CBAR();
}
DI void ctr_arrive(unsigned char* ws, int kind, int t32) {
  const int lane = fresh_lane();
  GAS unsigned* p = (GAS unsigned*)(ws + OFF_CTR + (kind * 64 + t32) * 1024);
  asm volatile("s_waitcnt vmcnt(0)\n\ts_mov_b64 exec, 15\n\ts_nop 3\n\tglobal_atomic_add %0, %1, %2\n\ts_mov_b64 exec, -1" :: "v"(lane * 256), "v"(1u), "s"(p) : "memory");
}

DI void dma_fill(v4u srd, unsigned voff, unsigned soff, unsigned ldst) {
  unsigned keep, so;
  asm volatile(
      "s_mov_b32 %0, m0\n\ts_mov_b32 m0, %5\n\ts_add_u32 %1, %4, 0x1000\n\t"
      "buffer_load_dwordx4 %2, %3, %4 offen sc1 lds\n\tbuffer_load_dwordx4 %2, %3, %4 offen offset:1024 sc1 lds\n\t"
      "buffer_load_dwordx4 %2, %3, %4 offen offset:2048 sc1 lds\n\tbuffer_load_dwordx4 %2, %3, %4 offen offset:3072 sc1 lds\n\t"
      "s_add_u32 m0, m0, 0x1000\n\ts_nop 0\n\t"
      "buffer_load_dwordx4 %2, %3, %1 offen sc1 lds\n\tbuffer_load_dwordx4 %2, %3, %1 offen offset:1024 sc1 lds\n\t"
      "buffer_load_dwordx4 %2, %3, %1 offen offset:2048 sc1 lds\n\tbuffer_load_dwordx4 %2, %3, %1 offen offset:3072 sc1 lds\n\t"
      "s_add_u32 m0, m0, 0x1000\n\ts_add_u32 %1, %1, 0x1000\n\t"
      "buffer_load_dwordx4 %2, %3, %1 offen sc1 lds\n\tbuffer_load_dwordx4 %2, %3, %1 offen offset:1024 sc1 lds\n\t"
      "s_mov_b32 m0, %0"
      : "=&s"(keep), "=&s"(so) : "v"(voff), "s"(srd), "s"(soff), "s"(ldst) : "memory", "scc");
}

DI void dma_raw(const unsigned char* base, unsigned bytes, unsigned voff, unsigned soff, unsigned sstep, unsigned ldst, int n) {
  const v4u srd = {(unsigned)(uintptr_t)base, (unsigned)((uintptr_t)base >> 32) & 0xffffu, bytes, 0x00020000u};
#pragma unroll 1
  for (int i = 0; i < n; ++i) {
    unsigned keep;
    asm volatile("s_mov_b32 %0, m0\n\ts_mov_b32 m0, %4\n\ts_nop 0\n\tbuffer_load_dwordx4 %1, %2, %3 offen lds\n\ts_mov_b32 m0, %0"
                 : "=&s"(keep) : "v"(voff), "s"(srd), "s"(soff + i * sstep), "s"(ldst + i * 1024) : "memory");
  }
}
DI unsigned lds_inc1(unsigned a) {
  unsigned r;
  asm volatile("s_mov_b64 exec, 1\n\tds_add_rtn_u32 %0, %1, %2\n\ts_waitcnt lgkmcnt(0)\n\ts_mov_b64 exec, -1" : "=&v"(r) : "v"(a), "v"(1u) : "memory");
  return ufl(r);
}
DI void lds_add1(unsigned a) { asm volatile("s_mov_b64 exec, 1\n\tds_add_u32 %0, %1\n\ts_mov_b64 exec, -1" :: "v"(a), "v"(1u) : "memory"); }

struct Ring {
  unsigned lv;
  unsigned lf, ls;
  unsigned prog;
  unsigned bud;
};
DI void ring_check(Ring& r, int f) {
  if (__builtin_expect(ufl(r.lv) <= r.lf + f, 0)) r.lv = lds_wait_gt(L_LANDED, r.lf + f, r.bud);
  CBAR();
}
DI void ring_peek(Ring& r) { r.lv = lds_ld(L_LANDED); }
constexpr int BSTEP = 61440;

template <int NB, int W, int DIST, int F0, int S0, class Tab, int CS, class Wait>
DI void gemm32(f32x16 (&acc)[NB], float& rs, Ring& rg, rsrc_t ws, unsigned abase, unsigned stbase, Wait&& wait) {
  const int lane = fresh_lane();
  const unsigned voff = lane * 16;
  v4u a[W], b[DIST + 1][NB];
  unsigned lb[3];
#pragma unroll
  for (int i = 0; i < 3; ++i) { lb[i] = voff + i * BSTEP; asm volatile("" : "+v"(lb[i])); }
  unsigned pa = rg.prog, pv = rg.ls + S0;
  asm volatile("" : "+v"(pa), "+v"(pv));
  v2u cf[NB], xm;
  v4u st[4];
  float s_ = 0.f, q_ = 0.f, s1_ = 0.f, q1_ = 0.f;
  unsigned csv = 0;
  auto readB = [&](auto P) INL {
    constexpr int ph = P.v;
    if constexpr (ph < 40 * NB) {
      constexpr int ad = ARENA + Tab::u(ph / 10) * UNIT + (ph % 10) * 1024;
      b[(ph / NB) % (DIST + 1)][ph % NB] = lds128(lb[ad / BSTEP] + ad % BSTEP);
    }
    if constexpr (ph % 10 == 3 && ph + 7 < 40 * NB) ring_peek(rg);
    if constexpr (ph % 10 == 8 && ph + 2 < 40 * NB) ring_check(rg, F0 + ph / 10 + 1);
    if constexpr (ph % 10 == 0 && ph > 0 && ph <= 40 * NB) { CBAR(); ++pv; lds_st(pa, pv); CBAR(); }
  };
  wait();
  sfor<0, W>([&](auto S) INL { a[S.v] = lda<S.v>(ws, voff, abase); });
  SCHED();
  ring_peek(rg);
  ring_check(rg, F0);
  sfor<0, DIST * NB>(readB);
  SCHED();
  constexpr int X1 = 40 * NB - 14;
  constexpr int X0 = X1 - 28;
  sfor<0, 40 * NB>([&](auto SL) INL {
    constexpr int sl = SL.v, s = sl / NB, t = sl % NB;
    if constexpr (s == 0) { f32x16 z; _Pragma("unroll") for (int r = 0; r < 16; ++r) z[r] = 0.f; acc[t] = mfma32(b[0][t], a[0], z); }
    else acc[t] = mfma32(b[s % (DIST + 1)][t], a[s % W], acc[t]);
    SCHED();
    readB(IC<sl + DIST * NB>{});
    if constexpr (t == NB - 1 && s + W < 40) a[s % W] = lda<s + W>(ws, voff, abase);
    if constexpr (sl >= X0 && sl < X0 + 8 && (sl - X0) % 2 == 0) st[(sl - X0) / 2] = lda<(sl - X0) / 2>(ws, voff, stbase);
    if constexpr (sl == X1 - 8) csv = lds_ld(L_CSUM);
    if constexpr (sl == X1 - 3) { if (__builtin_expect(ufl(csv) < NB - 1, 0)) lds_wait_gt(L_CSUM, NB - 2, rg.bud); CBAR(); }
    if constexpr (sl >= X1 && sl < X1 + 3) {
      constexpr int i = sl - X1 + 1;
      const unsigned u0 = st[i][0], u1 = st[i][1], u2 = st[i][2], u3 = st[i][3];
      if constexpr (i == 1) { const unsigned w0 = st[0][0], w1 = st[0][1], w2 = st[0][2], w3 = st[0][3]; s_ = asf(w0); q_ = asf(w1); s1_ = asf(w2); q1_ = asf(w3); }
      s_ += asf(u0); q_ += asf(u1); s1_ += asf(u2); q1_ += asf(u3);
    }
    if constexpr (sl == X1 + 3) { const bool up = lane & 8; s_ = (up ? s1_ : s_) + ror8(up ? s_ : s1_); }
    if constexpr (sl == X1 + 4) { const bool up = lane & 8; q_ = (up ? q1_ : q_) + ror8(up ? q_ : q1_); }
    if constexpr (sl == X1 + 5) s_ = sum_halves(s_) * (1.f / 640.f);
    if constexpr (sl == X1 + 6) q_ = sum_halves(q_) * (1.f / 640.f);
    if constexpr (sl == X1 + 7) {
      q_ = fmaxf(q_ - s_ * s_, 0.f);
      const unsigned nm = pk(-s_, -s_);
      xm[0] = lane < 32 ? nm : 0u; xm[1] = lane < 32 ? (nm & 0xffffu) : 0u;
    }
    if constexpr (sl == X1 + 8) {
      q_ = rbf(rbf(q_) + 1e-5f);
      sfor<0, NB>([&](auto T) INL { cf[T.v] = lds64(CS + T.v * 256 + (lane & 31) * 8); });
    }
    if constexpr (sl == X1 + 9) rs = __builtin_amdgcn_rcpf(rbf(__builtin_amdgcn_sqrtf(q_)));
    SCHED();
  });
  sfor<0, NB>([&](auto T) INL { acc[T.v] = mfma32k8(cf[T.v], xm, acc[T.v]); });
}

template <int NS, int W, int DIST, int F0, int S0, class Tab, class Wait>
DI void gemm16(Ring& rg, rsrc_t ws, unsigned abase, unsigned (&res)[6], unsigned xbase, unsigned stbase, bool last, void* out, int t16i, int ct, Wait&& wait) {
  const int lane = fresh_lane();
  const unsigned voff = lane * 16;
  v4u a[W], b[DIST + 1][3];
  unsigned lb[3], lc[3];
  const int g4 = lane >> 4;
#pragma unroll
  for (int i = 0; i < 3; ++i) {
    lb[i] = voff + i * BSTEP; asm volatile("" : "+v"(lb[i]));
    lc[i] = g4 * 128 + (lane & 7) * 16 + i * BSTEP + ((lane & 8) ? 0x40000 : 0); asm volatile("" : "+v"(lc[i]));
  }
  unsigned pa = rg.prog, pv = rg.ls + S0;
  asm volatile("" : "+v"(pa), "+v"(pv));
  f32x4 acc[3];
#pragma unroll
  for (int t = 0; t < 3; ++t) acc[t] = f32x4{asf(res[2 * t] << 16), asf(res[2 * t] & 0xffff0000u), asf(res[2 * t + 1] << 16), asf(res[2 * t + 1] & 0xffff0000u)};
  auto readB = [&](auto S, auto T) INL {
    constexpr int s = S.v, t = T.v;
    if constexpr (s < NS) {
      constexpr int ad = ARENA + Tab::u(s / 4) * UNIT + (s % 4) * 2560;
      b[s % (DIST + 1)][t] = lds128((t < 2 ? lb : lc)[ad / BSTEP] + ad % BSTEP + t * 1024);
    }
    if constexpr (t == 0 && s % 4 == 1 && s + 3 < NS) ring_peek(rg);
    if constexpr (t == 1 && s % 4 == 3 && s + 1 < NS) ring_check(rg, F0 + s / 4 + 1);
    if constexpr (t == 0 && s % 4 == 0 && s > 0 && s <= NS) { CBAR(); ++pv; lds_st(pa, pv); CBAR(); }
  };
  const unsigned t32 = (t16i & 1) * 16 + (lane & 15);
  const unsigned pi = 2 * (g4 & 1) + (g4 >> 1);
  unsigned so1 = xbase + (5 * ct) * 512 + pi * 512 + t32 * 16, so2 = xbase + (5 * ct + 4) * 512 + t32 * 16 + (lane < 16 ? 0u : WS_USED);
  unsigned so3 = stbase + st_line(ct, t16i & 1) + (lane & 7) * 16 + (lane & 8) + (lane < 16 ? 0u : WS_USED);
  asm volatile("" : "+v"(so1), "+v"(so2), "+v"(so3), "+v"(acc[0]), "+v"(acc[1]), "+v"(acc[2]));
  wait();
  sfor<0, W>([&](auto S) INL { a[S.v] = lda<S.v>(ws, voff, abase); });
  SCHED();
  ring_peek(rg);
  ring_check(rg, F0);
  sfor<0, DIST * 3>([&](auto I) INL { readB(IC<I.v / 3>{}, IC<I.v % 3>{}); });
  SCHED();
  if constexpr (NS == 48) { STAMP1(27); }
  sfor<0, NS>([&](auto S) INL {
    constexpr int s = S.v;
    if constexpr (NS == 48 && s % 12 == 1) STAMP3(24 + s / 12);
    sfor<0, 3>([&](auto T) INL {
      constexpr int t = 2 - T.v;
      mfma16i(acc[t], b[s % (DIST + 1)][t], a[s % W]);
      SCHED();
      readB(IC<s + DIST>{}, T);
      if constexpr (T.v == 2 && s + W < NS) a[s % W] = lda<s + W>(ws, voff, abase);
      SCHED();
    });
  });
  mfma_fence(acc[0], acc[1], acc[2]);
  if constexpr (NS == 48) { STAMP1(28); STAMP3(28); }
#pragma unroll
  for (int t = 0; t < 3; ++t) { res[2 * t] = pk(acc[t][0], acc[t][1]); res[2 * t + 1] = pk(acc[t][2], acc[t][3]); }
  auto s0 = __builtin_amdgcn_permlane16_swap(res[0], res[2], false, false);
  auto s1 = __builtin_amdgcn_permlane16_swap(res[1], res[3], false, false);
  auto s2 = __builtin_amdgcn_permlane16_swap(res[4], res[4], false, false);
  auto s3 = __builtin_amdgcn_permlane16_swap(res[5], res[5], false, false);
  const v4u o01 = {s0[0], s1[0], s0[1], s1[1]};
  const v4u o2 = {s2[0], s3[0], s2[1], s3[1]};
  if (last) {
    GAS char* ob = (GAS char*)out + (size_t)(t16i * 16 + (lane & 15)) * 1280 + ct * 80;
    *(GAS v4u*)(ob + pi * 16) = o01;
    if (lane < 16) *(GAS v4u*)(ob + 64) = o2;
  } else {
    gst128(o01, ws, so1, 0);
    gst128(o2, ws, so2, 0);
    const f32x4 sv = acc[0] + acc[1] + acc[2], qv = acc[0] * acc[0] + acc[1] * acc[1] + acc[2] * acc[2];
    float s = (sv[0] + sv[1]) + (sv[2] + sv[3]), q = (qv[0] + qv[1]) + (qv[2] + qv[3]);
    s = sum_halves(sum_rows16(s));
    q = sum_halves(sum_rows16(q));
    gst64(v2u{asu(s), asu(q)}, ws, so3, 0);
  }
}

constexpr float QSCALE = 0.22360679774997896f * 1.4426950408889634f;
constexpr float NEG = -1e30f;

DI float max16(const f32x16& s) {
  const float a = max3(s[0], s[1], s[2]), b = max3(s[3], s[4], s[5]), c = max3(s[6], s[7], s[8]);
  const float d = max3(s[9], s[10], s[11]), e = max3(s[12], s[13], s[14]);
  return __builtin_elementwise_maximum(max3(a, b, c), max3(d, e, s[15]));
}
DI void kv_write(unsigned kb, int lane, v4u k1, v4u ta, v4u tb) {
  *(LAS v4u*)(kb + lane * 16) = k1;
  *(LAS v2u*)(kb + 1024 + (lane ^ 32) * 8) = v2u{ta[0], ta[1]};
  const unsigned vb = kb + 1536 + (lane & 31) * 48 + (lane >> 5) * 8;
  *(LAS v2u*)(vb) = v2u{ta[2], ta[3]};
  *(LAS v2u*)(vb + 16) = v2u{tb[0], tb[1]};
  *(LAS v2u*)(vb + 32) = v2u{tb[2], tb[3]};
}
constexpr float MB = 16.f;

DI int kmapO4(int P, int t) {
  const int p = P / 5, r = P - 5 * p;
  if (r == 2) return 20 * (2 * p + t) + 16;
  return 20 * (2 * p + (r > 2)) + 8 * t + 4 * (r > 2 ? r - 3 : r);
}
DI v4u tr_frag(unsigned a0, unsigned a1) { const v2u x = ldstr(a0), y = ldstr(a1); return v4u{x[0], x[1], y[0], y[1]}; }
DI float sum8(v4u f, float s) {
#pragma unroll
  for (int i = 0; i < 4; ++i) { const unsigned u = f[i]; s = dot2(u, 0x3f803f80u, s); }
  return s;
}

DI void csum_rows(f32x4 c, unsigned la) {
  unsigned o[8];
#pragma unroll
  for (int i = 0; i < 4; ++i) {
    const float v = c[i], hi = rbf(v), mid = rbf(v - hi), lo = rbf(v - hi - mid);
    o[2 * i] = pk(hi, mid); o[2 * i + 1] = pk(lo, 0.f);
  }
  *(LAS v4u*)la = v4u{o[0], o[1], o[2], o[3]};
  *(LAS v4u*)(la + 16) = v4u{o[4], o[5], o[6], o[7]};
}

__global__ void __launch_bounds__(768) mega_kernel(unsigned char* wsp, void* out, const unsigned char* xin, const unsigned char* wqkv, const unsigned char* wo,
                                                   const unsigned char* wg, const unsigned char* wu, const unsigned char* wd) {
  __shared__ __attribute__((aligned(16))) char smem[LDS_BYTES];
  const int wave = ufl(threadIdx.x >> 6);
  asm volatile("" :: "v"((LAS char*)smem) : "memory");
  const int bx = blockIdx.x & 7, br = blockIdx.x >> 3;
#define GMAP 5
  const int band = (bx & 4) + (br >> 3), j = (bx & 3) * 8 + (br & 7);
  const int ct = (j >> 4) * 8 + (j & 7), rsel = (j >> 3) & 1;
  const rsrc_t ws = __builtin_amdgcn_make_buffer_rsrc(wsp, 0, WS_USED, 0x00020000);
  constexpr unsigned OOR = 0x40000000u;
  if (wave == 0) lds_st(MISC + (fresh_lane() & 31) * 4, 0u);
  WG_BARRIER();
  const int tk = (wave - 4) * 256 + bx * 32 + br;
  const unsigned sb = wave < 4 ? STG_X + (wave - 1) * 5120 : wave < 6 ? STG + (wave - 4) * 6144 : STG - 4096 + (wave - 4) * 8192;
  {
    const int lane = fresh_lane();
    const bool isx = wave < 4;
    dma_raw(isx ? xin : wqkv, isx ? 2048 * 1280 : 640 * 3840, isx || (lane & 7) < 5 ? (lane >> 3) * 1280 + (lane & 7) * 16 : OOR,
            isx ? (band * 256 + j * 8) * 1280 + (wave - 1) * 640 : (tk >> 4) * (16 * 3840) + (tk & 15) * 80, isx ? 128 : 10240, sb,
            opq(isx ? (wave == 1 || wave == 2 ? 5 : 0) : tk < 640 ? 6 : 0));
  }

  if (wave == 0) {
    const int lane = fresh_lane();
    __builtin_amdgcn_s_setprio(3);
    const v4u wsd = {(unsigned)(uintptr_t)wsp, (unsigned)((uintptr_t)wsp >> 32) & 0xffffu, WS_USED, 0x00020000u};
    unsigned bud = 0;
    const unsigned tag = ufl(__builtin_amdgcn_raw_buffer_load_b32(ws, 0, OFF_EP, SC1)) + 1u;
    lds_st(L_TAG, tag);
    CBAR();
    lds_st(L_TAGV, 1u);
    for (;;) {
      if (((unsigned)__ballot(rmw_poll_pairs((GAS unsigned*)(wsp + OFF_IN + (band * 32 + j) * 512), lane * 8, 0xfffffu, tag)) & 0xfffffu) == 0xfffffu) break;
      SPEND(bud);
    }
    lds_st(L_WQOK, 1u);
#pragma unroll 1
    for (int f = 0; f < 6; ++f) dma_fill(wsd, lane * 16, OFF_WQ + j * 81920 + f * UNIT, ARENA + f * UNIT);
    asm volatile("s_waitcnt vmcnt(50)" ::: "memory"); lds_st(L_LANDED, 1u);
    dma_fill(wsd, lane * 16, OFF_WQ + j * 81920 + 6 * UNIT, ARENA + 6 * UNIT);
    asm volatile("s_waitcnt vmcnt(50)" ::: "memory"); lds_st(L_LANDED, 2u);
    dma_fill(wsd, lane * 16, OFF_WQ + j * 81920 + 7 * UNIT, ARENA + 7 * UNIT);
    asm volatile("s_waitcnt vmcnt(50)" ::: "memory"); lds_st(L_LANDED, 3u);
    asm volatile("s_waitcnt vmcnt(40)" ::: "memory"); lds_st(L_LANDED, 4u);
    asm volatile("s_waitcnt vmcnt(30)" ::: "memory"); lds_st(L_LANDED, 5u);
    asm volatile("s_waitcnt vmcnt(20)" ::: "memory"); lds_st(L_LANDED, 6u);
    asm volatile("s_waitcnt vmcnt(10)" ::: "memory"); lds_st(L_LANDED, 7u);
    asm volatile("s_waitcnt vmcnt(0)" ::: "memory"); lds_st(L_LANDED, 8u);
    __builtin_amdgcn_s_setprio(PRIO_H);
    lds_wait_gt(L_W2OK, 0u, bud);
    unsigned issued = 8, published = 8;
#pragma unroll 1
    for (int lp = 1; lp < 8; ++lp) {
      const int L = lp >> 2, ph = lp & 3;
      unsigned long long units, needs_lo, needs_hi; int n; unsigned src;
      if (ph == 0) { units = 0x76543210ull; n = 8; src = OFF_WQ + j * 81920;
        needs_lo = 17ull | 25ull << 8 | 26ull << 16 | 27ull << 24 | 28ull << 32 | 29ull << 40 | 32ull << 48 | 33ull << 56; needs_hi = 0; }
      else if (ph == 1) { units = 0x54321ull; n = 5; src = OFF_WO + ct * 51200;
        needs_lo = 40ull | 41ull << 8 | 42ull << 16 | 43ull << 24 | 44ull << 32; needs_hi = 0; }
      else if (ph == 2) { units = 0x21CBA98760EDull; n = 12; src = OFF_WG + j * 122880;
        needs_lo = 30ull | 31ull << 8 | 39ull << 16 | 45ull << 24 | 46ull << 32 | 47ull << 40 | 47ull << 48 | 47ull << 56;
        needs_hi = 47ull | 47ull << 8 | 48ull << 16 | 49ull << 24; }
      else { units = 0xCBA9876ED543ull; n = 12; src = OFF_WD + ct * 122880;
        needs_lo = 50ull | 51ull << 8 | 52ull << 16 | 53ull << 24 | 54ull << 32 | 56ull << 40 | 57ull << 48 | 58ull << 56;
        needs_hi = 59ull | 60ull << 8 | 61ull << 16 | 62ull << 24; }
#pragma unroll 1
      for (int f = 0; f < n; ++f) {
        const int unit = (int)((units >> (4 * f)) & 15ull);
        const int nd = (int)(((f < 8 ? needs_lo >> (8 * f) : needs_hi >> (8 * (f - 8)))) & 255ull) + (L - 1) * NSTAGE;
        if (nd > 0) {
          unsigned v = lds_ld(L_PROG + (lane & 7) * 4);
          if (!__all((int)v >= nd)) {
            if (published != issued) {
              if (issued - published >= 2) { asm volatile("s_waitcnt vmcnt(10)" ::: "memory"); lds_st(L_LANDED, issued - 1); }
              asm volatile("s_waitcnt vmcnt(0)" ::: "memory");
              published = issued; lds_st(L_LANDED, published);
            }
            prog_wait((unsigned)nd, bud);
          }
        }
        dma_fill(wsd, lane * 16, src + f * UNIT, ARENA + unit * UNIT);
        ++issued;
        if (issued - published > LDEPTH) {
          asm volatile("s_waitcnt vmcnt(%0)" :: "i"(10 * LDEPTH) : "memory");
          published = issued - LDEPTH; lds_st(L_LANDED, published);
        }
      }
    }
    asm volatile("s_waitcnt vmcnt(10)" ::: "memory");
    lds_st(L_LANDED, issued - 1);
    asm volatile("s_waitcnt vmcnt(0)" ::: "memory");
    lds_st(L_LANDED, issued);
    return;
  }

  if (wave >= 4) {
    const int cw = wave - 4, grp = cw >> 2, wi = cw & 3;
    const int qt = (1 - grp) * 4 + wi;
    const int t32_ = band * 8 + qt;
    const int t16_ = band * 16 + (1 - grp) * 8 + rsel * 4 + wi;
    const int rep = j & 3;
    Ring rg;
    rg.prog = L_PROG + cw * 4;
    rg.bud = 0;
#define PRIO_E 2
#define PRIO_L 1
    if (grp == 0) __builtin_amdgcn_s_setprio(PRIO_E); else __builtin_amdgcn_s_setprio(PRIO_L);
    const int dslot = qt + (qt < 4 ? 4 : 8);
    STAMP(30);
    STAMP(0);
    unsigned res[6] = {0u, 0u, 0u, 0u, 0u, 0u};
    unsigned tag;
    {
      const int lane = fresh_lane();
      const int gam = lane >> 4, q = (lane & 15) >> 2, p = lane & 3, hh = gam >> 1, g = (gam & 1) * 4 + p;
      const v4u ones = {0x3f803f80u, 0x3f803f80u, 0x3f803f80u, 0x3f803f80u};
      f32x16 z;
#pragma unroll
      for (int i = 0; i < 16; ++i) z[i] = 0.f;
      if (tk < 640) {
        const int hp = tk & 15, ks = tk >> 4;
        const unsigned r = sb + (8 * hh + q) * 384;
        unsigned c0 = r + (g < 4 ? 0 : 128) + (g & 3) * 8, c1 = r + (g == 0 ? 32 : g == 1 ? 128 + 32 : 256 + ((g < 7 ? g : 6) - 2) * 8);
        unsigned dst = OFF_WQ + hp * 163840 + ks * 2048 + lane * 16, cs = OFF_CSQ + (hp * 80 + ks) * 256 + lane * 4;
        unsigned fa = lane < 16 ? OFF_IN + ((lane >> 1) * 32 + 2 * hp + (lane & 1)) * 512 + ks * 4 : OOR;
        STAMP4(24);
#pragma unroll 1
        for (int pass = DRYPASS ? 0 : 1; pass < 2; ++pass) {
          const unsigned off = pass ? 0u : OOR;
          if (pass) DRAIN();
          STAMP4(25);
          v4u f[2][2];
#pragma unroll
          for (int h2 = 0; h2 < 2; ++h2) {
            f[h2][0] = tr_frag(c0 + h2 * 40, c0 + h2 * 40 + 1536);
            f[h2][1] = tr_frag(c1 + h2 * 40, c1 + h2 * 40 + 1536);
          }
#pragma unroll
          for (int h2 = 0; h2 < 2; ++h2) {
            if ((lane & 31) >= 28) f[h2][1] = v4u{0u, 0u, 0u, 0u};
            gst128(f[h2][0], ws, dst + off + h2 * 81920, 0); gst128(f[h2][1], ws, dst + off + h2 * 81920 + 1024, 0);
          }
#pragma unroll
          for (int h2 = 0; h2 < 2; ++h2) {
            const float s0 = mfma32(ones, f[h2][0], z)[0], s1 = mfma32(ones, f[h2][1], z)[0];
            gst32(asu(lane < 32 ? s0 : s1), ws, cs + off + h2 * 10240);
          }
          if (pass) lds_wait_gt(L_TAGV, 0u, rg.bud);
          tag = ufl(lds_ld(L_TAG));
          STAMP4(26);
          if (pass) DRAIN();
          STAMP4(27);
          gst32(tag, ws, fa + off);
        }
        lds_add1(L_CRIT);
      } else {
        const bool gu = tk < 1280, dn = tk < 1664;
        const int i = gu ? tk - 640 : dn ? tk - 1280 : tk - 1664;
        unsigned a0, a1, a2, a3, dst, o2;
        if (L2GATE) lds_wait_gt(L_CRIT, L2GATE - 1, rg.bud);
        if (gu) {
          const unsigned vo = (lane & 15) < 12 ? gam * 3072 + (lane & 15) * 16 : OOR, so = (i >> 4) * (16 * 3072) + (i & 15) * 192;
          dma_raw(wg, 640 * 3072, vo, so, 4 * 3072, sb, 4);
          dma_raw(wu, 640 * 3072, vo, so, 4 * 3072, sb + 4096, 4);
          const unsigned r = sb + (8 * hh + q) * 256;
          a0 = r + g * 8; a1 = a0 + 4096; a2 = r + 64 + (g & 3) * 8 + (g < 4 ? 0 : 4096); a3 = 96;
          dst = OFF_WG + (i & 15) * 245760 + (i >> 4) * 3072; o2 = 2048 + lane * 16;
        } else {
          const unsigned vo = (lane & 15) < 10 ? gam * 1280 + (lane & 15) * 16 : OOR;
          if (tk < 1824) {
#pragma unroll 1
            for (int u = 0; u < 8; ++u) {
              const int P = 4 * (i >> 3) + (u >> 1);
              const int row = dn ? 16 * (P >> 1) + 4 * (P & 1) + 8 * (u & 1) : kmapO4(P, u & 1);
              dma_raw(dn ? wd : wo, dn ? 1536 * 1280 : 640 * 1280, vo, row * 1280 + (i & 7) * 160, 0, sb + u * 1024, 1);
            }
          }
          const unsigned r = sb + gam * 2048 + q * 256;
          a0 = r + p * 8; a1 = a0 + 32; a2 = r + 64 + (p & 1) * 8; a3 = 80;
          dst = tk >= 1824 ? OOR : dn ? OFF_WD + (i & 7) * 245760 + (i >> 3) * 2560 : OFF_WO + (i & 7) * 102400 + (i >> 3) * 2560;
          o2 = (lane & 15) < 8 ? 2048 + gam * 128 + (lane & 7) * 16 : OOR;
        }
        const unsigned dstep = gu || dn ? 122880 : 51200;
        STAMP4(24);
        if (cw == 7) {
          const int ci = j * 7 + lane;
          gst32(0u, ws, lane < 7 ? OFF_CTR + ((ci >> 5) * 64 + band * 8 + ((ci >> 2) & 7)) * 1024 + (ci & 3) * 256 : OOR);
        }
        asm volatile("s_waitcnt vmcnt(0)" : "+v"(a0), "+v"(a1), "+v"(a2), "+v"(o2) :: "memory");
        STAMP4(25);
        v4u f[2][3];
#pragma unroll
        for (int h2 = 0; h2 < 2; ++h2) {
          f[h2][0] = tr_frag(a0 + h2 * a3, a0 + h2 * a3 + 1024);
          f[h2][1] = tr_frag(a1 + h2 * a3, a1 + h2 * a3 + 1024);
          f[h2][2] = tr_frag(a2 + h2 * a3, a2 + h2 * a3 + 1024);
        }
        if (S2GATE) lds_wait_gt(L_CRIT, S2GATE - 1, rg.bud);
#pragma unroll
        for (int h2 = 0; h2 < 2; ++h2) {
          gst128(f[h2][0], ws, dst + h2 * dstep + lane * 16, 0); gst128(f[h2][1], ws, dst + h2 * dstep + lane * 16 + 1024, 0);
          gst128(f[h2][2], ws, dst + h2 * dstep + o2, 0);
        }
        if (gu) {
#pragma unroll
          for (int h2 = 0; h2 < 2; ++h2) {
            const float s0 = mfma32(ones, f[h2][0], z)[0], s1 = mfma32(ones, f[h2][1], z)[0], s2 = mfma32(ones, f[h2][2], z)[0];
            const unsigned cs = OFF_CSG + ((2 * (i & 15) + h2) * 40 + (i >> 4)) * 512 + lane * 4;
            gst32(asu(lane < 32 ? s0 : s1), ws, cs);
            gst32(lane < 32 ? asu(s2) : 0u, ws, cs + 256);
          }
        }
        lds_wait_gt(L_TAGV, 0u, rg.bud);
        tag = ufl(lds_ld(L_TAG));
        STAMP4(26);
        DRAIN();
        STAMP4(27);
      }
      if (lds_inc1(L_STG) == 7u) gst32(tag, ws, lane == 0 ? OFF_F2 + blockIdx.x * 4 : OOR);
    }
    STAMP4(28);
    { unsigned xr = 0; lds_wait_bits(L_XRDY, 0xfu << (4 * qt), xr, rg.bud); }
    if (TURN0 > 0 && grp == 1) lds_wait_gt(rg.prog - 16, TURN0 - 1, rg.bud);
#pragma unroll 1
    for (int L = 0; L < 2; ++L) {
      rg.lf = L * NFILL; rg.ls = L * NSTAGE;
      const int t32i = opq(t32_), t16i = opq(t16_);
      PP(PHPRIO & 3);
      v4u q1; v2u q2;
      {
        f32x16 acc[2]; float rs;
        gemm32<2, WQ_, 2, 0, 0, TabQ, L_CQ>(acc, rs, rg, ws, OFF_XA + (2 * L) * XA_SZ + t32i * XA_T, OFF_ST + (2 * L) * ST_SZ + t32i * 4096,
                                            [&]() INL { if (L > 0) ctr_wait(wsp, 3, t32i, rep, rg.bud); STAMP(1 + L * 12); });
        STAMP(2 + L * 12);
        const int lane = fresh_lane(), h = lane >> 5;
        v4u k1, ta, tb;
        const float rq = rs * QSCALE, r0 = h ? rs : rq;
#pragma unroll
        for (int i = 0; i < 4; ++i) k1[i] = pk(acc[0][8 + 2 * i] * rs, acc[0][9 + 2 * i] * rs);
        ta[0] = pk(acc[1][0] * r0, acc[1][1] * r0); ta[1] = pk(acc[1][2] * r0, acc[1][3] * r0);
        ta[2] = pk(acc[1][4] * rs, acc[1][5] * rs); ta[3] = pk(acc[1][6] * rs, acc[1][7] * rs);
#pragma unroll
        for (int i = 0; i < 4; ++i) tb[i] = pk(acc[1][8 + 2 * i] * rs, acc[1][9 + 2 * i] * rs);
        if (h) { tb[2] = 0x3f803f80u; tb[3] = 0x3f803f80u; }
        q2[0] = h ? 0x3f80u : ta[0]; q2[1] = h ? 0u : ta[1];
        if (!h) { ta[0] = 0u; ta[1] = 0u; }
        if (band < 7) {
          const unsigned gb = OFF_KVH + L * KVH_SZ + ((band * 32 + j) * 8 + qt) * KVH_T;
          gst128(k1, ws, lane * 16, gb); gst128(ta, ws, lane * 16 + 1024, gb); gst128(tb, ws, lane * 16 + 2048, gb);
        }
        if (L > 0) prog_wait(L * NSTAGE, rg.bud); else lds_wait_gt(L_STG, 7u, rg.bud);
        kv_write(KV_OFF + dslot * KV_T, lane, k1, ta, tb);
        CBAR();
        lds_or1(L_RDY + L * 4, 1u << dslot);
#pragma unroll
        for (int i = 0; i < 4; ++i) q1[i] = pk(acc[0][2 * i] * rq, acc[0][2 * i + 1] * rq);
      }
      STAMP(3 + L * 12);
      PP((PHPRIO >> 2) & 3);
      {
        const int lane = fresh_lane(), h = lane >> 5;
        f32x16 o, negm, sc[2];
        const int topA = dslot, topB = grp == 0 ? 8 : 12;
        const int top0 = grp == 0 ? topA : topB, top1 = grp == 0 ? topB : topA;
        const unsigned vlo = 1536 + (4 * h + ((lane & 15) >> 2)) * 48 + (((lane >> 4) & 1) == 0 ? 8 * (lane & 3) : ((lane & 3) == 0 ? 32 : 40));
        unsigned ka[2], kb[2], va[2], vd;
        ka[0] = KV_OFF + (top0 - 4) * KV_T + lane * 16; ka[1] = KV_OFF + (top1 - 4) * KV_T + lane * 16;
        kb[0] = KV_OFF + (top0 - 4) * KV_T + 1024 + lane * 8; kb[1] = KV_OFF + (top1 - 4) * KV_T + 1024 + lane * 8;
        va[0] = KV_OFF + (top0 - 4) * KV_T + vlo; va[1] = KV_OFF + (top1 - 4) * KV_T + vlo;
        vd = KV_OFF + dslot * KV_T + vlo;
        asm volatile("" : "+v"(ka[0]), "+v"(ka[1]), "+v"(kb[0]), "+v"(kb[1]), "+v"(va[0]), "+v"(va[1]), "+v"(vd));
        const int ql = (lane & 31) - 4 * h;
        unsigned rdyv = lds_ld(L_RDY + L * 4);
        {
          f32x16 z;
#pragma unroll
          for (int r = 0; r < 16; ++r) { z[r] = 0.f; o[r] = 0.f; }
          sc[0] = mfma32(lds128(KV_OFF + dslot * KV_T + lane * 16), q1, z);
          sc[0] = mfma32k8(lds64(KV_OFF + dslot * KV_T + 1024 + lane * 8), q2, sc[0]);
#pragma unroll
          for (int r = 0; r < 16; ++r) sc[0][r] = ((r & 3) + 8 * (r >> 2)) <= ql ? sc[0][r] : NEG;
          const float m = max_halves(max16(sc[0])) + MB;
#pragma unroll
          for (int r = 0; r < 16; ++r) { sc[0][r] -= m; negm[r] = -m; }
        }
        STAMP2(24);
        unsigned rdy = ufl(rdyv);
        sfor<0, 9>([&](auto N) INL {
          constexpr int n = N.v;
          f32x16& s = sc[n & 1];
          f32x16& sn = sc[(n + 1) & 1];
          v4u k1; v2u k2, v0, v1, v2, v3;
          if constexpr (n == ANN) {
            if (band < 7) {
              DRAIN();
              gst32(tag, ws, lane == 0 ? OFF_HALOF + (((L * 8 + band) * 32 + j) * 2 + (qt >> 2)) * 256 + (qt & 3) * 4 : WS_USED);
            }
            STAMP2(25);
          }
          {
            const unsigned a = n == 0 ? vd : va[(n - 1) >> 2] + (3 - ((n - 1) & 3)) * KV_T;
            v0 = ldstr(a); v1 = ldstr(a + 384); v2 = ldstr(a + 768); v3 = ldstr(a + 1152);
          }
          if constexpr (n == 8) { CBAR(); lds_st(rg.prog, rg.ls + 9); CBAR(); }
          if constexpr (n == 4 || n == 8) {
            if (grp == (n == 4 ? 0 : 1)) {
#pragma unroll
              for (int r = 0; r < 16; ++r) s[r] = ((r & 3) + 8 * (r >> 2)) > ql ? s[r] : NEG;
            }
          }
          v4u p, p2;
          p[0] = pk(ex2(s[0]), ex2(s[1])); p[1] = pk(ex2(s[2]), ex2(s[3]));
          if constexpr (n < 8) {
            lds_wait_bits(L_RDY + L * 4, 1u << ((n < 4 ? top0 : top1) - 1 - (n & 3)), rdy, rg.bud);
            k1 = lds128(ka[n >> 2] + (3 - (n & 3)) * KV_T); k2 = lds64(kb[n >> 2] + (3 - (n & 3)) * KV_T);
          }
          p[2] = pk(ex2(s[4]), ex2(s[5])); p[3] = pk(ex2(s[6]), ex2(s[7]));
          p2[0] = pk(ex2(s[8]), ex2(s[9]));
          SCHED();
          if constexpr (n < 8) sn = mfma32(k1, q1, negm);
          p2[1] = pk(ex2(s[10]), ex2(s[11]));
          float e12 = ex2(s[12]);
          SCHED();
          if constexpr (n < 8) sn = mfma32k8(k2, q2, sn);
          p2[2] = pk(e12, ex2(s[13])); p2[3] = pk(ex2(s[14]), ex2(s[15]));
          const unsigned any = ((p[0] | p[1] | p[2]) | (p[3] | p2[0] | p2[1]) | (p2[2] | p2[3])) & 0x40004000u;
          if (__builtin_expect(__any(any != 0u), 0)) {
            const float d = fmaxf(max_halves(max16(s)) + MB, 0.f);
            const float f = ex2(-d);
#pragma unroll
            for (int r = 0; r < 16; ++r) { s[r] -= d; negm[r] -= d; o[r] *= f; if (n < 8) sn[r] -= d; }
#pragma unroll
            for (int i = 0; i < 4; ++i) { p[i] = pk(ex2(s[2 * i]), ex2(s[2 * i + 1])); p2[i] = pk(ex2(s[8 + 2 * i]), ex2(s[9 + 2 * i])); }
          }
          o = mfma32(v4u{v0[0], v0[1], v1[0], v1[1]}, p, o);
          o = mfma32(v4u{v2[0], v2[1], v3[0], v3[1]}, p2, o);
          SCHED();
          if constexpr (n == 3) STAMP2(26);
          if constexpr (n == 5) STAMP2(27);
          if constexpr (n == 7) STAMP2(28);
        });
        const float inv = __builtin_amdgcn_rcpf(o[12]);
        v4u o1;
#pragma unroll
        for (int i = 0; i < 4; ++i) o1[i] = pk(o[2 * i] * inv, o[2 * i + 1] * inv);
        const v2u o2 = {pk(o[8] * inv, o[9] * inv), pk(o[10] * inv, o[11] * inv)};
        const unsigned ob = OFF_OA + L * OA_SZ + (2 * t32i) * OA_T + (5 * (j >> 1)) * 256;
        const unsigned tv = ((lane >> 4) & 1) * OA_T + (lane & 15) * 16;
        gst128(o1, ws, tv + h * 256, ob + (3 * (j & 1)) * 256);
        gst64(o2, ws, tv + (h ? WS_USED : 0u), ob + 2 * 256 + (j & 1) * 8);
        STAMP(4 + L * 12);
        if (L == 0) lds_wait_gt(L_W2OK, 0u, rg.bud);
        ctr_arrive(wsp, 4 * L, t32i);
        if (L == 0) {
          const int g4 = lane >> 4;
          const unsigned ro = ((t16i & 1) * 16 + (lane & 15)) * 16 + (g4 & 1) * 8, rb = OFF_XA + (t16i >> 1) * XA_T + (5 * ct) * 512;
          const v2u r0 = gld64(ws, ro + (g4 >> 1) * 512, rb), r1 = gld64(ws, ro + (g4 >> 1) * 512 + 1024, rb), r2 = gld64(ws, g4 < 2 ? ro + 2048 : WS_USED, rb);
          res[0] = r0[0]; res[1] = r0[1]; res[2] = r1[0]; res[3] = r1[1]; res[4] = r2[0]; res[5] = r2[1];
        }
      }
      STAMP(5 + L * 12);
      PP((PHPRIO >> 4) & 3);
      gemm16<20, WO_, 3, 8, 9, TabO>(rg, ws, OFF_OA + L * OA_SZ + t16i * OA_T, res, OFF_XA + (2 * L + 1) * XA_SZ + (t16i >> 1) * XA_T,
                                     OFF_ST + (2 * L + 1) * ST_SZ + (t16i >> 1) * 4096, false, out, t16i, ct,
                                     [&]() INL { ctr_wait(wsp, 4 * L, t16i >> 1, rep, rg.bud); STAMP(6 + L * 12); });
      STAMP(7 + L * 12);
      ctr_arrive(wsp, 4 * L + 1, t16i >> 1);
      PP((PHPRIO >> 6) & 3);
      {
        f32x16 acc[3]; float rs;
        gemm32<3, WG_, 2, 13, 14, TabG, L_CG>(acc, rs, rg, ws, OFF_XA + (2 * L + 1) * XA_SZ + t32i * XA_T, OFF_ST + (2 * L + 1) * ST_SZ + t32i * 4096,
                                              [&]() INL { ctr_wait(wsp, 4 * L + 1, t32i, rep, rg.bud); STAMP(8 + L * 12); STAMP1(24); });
        STAMP1(25); STAMP1(26);
        const int lane = fresh_lane(), h = lane >> 5;
        const float r2 = rs * rs;
        const unsigned hb = OFF_HA + L * HA_SZ + (2 * t32i) * HA_T + (6 * j) * 256;
        const unsigned tv = ((lane >> 4) & 1) * HA_T + (lane & 15) * 16 + h * 256;
        v4u hv;
#pragma unroll
        for (int i = 0; i < 4; ++i) hv[i] = pk(acc[0][2 * i] * r2 * acc[1][2 * i], acc[0][2 * i + 1] * r2 * acc[1][2 * i + 1]);
        gst128(hv, ws, tv, hb);
#pragma unroll
        for (int i = 0; i < 4; ++i) hv[i] = pk(acc[0][8 + 2 * i] * r2 * acc[1][8 + 2 * i], acc[0][9 + 2 * i] * r2 * acc[1][9 + 2 * i]);
        gst128(hv, ws, tv + 512, hb);
#pragma unroll
        for (int i = 0; i < 4; ++i) hv[i] = pk(acc[2][2 * i] * r2 * acc[2][8 + 2 * i], acc[2][2 * i + 1] * r2 * acc[2][9 + 2 * i]);
        gst128(hv, ws, tv + 1024, hb);
      }
      STAMP(9 + L * 12);
      ctr_arrive(wsp, 4 * L + 2, t32i);
      PP((PHPRIO >> 8) & 3);
      gemm16<48, WD_, 3, 25, 26, TabD>(rg, ws, OFF_HA + L * HA_SZ + t16i * HA_T, res, OFF_XA + 2 * XA_SZ + (t16i >> 1) * XA_T,
                                       OFF_ST + 2 * ST_SZ + (t16i >> 1) * 4096, L == 1, out, t16i, ct,
                                       [&]() INL { ctr_wait(wsp, 4 * L + 2, t16i >> 1, rep, rg.bud); STAMP(10 + L * 12); });
      STAMP(11 + L * 12);
      if (L == 0) ctr_arrive(wsp, 3, t16i >> 1);
    }
  } else {
    const int lane = fresh_lane();
    const int half = 2 - wave;
    __builtin_amdgcn_s_setprio(3);
    unsigned bud = 0;
    unsigned tag = 0;
    if (wave == 3) { lds_wait_gt(L_TAGV, 0u, bud); tag = ufl(lds_ld(L_TAG)); }
    if (wave < 3) {
      const int hx = wave - 1;
      {
        const unsigned xa = OFF_XA + (band * 8 + (j >> 2)) * XA_T + ((j & 3) * 8 + (lane & 7)) * 16 + (hx * 40 + (lane >> 3)) * 512;
        unsigned la = STG_X + hx * 5120 + (lane & 7) * 128 + (lane >> 3) * 16;
        unsigned sa = OFF_ST + (band * 8 + (j >> 2)) * 4096 + st_line(hx * 8 + (lane >> 3), (j >> 1) & 1) + (lane & 7) * 16 + (j & 1) * 8;
        unsigned fa = lane < 32 ? OFF_IN + (band * 32 + lane) * 512 + 256 + (j * 2 + hx) * 4 : OOR;
#pragma unroll 1
        for (int pass = DRYPASS ? 0 : 1; pass < 2; ++pass) {
          const unsigned off = pass ? 0u : OOR;
          if (pass) DRAIN();
          v4u xv[5];
#pragma unroll
          for (int i = 0; i < 5; ++i) xv[i] = lds128(la + i * 1024);
#pragma unroll
          for (int i = 0; i < 5; ++i) gst128(xv[i], ws, xa + off + i * 4096, 0);
          float s = 0.f, qq = 0.f;
#pragma unroll
          for (int i = 0; i < 5; ++i) {
            s = sum8(xv[i], s);
#pragma unroll
            for (int k = 0; k < 4; ++k) { const unsigned u = xv[i][k]; qq = dot2(u, u, qq); }
          }
          gst64(v2u{asu(s), asu(qq)}, ws, sa + off, 0);
          if (pass) lds_wait_gt(L_TAGV, 0u, bud);
          tag = ufl(lds_ld(L_TAG));
          if (pass) DRAIN();
          gst32(tag, ws, fa + off);
        }
      }
      lds_add1(L_CRIT);
    } else {
      for (;;) {
        const unsigned m = (unsigned)__ballot(rmw_poll_pairs((GAS unsigned*)(wsp + OFF_IN + (band * 32 + j) * 512 + 256), lane * 8, 0xffffffffu, tag));
        lds_st(L_XRDY, m);
        if (m == 0xffffffffu) break;
        SPEND(bud);
      }
      lds_wait_gt(L_WQOK, 0u, bud);
      v4u pc[10];
      sfor<0, 10>([&](auto I) INL { pc[I.v] = lda<I.v>(ws, lane * 16, OFF_CSQ + j * 10240); });
      f32x4 c = __builtin_bit_cast(f32x4, pc[0]);
#pragma unroll
      for (int i = 1; i < 10; ++i) c += __builtin_bit_cast(f32x4, pc[i]);
#pragma unroll
      for (int i = 0; i < 4; ++i) c[i] = sum_halves(sum_rows16(c[i]));
      if (lane < 16) csum_rows(c, L_CQ + lane * 32);
      CBAR();
      lds_st(L_CSUM, 1u);
    }
    __builtin_amdgcn_s_setprio(PRIO_H);
    if (wave == 3) {
      lds_wait_gt(L_PROG, F2GATE, bud);
      for (;;) {
        const v4u f0 = gld128(ws, lane * 16, OFF_F2);
        const bool ok = f0[0] == tag && f0[1] == tag && f0[2] == tag && f0[3] == tag;
        if (__all(ok)) break;
        SPEND(bud);
        __builtin_amdgcn_s_sleep(16);
        CBAR();
      }
      CBAR();
      lds_st(L_W2OK, 1u);
      if (blockIdx.x == 0) gst32(tag, ws, lane == 0 ? OFF_EP : WS_USED);
      f32x4 c = {0.f, 0.f, 0.f, 0.f};
#pragma unroll 1
      for (int r = 0; r < 2; ++r) {
        v4u pc[10];
        sfor<0, 10>([&](auto I) INL { pc[I.v] = lda<I.v>(ws, lane * 16, OFF_CSG + j * 20480 + r * 10240); });
#pragma unroll
        for (int i = 0; i < 10; ++i) c += __builtin_bit_cast(f32x4, pc[i]);
      }
#pragma unroll
      for (int i = 0; i < 4; ++i) c[i] = sum_halves(c[i]);
      if (lane < 24) csum_rows(c, L_CG + lane * 32);
      CBAR();
      lds_st(L_CSUM, 2u);
      return;
    }
    if (band == 0) {
#pragma unroll 1
      for (int L = 0; L < 2; ++L) {
        if (L > 0) prog_wait(L * NSTAGE, bud); else lds_wait_gt(L_STG, 7u, bud);
        const v4u z = {0u, 0u, 0u, 0u};
#pragma unroll 1
        for (int sl = 0; sl < 4; ++sl) kv_write(KV_OFF + (8 * half + sl) * KV_T, lane, z, v4u{lane < 32 ? 0xf149u : 0u, 0u, 0u, 0u}, z);
        CBAR();
        lds_or1(L_RDY + L * 4, 0xfu << (8 * half));
      }
    } else {
#pragma unroll 1
      for (int L = 0; L < 2; ++L) {
        lds_wait_gt(L_PROG + (1 - half) * 16, L * NSTAGE + 5, bud);
        if (L > 0) prog_wait(L * NSTAGE, bud); else lds_wait_gt(L_STG, 7u, bud);
        unsigned pend = 0xfu;
        GAS unsigned* fl = (GAS unsigned*)(wsp + OFF_HALOF + (((L * 8 + band - 1) * 32 + j) * 2 + half) * 256);
        const unsigned gb = OFF_KVH + L * KVH_SZ + (((band - 1) * 32 + j) * 8 + 4 * half) * KVH_T;
        while (pend) {
          const unsigned nw = (unsigned)__ballot(rmw_poll_lanes(fl, lane * 4, 0xfu, 0u, 0u) == tag) & pend;
          __builtin_amdgcn_fence(__ATOMIC_ACQUIRE, "wavefront");
          CBAR();
          if (!nw) {
            SPEND(bud);
            continue;
          }
          pend &= ~nw;
          v4u x[4][3];
#pragma unroll
          for (int sl = 3; sl >= 0; --sl) {
            const unsigned vo = lane * 16 + ((nw >> sl & 1) ? 0u : WS_USED);
#pragma unroll
            for (int i = 0; i < 3; ++i) x[sl][i] = gld128(ws, vo + i * 1024, gb + sl * KVH_T);
          }
#pragma unroll
          for (int sl = 3; sl >= 0; --sl)
            if (nw >> sl & 1) {
              kv_write(KV_OFF + (8 * half + sl) * KV_T, lane, x[sl][0], x[sl][1], x[sl][2]);
              CBAR();
              lds_or1(L_RDY + L * 4, 1u << (8 * half + sl));
            }
        }
      }
    }
  }
}

extern "C" void kernel_launch(void* const* d_in, const int* in_sizes, int n_in, void* d_out, int out_size, void* d_ws, size_t ws_size,
                              hipStream_t stream) {
  typedef const unsigned char* cp;
  mega_kernel<<<256, 768, 0, stream>>>((unsigned char*)d_ws, d_out, (cp)d_in[0], (cp)d_in[1], (cp)d_in[2], (cp)d_in[4], (cp)d_in[5], (cp)d_in[6]);
}
```

```cpp
#include <hip/hip_runtime.h>
#include <hip/hip_bf16.h>
#include <stdint.h>
#pragma clang diagnostic ignored "-Wint-to-pointer-cast"

typedef unsigned v4u __attribute__((ext_vector_type(4)));
typedef unsigned v2u __attribute__((ext_vector_type(2)));
typedef short s16x8 __attribute__((ext_vector_type(8)));
typedef short s16x4 __attribute__((ext_vector_type(4)));
typedef float f32x16 __attribute__((ext_vector_type(16)));
typedef float f32x4 __attribute__((ext_vector_type(4)));
typedef float f32x2 __attribute__((ext_vector_type(2)));
typedef __bf16 bf2 __attribute__((ext_vector_type(2)));
typedef unsigned short u16;
#define LAS __attribute__((address_space(3)))
#define GAS __attribute__((address_space(1)))
#define DI __device__ __forceinline__
#define INL __attribute__((always_inline))
#define RLX_AGENT __ATOMIC_RELAXED, __HIP_MEMORY_SCOPE_AGENT
#define RLX_WG __ATOMIC_RELAXED, __HIP_MEMORY_SCOPE_WORKGROUP
#define CBAR() asm volatile("" ::: "memory")
#define SCHED() __builtin_amdgcn_sched_barrier(0)

constexpr unsigned OFF_CTR = 0;
constexpr unsigned OFF_HALOF = 7 * 64 * 1024;
constexpr unsigned OFF_IN = OFF_HALOF + 2 * 8 * 32 * 512;
constexpr unsigned OFF_F2 = OFF_IN + 256 * 512;
constexpr unsigned OFF_EP = OFF_F2 + 1024;
constexpr unsigned OFF_STAMP = 64u << 20;
constexpr unsigned OFF_WQ = 1u << 20;
constexpr unsigned OFF_WO = OFF_WQ + 32 * 81920;
constexpr unsigned OFF_WG = OFF_WO + 16 * 51200;
constexpr unsigned OFF_WD = OFF_WG + 32 * 122880;
constexpr unsigned XA_T = 40960, XA_SZ = 64 * XA_T;
constexpr unsigned OFF_XA = OFF_WD + 16 * 122880;
constexpr unsigned ST_SZ = 64 * 4096;
constexpr unsigned OFF_ST = OFF_XA + 4 * XA_SZ;
constexpr unsigned OA_T = 20480, OA_SZ = 128 * OA_T;
constexpr unsigned OFF_OA = OFF_ST + 4 * ST_SZ;
constexpr unsigned HA_T = 49152, HA_SZ = 128 * HA_T;
constexpr unsigned OFF_HA = OFF_OA + 2 * OA_SZ;
constexpr unsigned KVH_T = 3072, KVH_SZ = 8 * 32 * 8 * KVH_T;
constexpr unsigned OFF_KVH = OFF_HA + 2 * HA_SZ;
constexpr unsigned OFF_CSQ = OFF_KVH + 2 * KVH_SZ;
constexpr unsigned OFF_CSG = OFF_CSQ + 32 * 40 * 256;
constexpr unsigned WS_USED = OFF_CSG + 32 * 40 * 512;
static_assert(OFF_EP + 256 <= OFF_WQ, "");

constexpr int UNIT = 10240;
constexpr int MISC = 0, ARENA = 2048;
constexpr int KV_OFF = ARENA + 8 * UNIT, KV_T = 3072;
constexpr int L_PROG = MISC, L_LANDED = MISC + 64, L_RDY = MISC + 68, L_CSUM = MISC + 76;
constexpr int L_TAG = MISC + 80, L_TAGV = MISC + 84, L_XRDY = MISC + 88, L_STG = MISC + 92, L_WQOK = MISC + 96, L_W2OK = MISC + 100, L_CRIT = MISC + 104;
constexpr int STG = ARENA + 8 * UNIT;
constexpr int STG_X = STG + 61440;
constexpr int L_CQ = MISC + 256, L_CG = MISC + 768, L_STAMP = ARENA + 15 * UNIT;
constexpr int LDS_BYTES = ARENA + 15 * UNIT + 4096;
#define LDEPTH 2
#define WQ_ 12
#define WG_ 10
#define WO_ 10
#define WD_ 10
#define PRIO_H 0
#define PHPRIO 438
#define PP(p) do { if (PHPRIO) __builtin_amdgcn_s_setprio(p); } while (0)
#define PRETOUCH 1
#define L2GATE 0
#define DRYPASS 0
#define HINT 0
#define S2GATE 4
#define F2GATE 3u
#define ANN 0
#define TURN0 3
constexpr int NFILL = 37, NSTAGE = 38;
constexpr unsigned SPIN_CAP = 1u << 18;

struct TabQ { static constexpr int u(int i) { return i; } };
struct TabO { static constexpr int u(int i) { return i + 1; } };
struct TabG { static constexpr int u(int i) { constexpr int t[12] = {13, 14, 0, 6, 7, 8, 9, 10, 11, 12, 1, 2}; return t[i]; } };
struct TabD { static constexpr int u(int i) { constexpr int t[12] = {3, 4, 5, 13, 14, 6, 7, 8, 9, 10, 11, 12}; return t[i]; } };

template <int V> struct IC { static constexpr int v = V; };
template <int I, int N, class F> DI void sfor(F&& f) { if constexpr (I < N) { f(IC<I>{}); sfor<I + 1, N>(f); } }
DI int fresh_lane() { int l; asm volatile("v_mbcnt_lo_u32_b32 %0, -1, 0\n\tv_mbcnt_hi_u32_b32 %0, -1, %0" : "=v"(l)); return l; }
DI int opq(int v) { asm volatile("" : "+s"(v)); return v; }
DI float asf(unsigned u) { return __uint_as_float(u); }
DI unsigned asu(float f) { return __float_as_uint(f); }
DI unsigned pk(float a, float b) { f32x2 t = {a, b}; bf2 p = __builtin_convertvector(t, bf2); return __builtin_bit_cast(unsigned, p); }
DI float rbf(float a) { return asf(pk(a, a) << 16); }
DI float ex2(float a) { return __builtin_amdgcn_exp2f(a); }
DI float dot2(unsigned a, unsigned b, float c) { return __builtin_amdgcn_fdot2_f32_bf16(__builtin_bit_cast(bf2, a), __builtin_bit_cast(bf2, b), c, false); }
DI f32x16 mfma32(v4u a, v4u b, f32x16 c) { return __builtin_amdgcn_mfma_f32_32x32x16_bf16(__builtin_bit_cast(s16x8, a), __builtin_bit_cast(s16x8, b), c, 0, 0, 0); }
DI f32x16 mfma32k8(v2u a, v2u b, f32x16 c) { return __builtin_amdgcn_mfma_f32_32x32x8bf16_1k(__builtin_bit_cast(s16x4, a), __builtin_bit_cast(s16x4, b), c, 0, 0, 0); }
DI f32x4 mfma16(v4u a, v4u b, f32x4 c) { return __builtin_amdgcn_mfma_f32_16x16x32_bf16(__builtin_bit_cast(s16x8, a), __builtin_bit_cast(s16x8, b), c, 0, 0, 0); }
DI void mfma16i(f32x4& c, v4u a, v4u b) { asm volatile("v_mfma_f32_16x16x32_bf16 %0, %1, %2, %0" : "+v"(c) : "v"(a), "v"(b)); }
DI void mfma_fence(f32x4& a, f32x4& b, f32x4& c) { asm volatile("s_nop 7" : "+v"(a), "+v"(b), "+v"(c)); }
DI unsigned ufl(unsigned v) { return __builtin_amdgcn_readfirstlane(v); }
DI unsigned lds_ld(unsigned a) { return __hip_atomic_load((LAS unsigned*)a, RLX_WG); }
DI void lds_st(unsigned a, unsigned v) { __hip_atomic_store((LAS unsigned*)a, v, RLX_WG); }
DI void lds_or1(unsigned a, unsigned v) { asm volatile("s_mov_b64 exec, 1\n\tds_or_b32 %0, %1\n\ts_mov_b64 exec, -1" :: "v"(a), "v"(v) : "memory"); }
DI v4u lds128(unsigned a) { return *(LAS v4u*)a; }
DI v2u lds64(unsigned a) { return *(LAS v2u*)a; }
DI v2u ldstr(unsigned a) { return __builtin_bit_cast(v2u, __builtin_amdgcn_ds_read_tr16_b64_v4i16((LAS s16x4*)a)); }
DI float sum_halves(float x) { auto r = __builtin_amdgcn_permlane32_swap(asu(x), asu(x), false, false); unsigned a = r[0], b = r[1]; return asf(a) + asf(b); }
DI float max_halves(float x) { auto r = __builtin_amdgcn_permlane32_swap(asu(x), asu(x), false, false); unsigned a = r[0], b = r[1]; return fmaxf(asf(a), asf(b)); }
DI float sum_rows16(float x) { auto r = __builtin_amdgcn_permlane16_swap(asu(x), asu(x), false, false); unsigned a = r[0], b = r[1]; return asf(a) + asf(b); }
DI float ror8(float x) { return asf(__builtin_amdgcn_update_dpp(0u, asu(x), 0x128, 0xf, 0xf, false)); }
DI unsigned st_line(int c, int hf) { return ((c >> 2) * 8 + ((c >> 1) & 1) * 4 + hf * 2 + (c & 1)) * 128; }
DI float max3(float a, float b, float c) { return __builtin_elementwise_maximum(__builtin_elementwise_maximum(a, b), c); }

typedef __amdgpu_buffer_rsrc_t rsrc_t;
constexpr int SC1 = 16;
DI v4u gld128(rsrc_t r, unsigned v, unsigned s) { return __builtin_amdgcn_raw_buffer_load_b128(r, v, s, SC1); }
DI v2u gld64(rsrc_t r, unsigned v, unsigned s) { return __builtin_amdgcn_raw_buffer_load_b64(r, v, s, SC1); }
DI void gst128(v4u x, rsrc_t r, unsigned v, unsigned s) { __builtin_amdgcn_raw_buffer_store_b128(x, r, v + s, 0, SC1); }
DI void gst64(v2u x, rsrc_t r, unsigned v, unsigned s) { __builtin_amdgcn_raw_buffer_store_b64(x, r, v, s, SC1); }
template <int S> DI v4u lda(rsrc_t r, unsigned voff, unsigned base) { return gld128(r, voff + (S & 3) * 1024, base + (S >> 2) * 4096); }

#define STAMP_HI (SPIN_CAP - rg.bud)
#define STAMP(i) do {} while (0)
#define INNER 1
#define STAMP1(i) do { if (INNER == 1) STAMP(i); } while (0)
#define STAMP2(i) do { if (INNER == 2) STAMP(i); } while (0)
#define STAMP4(i) do { if (INNER == 4) STAMP(i); } while (0)
#define STAMP3(i) do { if (INNER == 3 && rg.ls == 0) STAMP(i); } while (0)

#define SPEND(bud) { if ((bud) >= SPIN_CAP) break; ++(bud); }
DI unsigned lds_wait_gt(unsigned a, unsigned f, unsigned& bud) {
  unsigned v;
  for (;;) {
    v = lds_ld(a);
    if (ufl(v) > f) break;
    SPEND(bud);
    __builtin_amdgcn_s_sleep(1);
  }
  CBAR();
  return v;
}
DI void lds_wait_bits(unsigned a, unsigned bits, unsigned& cache, unsigned& bud) {
  if (__builtin_expect((cache & bits) == bits, 1)) return;
  for (;;) {
    cache = ufl(lds_ld(a));
    if ((cache & bits) == bits) break;
    SPEND(bud);
    __builtin_amdgcn_s_sleep(1);
  }
  CBAR();
}
DI void prog_wait(unsigned need, unsigned& bud) {
  const int lane = fresh_lane();
  for (;;) {
    unsigned v = lds_ld(L_PROG + (lane & 7) * 4);
    if (__all(v >= need)) break;
    SPEND(bud);
    __builtin_amdgcn_s_sleep(1);
  }
  CBAR();
}
DI unsigned rmw_poll(GAS unsigned* p) {
  unsigned f;
  asm volatile("s_mov_b64 exec, 1\n\ts_nop 3\n\tglobal_atomic_or %0, %1, %1, %2 sc0\n\ts_waitcnt vmcnt(0)\n\ts_mov_b64 exec, -1" : "=&v"(f) : "v"(0u), "s"(p) : "memory");
  return ufl(f);
}
DI unsigned rmw_poll_lanes(GAS unsigned* p, unsigned voff, unsigned mlo, unsigned mhi, unsigned init) {
  asm volatile("s_mov_b32 exec_lo, %3\n\ts_mov_b32 exec_hi, %4\n\ts_nop 3\n\tglobal_atomic_or %0, %1, %5, %2 sc0\n\ts_waitcnt vmcnt(0)\n\ts_mov_b64 exec, -1"
               : "+v"(init) : "v"(voff), "s"(p), "s"(mlo), "s"(mhi), "v"(0u) : "memory");
  return init;
}
DI bool rmw_poll_pairs(GAS unsigned* p, unsigned voff, unsigned mlo, unsigned tag) {
  v2u f = {0u, 0u};
  asm volatile("s_mov_b32 exec_lo, %3\n\ts_mov_b32 exec_hi, 0\n\ts_nop 3\n\tglobal_atomic_or_x2 %0, %1, %4, %2 sc0\n\ts_waitcnt vmcnt(0)\n\ts_mov_b64 exec, -1"
               : "+v"(f) : "v"(voff), "s"(p), "s"(mlo), "v"(v2u{0u, 0u}) : "memory");
  return f[0] == tag && f[1] == tag;
}
DI void gst32(unsigned x, rsrc_t r, unsigned v) { __builtin_amdgcn_raw_buffer_store_b32(x, r, v, 0, SC1); }
#define DRAIN() asm volatile("s_waitcnt vmcnt(0)" ::: "memory")
#define WG_BARRIER() do { asm volatile("s_waitcnt lgkmcnt(0)" ::: "memory"); __builtin_amdgcn_s_barrier(); CBAR(); } while (0)
DI void ctr_wait(unsigned char* ws, int kind, int t32, int rep, unsigned& bud) {
  GAS unsigned* p = (GAS unsigned*)(ws + OFF_CTR + (kind * 64 + t32) * 1024 + rep * 256);
  for (;;) {
    if (rmw_poll(p) >= 32u) break;
    SPEND(bud);
  }
  __builtin_amdgcn_fence(__ATOMIC_ACQUIRE, "wavefront");
  CBAR();
}
DI void ctr_arrive(unsigned char* ws, int kind, int t32) {
  const int lane = fresh_lane();
  GAS unsigned* p = (GAS unsigned*)(ws + OFF_CTR + (kind * 64 + t32) * 1024);
  if (!HINT) DRAIN();
  asm volatile("s_mov_b64 exec, 15\n\ts_nop 3\n\tglobal_atomic_add %0, %1, %2\n\ts_mov_b64 exec, -1" :: "v"(lane * 256), "v"(1u), "s"(p) : "memory");
}

DI void dma_fill(v4u srd, unsigned voff, unsigned soff, unsigned ldst) {
  unsigned keep, so;
  asm volatile(
      "s_mov_b32 %0, m0\n\ts_mov_b32 m0, %5\n\ts_add_u32 %1, %4, 0x1000\n\t"
      "buffer_load_dwordx4 %2, %3, %4 offen sc1 lds\n\tbuffer_load_dwordx4 %2, %3, %4 offen offset:1024 sc1 lds\n\t"
      "buffer_load_dwordx4 %2, %3, %4 offen offset:2048 sc1 lds\n\tbuffer_load_dwordx4 %2, %3, %4 offen offset:3072 sc1 lds\n\t"
      "s_add_u32 m0, m0, 0x1000\n\ts_nop 0\n\t"
      "buffer_load_dwordx4 %2, %3, %1 offen sc1 lds\n\tbuffer_load_dwordx4 %2, %3, %1 offen offset:1024 sc1 lds\n\t"
      "buffer_load_dwordx4 %2, %3, %1 offen offset:2048 sc1 lds\n\tbuffer_load_dwordx4 %2, %3, %1 offen offset:3072 sc1 lds\n\t"
      "s_add_u32 m0, m0, 0x1000\n\ts_add_u32 %1, %1, 0x1000\n\t"
      "buffer_load_dwordx4 %2, %3, %1 offen sc1 lds\n\tbuffer_load_dwordx4 %2, %3, %1 offen offset:1024 sc1 lds\n\t"
      "s_mov_b32 m0, %0"
      : "=&s"(keep), "=&s"(so) : "v"(voff), "s"(srd), "s"(soff), "s"(ldst) : "memory", "scc");
}

DI void dma_raw(const unsigned char* base, unsigned bytes, unsigned voff, unsigned soff, unsigned sstep, unsigned ldst, int n) {
  const v4u srd = {(unsigned)(uintptr_t)base, (unsigned)((uintptr_t)base >> 32) & 0xffffu, bytes, 0x00020000u};
#pragma unroll 1
  for (int i = 0; i < n; ++i) {
    unsigned keep;
    asm volatile("s_mov_b32 %0, m0\n\ts_mov_b32 m0, %4\n\ts_nop 0\n\tbuffer_load_dwordx4 %1, %2, %3 offen lds\n\ts_mov_b32 m0, %0"
                 : "=&s"(keep) : "v"(voff), "s"(srd), "s"(soff + i * sstep), "s"(ldst + i * 1024) : "memory");
  }
}
DI unsigned lds_inc1(unsigned a) {
  unsigned r;
  asm volatile("s_mov_b64 exec, 1\n\tds_add_rtn_u32 %0, %1, %2\n\ts_waitcnt lgkmcnt(0)\n\ts_mov_b64 exec, -1" : "=&v"(r) : "v"(a), "v"(1u) : "memory");
  return ufl(r);
}
DI void lds_add1(unsigned a) { asm volatile("s_mov_b64 exec, 1\n\tds_add_u32 %0, %1\n\ts_mov_b64 exec, -1" :: "v"(a), "v"(1u) : "memory"); }

constexpr unsigned SENT = 0xffffffffu;
typedef unsigned long long u64;
DI u64 stale2(v4u x, v4u y) {
  const unsigned a = x[0], b = x[2], c = y[0], d = y[2];
  u64 m = __ballot(max(max(max(a, b), c), d) == SENT);
  asm volatile("" : "+s"(m));
  return m;
}
template <int F> DI void fresh2(u64 bad, v4u& x, v4u& y, __amdgpu_buffer_rsrc_t ws, unsigned voff, unsigned base, unsigned& bud) {
  if (__builtin_expect(bad != 0, 0)) {
    for (;;) {
      CBAR();
      x = lda<F>(ws, voff, base); y = lda<F + 1>(ws, voff, base);
      if (!stale2(x, y)) break;
      SPEND(bud);
    }
  }
}

struct Ring {
  unsigned lv, lvs;
  unsigned lf, ls;
  unsigned prog;
  unsigned bud;
};
DI void ring_check(Ring& r, int f) {
  if (__builtin_expect(r.lvs <= r.lf + f, 0)) lds_wait_gt(L_LANDED, r.lf + f, r.bud);
  CBAR();
}
DI void ring_latch(Ring& r) { r.lvs = opq(ufl(r.lv)); }
DI void ring_peek(Ring& r) { r.lv = lds_ld(L_LANDED); }
constexpr int BSTEP = 61440;

template <int NB, int W, int DIST, int F0, int S0, class Tab, int CS, class Wait>
DI void gemm32(f32x16 (&acc)[NB], float& rs, Ring& rg, rsrc_t ws, unsigned abase, unsigned stbase, Wait&& wait) {
  const int lane = fresh_lane();
  const unsigned voff = lane * 16;
  v4u a[W], b[DIST + 1][NB];
  unsigned lb[3];
#pragma unroll
  for (int i = 0; i < 3; ++i) { lb[i] = voff + i * BSTEP; asm volatile("" : "+v"(lb[i])); }
  unsigned pa = rg.prog, pv = rg.ls + S0;
  asm volatile("" : "+v"(pa), "+v"(pv));
  v2u cf[NB], xm;
  v4u st[4];
  float s_ = 0.f, q_ = 0.f, s1_ = 0.f, q1_ = 0.f;
  unsigned csv = 0;
  u64 bs0 = 0, bs1 = 0;
  auto readB = [&](auto P) INL {
    constexpr int ph = P.v;
    if constexpr (ph < 40 * NB) {
      constexpr int ad = ARENA + Tab::u(ph / 10) * UNIT + (ph % 10) * 1024;
      b[(ph / NB) % (DIST + 1)][ph % NB] = lds128(lb[ad / BSTEP] + ad % BSTEP);
    }
    if constexpr (ph % 10 == 3 && ph + 7 < 40 * NB) ring_peek(rg);
    if constexpr (ph % 10 == 7 && ph + 3 < 40 * NB) ring_latch(rg);
    if constexpr (ph % 10 == 9 && ph + 1 < 40 * NB) ring_check(rg, F0 + ph / 10 + 1);
    if constexpr (ph % 10 == 0 && ph > 0 && ph <= 40 * NB) { CBAR(); ++pv; lds_st(pa, pv); CBAR(); }
  };
  wait();
  sfor<0, W>([&](auto S) INL { a[S.v] = lda<S.v>(ws, voff, abase); });
  SCHED();
  ring_peek(rg);
  ring_latch(rg);
  ring_check(rg, F0);
  sfor<0, DIST * NB>(readB);
  u64 bad = 0;
  if (HINT == 1) fresh2<0>(stale2(a[0], a[1]), a[0], a[1], ws, voff, abase, rg.bud);
  SCHED();
  constexpr int X1 = 40 * NB - 14;
  constexpr int X0 = X1 - 28;
  sfor<0, 40 * NB>([&](auto SL) INL {
    constexpr int sl = SL.v, s = sl / NB, t = sl % NB;
    if constexpr (s == 0) { f32x16 z; _Pragma("unroll") for (int r = 0; r < 16; ++r) z[r] = 0.f; acc[t] = mfma32(b[0][t], a[0], z); }
    else acc[t] = mfma32(b[s % (DIST + 1)][t], a[s % W], acc[t]);
    SCHED();
    readB(IC<sl + DIST * NB>{});
    if constexpr (t == NB - 1 && s + W < 40) a[s % W] = lda<s + W>(ws, voff, abase);
    if constexpr (HINT == 1 && t == NB - 1 && (s & 1) && s + 2 < 40) fresh2<s + 1>(bad, a[(s + 1) % W], a[(s + 2) % W], ws, voff, abase, rg.bud);
    if constexpr (HINT == 1 && t == NB - 2 && (s & 1) && s + 2 < 40) bad = stale2(a[(s + 1) % W], a[(s + 2) % W]);
    if constexpr (HINT == 1 && sl == X1 - 5) bs0 = stale2(st[0], st[1]);
    if constexpr (HINT == 1 && sl == X1 - 4) bs1 = stale2(st[2], st[3]);
    if constexpr (HINT == 1 && sl == X1 - 2) fresh2<0>(bs0, st[0], st[1], ws, voff, stbase, rg.bud);
    if constexpr (HINT == 1 && sl == X1 - 1) fresh2<2>(bs1, st[2], st[3], ws, voff, stbase, rg.bud);
    if constexpr (sl >= X0 && sl < X0 + 8 && (sl - X0) % 2 == 0) st[(sl - X0) / 2] = lda<(sl - X0) / 2>(ws, voff, stbase);
    if constexpr (sl == X1 - 10) csv = lds_ld(L_CSUM);
    if constexpr (sl == X1 - 6) csv = opq(ufl(csv));
    if constexpr (sl == X1 - 3) { if (__builtin_expect(ufl(csv) < NB - 1, 0)) lds_wait_gt(L_CSUM, NB - 2, rg.bud); CBAR(); }
    if constexpr (sl >= X1 && sl < X1 + 3) {
      constexpr int i = sl - X1 + 1;
      const unsigned u0 = st[i][0], u1 = st[i][1], u2 = st[i][2], u3 = st[i][3];
      if constexpr (i == 1) { const unsigned w0 = st[0][0], w1 = st[0][1], w2 = st[0][2], w3 = st[0][3]; s_ = asf(w0); q_ = asf(w1); s1_ = asf(w2); q1_ = asf(w3); }
      s_ += asf(u0); q_ += asf(u1); s1_ += asf(u2); q1_ += asf(u3);
    }
    if constexpr (sl == X1 + 3) { const bool up = lane & 8; s_ = (up ? s1_ : s_) + ror8(up ? s_ : s1_); }
    if constexpr (sl == X1 + 4) { const bool up = lane & 8; q_ = (up ? q1_ : q_) + ror8(up ? q_ : q1_); }
    if constexpr (sl == X1 + 5) s_ = sum_halves(s_) * (1.f / 640.f);
    if constexpr (sl == X1 + 6) q_ = sum_halves(q_) * (1.f / 640.f);
    if constexpr (sl == X1 + 7) {
      q_ = fmaxf(q_ - s_ * s_, 0.f);
      const unsigned nm = pk(-s_, -s_);
      xm[0] = lane < 32 ? nm : 0u; xm[1] = lane < 32 ? (nm & 0xffffu) : 0u;
    }
    if constexpr (sl == X1 + 8) {
      q_ = rbf(rbf(q_) + 1e-5f);
      sfor<0, NB>([&](auto T) INL { cf[T.v] = lds64(CS + T.v * 256 + (lane & 31) * 8); });
    }
    if constexpr (sl == X1 + 9) rs = __builtin_amdgcn_rcpf(rbf(__builtin_amdgcn_sqrtf(q_)));
    SCHED();
  });
  sfor<0, NB>([&](auto T) INL { acc[T.v] = mfma32k8(cf[T.v], xm, acc[T.v]); });
}

template <int NS, int W, int DIST, int F0, int S0, class Tab, class Wait>
DI void gemm16(Ring& rg, rsrc_t ws, unsigned abase, unsigned (&res)[6], unsigned xbase, unsigned stbase, bool last, void* out, int t16i, int ct, Wait&& wait) {
  const int lane = fresh_lane();
  const unsigned voff = lane * 16;
  v4u a[W], b[DIST + 1][3];
  unsigned lb[3], lc[3];
  const int g4 = lane >> 4;
#pragma unroll
  for (int i = 0; i < 3; ++i) {
    lb[i] = voff + i * BSTEP; asm volatile("" : "+v"(lb[i]));
    lc[i] = g4 * 128 + (lane & 7) * 16 + i * BSTEP + ((lane & 8) ? 0x40000 : 0); asm volatile("" : "+v"(lc[i]));
  }
  unsigned pa = rg.prog, pv = rg.ls + S0;
  asm volatile("" : "+v"(pa), "+v"(pv));
  f32x4 acc[3];
#pragma unroll
  for (int t = 0; t < 3; ++t) acc[t] = f32x4{asf(res[2 * t] << 16), asf(res[2 * t] & 0xffff0000u), asf(res[2 * t + 1] << 16), asf(res[2 * t + 1] & 0xffff0000u)};
  auto readB = [&](auto S, auto T) INL {
    constexpr int s = S.v, t = T.v;
    if constexpr (s < NS) {
      constexpr int ad = ARENA + Tab::u(s / 4) * UNIT + (s % 4) * 2560;
      b[s % (DIST + 1)][t] = lds128((t < 2 ? lb : lc)[ad / BSTEP] + ad % BSTEP + t * 1024);
    }
    if constexpr (t == 0 && s % 4 == 1 && s + 3 < NS) ring_peek(rg);
    if constexpr (t == 0 && s % 4 == 3 && s + 1 < NS) ring_latch(rg);
    if constexpr (t == 2 && s % 4 == 3 && s + 1 < NS) ring_check(rg, F0 + s / 4 + 1);
    if constexpr (t == 0 && s % 4 == 0 && s > 0 && s <= NS) { CBAR(); ++pv; lds_st(pa, pv); CBAR(); }
  };
  const unsigned t32 = (t16i & 1) * 16 + (lane & 15);
  const unsigned pi = 2 * (g4 & 1) + (g4 >> 1);
  unsigned so1 = xbase + (5 * ct) * 512 + pi * 512 + t32 * 16, so2 = xbase + (5 * ct + 4) * 512 + t32 * 16 + (lane < 16 ? 0u : WS_USED);
  unsigned so3 = stbase + st_line(ct, t16i & 1) + (lane & 7) * 16 + (lane & 8) + (lane < 16 ? 0u : WS_USED);
  asm volatile("" : "+v"(so1), "+v"(so2), "+v"(so3), "+v"(acc[0]), "+v"(acc[1]), "+v"(acc[2]));
  if (HINT && HINT < 3 && !last) { const v4u sv = {SENT, SENT, SENT, SENT}; gst128(sv, ws, so1, 0); gst128(sv, ws, so2, 0); gst64(v2u{SENT, SENT}, ws, so3, 0); }
  wait();
  sfor<0, W>([&](auto S) INL { a[S.v] = lda<S.v>(ws, voff, abase); });
  SCHED();
  ring_peek(rg);
  ring_latch(rg);
  ring_check(rg, F0);
  sfor<0, DIST * 3>([&](auto I) INL { readB(IC<I.v / 3>{}, IC<I.v % 3>{}); });
  u64 bad = 0;
  if (HINT == 1) fresh2<0>(stale2(a[0], a[1]), a[0], a[1], ws, voff, abase, rg.bud);
  SCHED();
  if constexpr (NS == 48) { STAMP1(27); }
  sfor<0, NS>([&](auto S) INL {
    constexpr int s = S.v;
    if constexpr (NS == 48 && s % 12 == 1) STAMP3(24 + s / 12);
    sfor<0, 3>([&](auto T) INL {
      constexpr int t = 2 - T.v;
      mfma16i(acc[t], b[s % (DIST + 1)][t], a[s % W]);
      SCHED();
      readB(IC<s + DIST>{}, T);
      if constexpr (T.v == 2 && s + W < NS) a[s % W] = lda<s + W>(ws, voff, abase);
      if constexpr (HINT == 1 && T.v == 0 && (s & 1) && s + 2 < NS) bad = stale2(a[(s + 1) % W], a[(s + 2) % W]);
      if constexpr (HINT == 1 && T.v == 2 && (s & 1) && s + 2 < NS) fresh2<s + 1>(bad, a[(s + 1) % W], a[(s + 2) % W], ws, voff, abase, rg.bud);
      SCHED();
    });
  });
  mfma_fence(acc[0], acc[1], acc[2]);
  if constexpr (NS == 48) { STAMP1(28); STAMP3(28); }
#pragma unroll
  for (int t = 0; t < 3; ++t) { res[2 * t] = pk(acc[t][0], acc[t][1]); res[2 * t + 1] = pk(acc[t][2], acc[t][3]); }
  auto s0 = __builtin_amdgcn_permlane16_swap(res[0], res[2], false, false);
  auto s1 = __builtin_amdgcn_permlane16_swap(res[1], res[3], false, false);
  auto s2 = __builtin_amdgcn_permlane16_swap(res[4], res[4], false, false);
  auto s3 = __builtin_amdgcn_permlane16_swap(res[5], res[5], false, false);
  const v4u o01 = {s0[0], s1[0], s0[1], s1[1]};
  const v4u o2 = {s2[0], s3[0], s2[1], s3[1]};
  if (last) {
    GAS char* ob = (GAS char*)out + (size_t)(t16i * 16 + (lane & 15)) * 1280 + ct * 80;
    *(GAS v4u*)(ob + pi * 16) = o01;
    if (lane < 16) *(GAS v4u*)(ob + 64) = o2;
  } else {
    gst128(o01, ws, so1, 0);
    gst128(o2, ws, so2, 0);
    const f32x4 sv = acc[0] + acc[1] + acc[2], qv = acc[0] * acc[0] + acc[1] * acc[1] + acc[2] * acc[2];
    float s = (sv[0] + sv[1]) + (sv[2] + sv[3]), q = (qv[0] + qv[1]) + (qv[2] + qv[3]);
    s = sum_halves(sum_rows16(s));
    q = sum_halves(sum_rows16(q));
    gst64(v2u{asu(s), asu(q)}, ws, so3, 0);
  }
}

constexpr float QSCALE = 0.22360679774997896f * 1.4426950408889634f;
constexpr float NEG = -1e30f;

DI float max16(const f32x16& s) {
  const float a = max3(s[0], s[1], s[2]), b = max3(s[3], s[4], s[5]), c = max3(s[6], s[7], s[8]);
  const float d = max3(s[9], s[10], s[11]), e = max3(s[12], s[13], s[14]);
  return __builtin_elementwise_maximum(max3(a, b, c), max3(d, e, s[15]));
}
DI void kv_write(unsigned kb, int lane, v4u k1, v4u ta, v4u tb) {
  *(LAS v4u*)(kb + lane * 16) = k1;
  *(LAS v2u*)(kb + 1024 + (lane ^ 32) * 8) = v2u{ta[0], ta[1]};
  const unsigned vb = kb + 1536 + (lane & 31) * 48 + (lane >> 5) * 8;
  *(LAS v2u*)(vb) = v2u{ta[2], ta[3]};
  *(LAS v2u*)(vb + 16) = v2u{tb[0], tb[1]};
  *(LAS v2u*)(vb + 32) = v2u{tb[2], tb[3]};
}
constexpr float MB = 16.f;

DI int kmapO4(int P, int t) {
  const int p = P / 5, r = P - 5 * p;
  if (r == 2) return 20 * (2 * p + t) + 16;
  return 20 * (2 * p + (r > 2)) + 8 * t + 4 * (r > 2 ? r - 3 : r);
}
DI v4u tr_frag(unsigned a0, unsigned a1) { const v2u x = ldstr(a0), y = ldstr(a1); return v4u{x[0], x[1], y[0], y[1]}; }
DI float sum8(v4u f, float s) {
#pragma unroll
  for (int i = 0; i < 4; ++i) { const unsigned u = f[i]; s = dot2(u, 0x3f803f80u, s); }
  return s;
}

DI void csum_rows(f32x4 c, unsigned la) {
  unsigned o[8];
#pragma unroll
  for (int i = 0; i < 4; ++i) {
    const float v = c[i], hi = rbf(v), mid = rbf(v - hi), lo = rbf(v - hi - mid);
    o[2 * i] = pk(hi, mid); o[2 * i + 1] = pk(lo, 0.f);
  }
  *(LAS v4u*)la = v4u{o[0], o[1], o[2], o[3]};
  *(LAS v4u*)(la + 16) = v4u{o[4], o[5], o[6], o[7]};
}

__global__ void __launch_bounds__(768) mega_kernel(unsigned char* wsp, void* out, const unsigned char* xin, const unsigned char* wqkv, const unsigned char* wo,
                                                   const unsigned char* wg, const unsigned char* wu, const unsigned char* wd) {
  __shared__ __attribute__((aligned(16))) char smem[LDS_BYTES];
  const int wave = ufl(threadIdx.x >> 6);
  asm volatile("" :: "v"((LAS char*)smem) : "memory");
  const int bx = blockIdx.x & 7, br = blockIdx.x >> 3;
#define GMAP 5
  const int band = (bx & 4) + (br >> 3), j = (bx & 3) * 8 + (br & 7);
  const int ct = (j >> 4) * 8 + (j & 7), rsel = (j >> 3) & 1;
  const rsrc_t ws = __builtin_amdgcn_make_buffer_rsrc(wsp, 0, WS_USED, 0x00020000);
  constexpr unsigned OOR = 0x40000000u;
  if (wave == 0) lds_st(MISC + (fresh_lane() & 31) * 4, 0u);
  WG_BARRIER();
  const int tk = (wave - 4) * 256 + bx * 32 + br;
  const unsigned sb = wave < 4 ? STG_X + (wave - 1) * 5120 : wave < 6 ? STG + (wave - 4) * 6144 : STG - 4096 + (wave - 4) * 8192;
  {
    const int lane = fresh_lane();
    const bool isx = wave < 4;
    dma_raw(isx ? xin : wqkv, isx ? 2048 * 1280 : 640 * 3840, isx || (lane & 7) < 5 ? (lane >> 3) * 1280 + (lane & 7) * 16 : OOR,
            isx ? (band * 256 + j * 8) * 1280 + (wave - 1) * 640 : (tk >> 4) * (16 * 3840) + (tk & 15) * 80, isx ? 128 : 10240, sb,
            opq(isx ? (wave == 1 || wave == 2 ? 5 : 0) : tk < 640 ? 6 : 0));
  }

  if (wave == 0) {
    const int lane = fresh_lane();
    __builtin_amdgcn_s_setprio(3);
    const v4u wsd = {(unsigned)(uintptr_t)wsp, (unsigned)((uintptr_t)wsp >> 32) & 0xffffu, WS_USED, 0x00020000u};
    unsigned bud = 0;
    const unsigned tag = ufl(__builtin_amdgcn_raw_buffer_load_b32(ws, 0, OFF_EP, SC1)) + 1u;
    lds_st(L_TAG, tag);
    CBAR();
    lds_st(L_TAGV, 1u);
    for (;;) {
      if (((unsigned)__ballot(rmw_poll_pairs((GAS unsigned*)(wsp + OFF_IN + (band * 32 + j) * 512), lane * 8, 0xfffffu, tag)) & 0xfffffu) == 0xfffffu) break;
      SPEND(bud);
    }
    lds_st(L_WQOK, 1u);
#pragma unroll 1
    for (int f = 0; f < 6; ++f) dma_fill(wsd, lane * 16, OFF_WQ + j * 81920 + f * UNIT, ARENA + f * UNIT);
    asm volatile("s_waitcnt vmcnt(50)" ::: "memory"); lds_st(L_LANDED, 1u);
    dma_fill(wsd, lane * 16, OFF_WQ + j * 81920 + 6 * UNIT, ARENA + 6 * UNIT);
    asm volatile("s_waitcnt vmcnt(50)" ::: "memory"); lds_st(L_LANDED, 2u);
    dma_fill(wsd, lane * 16, OFF_WQ + j * 81920 + 7 * UNIT, ARENA + 7 * UNIT);
    asm volatile("s_waitcnt vmcnt(50)" ::: "memory"); lds_st(L_LANDED, 3u);
    asm volatile("s_waitcnt vmcnt(40)" ::: "memory"); lds_st(L_LANDED, 4u);
    asm volatile("s_waitcnt vmcnt(30)" ::: "memory"); lds_st(L_LANDED, 5u);
    asm volatile("s_waitcnt vmcnt(20)" ::: "memory"); lds_st(L_LANDED, 6u);
    asm volatile("s_waitcnt vmcnt(10)" ::: "memory"); lds_st(L_LANDED, 7u);
    asm volatile("s_waitcnt vmcnt(0)" ::: "memory"); lds_st(L_LANDED, 8u);
    __builtin_amdgcn_s_setprio(PRIO_H);
    lds_wait_gt(L_W2OK, 0u, bud);
    unsigned issued = 8, published = 8;
#pragma unroll 1
    for (int lp = 1; lp < 8; ++lp) {
      const int L = lp >> 2, ph = lp & 3;
      unsigned long long units, needs_lo, needs_hi; int n; unsigned src;
      if (ph == 0) { units = 0x76543210ull; n = 8; src = OFF_WQ + j * 81920;
        needs_lo = 17ull | 25ull << 8 | 26ull << 16 | 27ull << 24 | 28ull << 32 | 29ull << 40 | 32ull << 48 | 33ull << 56; needs_hi = 0; }
      else if (ph == 1) { units = 0x54321ull; n = 5; src = OFF_WO + ct * 51200;
        needs_lo = 40ull | 41ull << 8 | 42ull << 16 | 43ull << 24 | 44ull << 32; needs_hi = 0; }
      else if (ph == 2) { units = 0x21CBA98760EDull; n = 12; src = OFF_WG + j * 122880;
        needs_lo = 30ull | 31ull << 8 | 39ull << 16 | 45ull << 24 | 46ull << 32 | 47ull << 40 | 47ull << 48 | 47ull << 56;
        needs_hi = 47ull | 47ull << 8 | 48ull << 16 | 49ull << 24; }
      else { units = 0xCBA9876ED543ull; n = 12; src = OFF_WD + ct * 122880;
        needs_lo = 50ull | 51ull << 8 | 52ull << 16 | 53ull << 24 | 54ull << 32 | 56ull << 40 | 57ull << 48 | 58ull << 56;
        needs_hi = 59ull | 60ull << 8 | 61ull << 16 | 62ull << 24; }
#pragma unroll 1
      for (int f = 0; f < n; ++f) {
        const int unit = (int)((units >> (4 * f)) & 15ull);
        const int nd = (int)(((f < 8 ? needs_lo >> (8 * f) : needs_hi >> (8 * (f - 8)))) & 255ull) + (L - 1) * NSTAGE;
        if (nd > 0) {
          unsigned v = lds_ld(L_PROG + (lane & 7) * 4);
          if (!__all((int)v >= nd)) {
            if (published != issued) {
              if (issued - published >= 2) { asm volatile("s_waitcnt vmcnt(10)" ::: "memory"); lds_st(L_LANDED, issued - 1); }
              asm volatile("s_waitcnt vmcnt(0)" ::: "memory");
              published = issued; lds_st(L_LANDED, published);
            }
            prog_wait((unsigned)nd, bud);
          }
        }
        dma_fill(wsd, lane * 16, src + f * UNIT, ARENA + unit * UNIT);
        ++issued;
        if (issued - published > LDEPTH) {
          asm volatile("s_waitcnt vmcnt(%0)" :: "i"(10 * LDEPTH) : "memory");
          published = issued - LDEPTH; lds_st(L_LANDED, published);
        }
      }
    }
    asm volatile("s_waitcnt vmcnt(10)" ::: "memory");
    lds_st(L_LANDED, issued - 1);
    asm volatile("s_waitcnt vmcnt(0)" ::: "memory");
    lds_st(L_LANDED, issued);
    return;
  }

  if (wave >= 4) {
    const int cw = wave - 4, grp = cw >> 2, wi = cw & 3;
    const int qt = (1 - grp) * 4 + wi;
    const int t32_ = band * 8 + qt;
    const int t16_ = band * 16 + (1 - grp) * 8 + rsel * 4 + wi;
    const int rep = j & 3;
    Ring rg;
    rg.prog = L_PROG + cw * 4;
    rg.bud = 0;
#define PRIO_E 2
#define PRIO_L 1
    if (grp == 0) __builtin_amdgcn_s_setprio(PRIO_E); else __builtin_amdgcn_s_setprio(PRIO_L);
    const int dslot = qt + (qt < 4 ? 4 : 8);
    STAMP(30);
    STAMP(0);
    unsigned res[6] = {0u, 0u, 0u, 0u, 0u, 0u};
    unsigned tag;
    {
      const int lane = fresh_lane();
      const int gam = lane >> 4, q = (lane & 15) >> 2, p = lane & 3, hh = gam >> 1, g = (gam & 1) * 4 + p;
      const v4u ones = {0x3f803f80u, 0x3f803f80u, 0x3f803f80u, 0x3f803f80u};
      f32x16 z;
#pragma unroll
      for (int i = 0; i < 16; ++i) z[i] = 0.f;
      if (tk < 640) {
        const int hp = tk & 15, ks = tk >> 4;
        const unsigned r = sb + (8 * hh + q) * 384;
        unsigned c0 = r + (g < 4 ? 0 : 128) + (g & 3) * 8, c1 = r + (g == 0 ? 32 : g == 1 ? 128 + 32 : 256 + ((g < 7 ? g : 6) - 2) * 8);
        unsigned dst = OFF_WQ + hp * 163840 + ks * 2048 + lane * 16, cs = OFF_CSQ + (hp * 80 + ks) * 256 + lane * 4;
        unsigned fa = lane < 16 ? OFF_IN + ((lane >> 1) * 32 + 2 * hp + (lane & 1)) * 512 + ks * 4 : OOR;
        STAMP4(24);
#pragma unroll 1
        for (int pass = DRYPASS ? 0 : 1; pass < 2; ++pass) {
          const unsigned off = pass ? 0u : OOR;
          if (pass) DRAIN();
          STAMP4(25);
          v4u f[2][2];
#pragma unroll
          for (int h2 = 0; h2 < 2; ++h2) {
            f[h2][0] = tr_frag(c0 + h2 * 40, c0 + h2 * 40 + 1536);
            f[h2][1] = tr_frag(c1 + h2 * 40, c1 + h2 * 40 + 1536);
          }
#pragma unroll
          for (int h2 = 0; h2 < 2; ++h2) {
            if ((lane & 31) >= 28) f[h2][1] = v4u{0u, 0u, 0u, 0u};
            gst128(f[h2][0], ws, dst + off + h2 * 81920, 0); gst128(f[h2][1], ws, dst + off + h2 * 81920 + 1024, 0);
          }
#pragma unroll
          for (int h2 = 0; h2 < 2; ++h2) {
            const float s0 = mfma32(ones, f[h2][0], z)[0], s1 = mfma32(ones, f[h2][1], z)[0];
            gst32(asu(lane < 32 ? s0 : s1), ws, cs + off + h2 * 10240);
          }
          if (pass) lds_wait_gt(L_TAGV, 0u, rg.bud);
          tag = ufl(lds_ld(L_TAG));
          STAMP4(26);
          if (pass) DRAIN();
          STAMP4(27);
          gst32(tag, ws, fa + off);
        }
        lds_add1(L_CRIT);
      } else {
        const bool gu = tk < 1280, dn = tk < 1664;
        const int i = gu ? tk - 640 : dn ? tk - 1280 : tk - 1664;
        unsigned a0, a1, a2, a3, dst, o2;
        if (L2GATE) lds_wait_gt(L_CRIT, L2GATE - 1, rg.bud);
        if (gu) {
          const unsigned vo = (lane & 15) < 12 ? gam * 3072 + (lane & 15) * 16 : OOR, so = (i >> 4) * (16 * 3072) + (i & 15) * 192;
          dma_raw(wg, 640 * 3072, vo, so, 4 * 3072, sb, 4);
          dma_raw(wu, 640 * 3072, vo, so, 4 * 3072, sb + 4096, 4);
          const unsigned r = sb + (8 * hh + q) * 256;
          a0 = r + g * 8; a1 = a0 + 4096; a2 = r + 64 + (g & 3) * 8 + (g < 4 ? 0 : 4096); a3 = 96;
          dst = OFF_WG + (i & 15) * 245760 + (i >> 4) * 3072; o2 = 2048 + lane * 16;
        } else {
          const unsigned vo = (lane & 15) < 10 ? gam * 1280 + (lane & 15) * 16 : OOR;
          if (tk < 1824) {
#pragma unroll 1
            for (int u = 0; u < 8; ++u) {
              const int P = 4 * (i >> 3) + (u >> 1);
              const int row = dn ? 16 * (P >> 1) + 4 * (P & 1) + 8 * (u & 1) : kmapO4(P, u & 1);
              dma_raw(dn ? wd : wo, dn ? 1536 * 1280 : 640 * 1280, vo, row * 1280 + (i & 7) * 160, 0, sb + u * 1024, 1);
            }
          }
          const unsigned r = sb + gam * 2048 + q * 256;
          a0 = r + p * 8; a1 = a0 + 32; a2 = r + 64 + (p & 1) * 8; a3 = 80;
          dst = tk >= 1824 ? OOR : dn ? OFF_WD + (i & 7) * 245760 + (i >> 3) * 2560 : OFF_WO + (i & 7) * 102400 + (i >> 3) * 2560;
          o2 = (lane & 15) < 8 ? 2048 + gam * 128 + (lane & 7) * 16 : OOR;
        }
        const unsigned dstep = gu || dn ? 122880 : 51200;
        STAMP4(24);
        if (cw == 7) {
          const int ci = j * 7 + lane;
          gst32(0u, ws, lane < 7 ? OFF_CTR + ((ci >> 5) * 64 + band * 8 + ((ci >> 2) & 7)) * 1024 + (ci & 3) * 256 : OOR);
        }
        asm volatile("s_waitcnt vmcnt(0)" : "+v"(a0), "+v"(a1), "+v"(a2), "+v"(o2) :: "memory");
        STAMP4(25);
        v4u f[2][3];
#pragma unroll
        for (int h2 = 0; h2 < 2; ++h2) {
          f[h2][0] = tr_frag(a0 + h2 * a3, a0 + h2 * a3 + 1024);
          f[h2][1] = tr_frag(a1 + h2 * a3, a1 + h2 * a3 + 1024);
          f[h2][2] = tr_frag(a2 + h2 * a3, a2 + h2 * a3 + 1024);
        }
        if (S2GATE) lds_wait_gt(L_CRIT, S2GATE - 1, rg.bud);
#pragma unroll
        for (int h2 = 0; h2 < 2; ++h2) {
          gst128(f[h2][0], ws, dst + h2 * dstep + lane * 16, 0); gst128(f[h2][1], ws, dst + h2 * dstep + lane * 16 + 1024, 0);
          gst128(f[h2][2], ws, dst + h2 * dstep + o2, 0);
        }
        if (gu) {
#pragma unroll
          for (int h2 = 0; h2 < 2; ++h2) {
            const float s0 = mfma32(ones, f[h2][0], z)[0], s1 = mfma32(ones, f[h2][1], z)[0], s2 = mfma32(ones, f[h2][2], z)[0];
            const unsigned cs = OFF_CSG + ((2 * (i & 15) + h2) * 40 + (i >> 4)) * 512 + lane * 4;
            gst32(asu(lane < 32 ? s0 : s1), ws, cs);
            gst32(lane < 32 ? asu(s2) : 0u, ws, cs + 256);
          }
        }
        lds_wait_gt(L_TAGV, 0u, rg.bud);
        tag = ufl(lds_ld(L_TAG));
        STAMP4(26);
        DRAIN();
        STAMP4(27);
      }
      if (lds_inc1(L_STG) == 7u) gst32(tag, ws, lane == 0 ? OFF_F2 + blockIdx.x * 4 : OOR);
    }
    STAMP4(28);
#pragma unroll 1
    for (int L = 0; L < 2; ++L) {
      rg.lf = L * NFILL; rg.ls = L * NSTAGE;
      const int t32i = opq(t32_), t16i = opq(t16_);
      PP(PHPRIO & 3);
      v4u q1; v2u q2;
      {
        f32x16 acc[2]; float rs;
        gemm32<2, WQ_, 2, 0, 0, TabQ, L_CQ>(acc, rs, rg, ws, OFF_XA + (2 * L) * XA_SZ + t32i * XA_T, OFF_ST + (2 * L) * ST_SZ + t32i * 4096,
                                            [&]() INL {
          if (HINT && HINT < 3) {
            const int lane = fresh_lane(), h = lane >> 5;
            const v4u sv = {SENT, SENT, SENT, SENT};
            if (band < 7) {
              const unsigned gb = OFF_KVH + L * KVH_SZ + ((band * 32 + j) * 8 + qt) * KVH_T;
              gst128(sv, ws, lane * 16, gb); gst128(sv, ws, lane * 16 + 1024, gb); gst128(sv, ws, lane * 16 + 2048, gb);
            }
            const unsigned ob = OFF_OA + L * OA_SZ + (2 * t32i) * OA_T + (5 * (j >> 1)) * 256;
            const unsigned tv = ((lane >> 4) & 1) * OA_T + (lane & 15) * 16;
            gst128(sv, ws, tv + h * 256, ob + (3 * (j & 1)) * 256);
            gst64(v2u{SENT, SENT}, ws, tv + (h ? WS_USED : 0u), ob + 2 * 256 + (j & 1) * 8);
          }
          if (L > 0) ctr_wait(wsp, 3, t32i, rep, rg.bud);
          else {
            DRAIN();
            unsigned xr = 0;
            lds_wait_bits(L_XRDY, 0xfu << (4 * qt), xr, rg.bud);
            if (TURN0 > 0 && grp == 1) lds_wait_gt(rg.prog - 16, TURN0 - 1, rg.bud);
          }
          STAMP(1 + L * 12); });
        STAMP(2 + L * 12);
        const int lane = fresh_lane(), h = lane >> 5;
        v4u k1, ta, tb;
        const float rq = rs * QSCALE, r0 = h ? rs : rq;
#pragma unroll
        for (int i = 0; i < 4; ++i) k1[i] = pk(acc[0][8 + 2 * i] * rs, acc[0][9 + 2 * i] * rs);
        ta[0] = pk(acc[1][0] * r0, acc[1][1] * r0); ta[1] = pk(acc[1][2] * r0, acc[1][3] * r0);
        ta[2] = pk(acc[1][4] * rs, acc[1][5] * rs); ta[3] = pk(acc[1][6] * rs, acc[1][7] * rs);
#pragma unroll
        for (int i = 0; i < 4; ++i) tb[i] = pk(acc[1][8 + 2 * i] * rs, acc[1][9 + 2 * i] * rs);
        if (h) { tb[2] = 0x3f803f80u; tb[3] = 0x3f803f80u; }
        q2[0] = h ? 0x3f80u : ta[0]; q2[1] = h ? 0u : ta[1];
        if (!h) { ta[0] = 0u; ta[1] = 0u; }
        if (band < 7) {
          const unsigned gb = OFF_KVH + L * KVH_SZ + ((band * 32 + j) * 8 + qt) * KVH_T;
          gst128(k1, ws, lane * 16, gb); gst128(ta, ws, lane * 16 + 1024, gb); gst128(tb, ws, lane * 16 + 2048, gb);
          if (HINT) gst32(tag, ws, lane == 0 ? OFF_HALOF + (((L * 8 + band) * 32 + j) * 2 + (qt >> 2)) * 256 + (qt & 3) * 4 : WS_USED);
        }
        if (L > 0) prog_wait(L * NSTAGE, rg.bud); else lds_wait_gt(L_STG, 7u, rg.bud);
        kv_write(KV_OFF + dslot * KV_T, lane, k1, ta, tb);
        CBAR();
        lds_or1(L_RDY + L * 4, 1u << dslot);
#pragma unroll
        for (int i = 0; i < 4; ++i) q1[i] = pk(acc[0][2 * i] * rq, acc[0][2 * i + 1] * rq);
      }
      STAMP(3 + L * 12);
      PP((PHPRIO >> 2) & 3);
      {
        const int lane = fresh_lane(), h = lane >> 5;
        f32x16 o, negm, sc[2];
        const int topA = dslot, topB = grp == 0 ? 8 : 12;
        const int top0 = grp == 0 ? topA : topB, top1 = grp == 0 ? topB : topA;
        const unsigned vlo = 1536 + (4 * h + ((lane & 15) >> 2)) * 48 + (((lane >> 4) & 1) == 0 ? 8 * (lane & 3) : ((lane & 3) == 0 ? 32 : 40));
        unsigned ka[2], kb[2], va[2], vd;
        ka[0] = KV_OFF + (top0 - 4) * KV_T + lane * 16; ka[1] = KV_OFF + (top1 - 4) * KV_T + lane * 16;
        kb[0] = KV_OFF + (top0 - 4) * KV_T + 1024 + lane * 8; kb[1] = KV_OFF + (top1 - 4) * KV_T + 1024 + lane * 8;
        va[0] = KV_OFF + (top0 - 4) * KV_T + vlo; va[1] = KV_OFF + (top1 - 4) * KV_T + vlo;
        vd = KV_OFF + dslot * KV_T + vlo;
        asm volatile("" : "+v"(ka[0]), "+v"(ka[1]), "+v"(kb[0]), "+v"(kb[1]), "+v"(va[0]), "+v"(va[1]), "+v"(vd));
        const int ql = (lane & 31) - 4 * h;
        unsigned rdyv = lds_ld(L_RDY + L * 4);
        {
          f32x16 z;
#pragma unroll
          for (int r = 0; r < 16; ++r) { z[r] = 0.f; o[r] = 0.f; }
          sc[0] = mfma32(lds128(KV_OFF + dslot * KV_T + lane * 16), q1, z);
          sc[0] = mfma32k8(lds64(KV_OFF + dslot * KV_T + 1024 + lane * 8), q2, sc[0]);
#pragma unroll
          for (int r = 0; r < 16; ++r) sc[0][r] = ((r & 3) + 8 * (r >> 2)) <= ql ? sc[0][r] : NEG;
          const float m = max_halves(max16(sc[0])) + MB;
#pragma unroll
          for (int r = 0; r < 16; ++r) { sc[0][r] -= m; negm[r] = -m; }
        }
        STAMP2(24);
        unsigned rdy = ufl(rdyv);
        sfor<0, 9>([&](auto N) INL {
          constexpr int n = N.v;
          f32x16& s = sc[n & 1];
          f32x16& sn = sc[(n + 1) & 1];
          v4u k1; v2u k2, v0, v1, v2, v3;
          if constexpr (n == ANN) {
            if (!HINT && band < 7) {
              DRAIN();
              gst32(tag, ws, lane == 0 ? OFF_HALOF + (((L * 8 + band) * 32 + j) * 2 + (qt >> 2)) * 256 + (qt & 3) * 4 : WS_USED);
            }
            STAMP2(25);
          }
          {
            const unsigned a = n == 0 ? vd : va[(n - 1) >> 2] + (3 - ((n - 1) & 3)) * KV_T;
            v0 = ldstr(a); v1 = ldstr(a + 384); v2 = ldstr(a + 768); v3 = ldstr(a + 1152);
          }
          if constexpr (n == 8) { CBAR(); lds_st(rg.prog, rg.ls + 9); CBAR(); }
          if constexpr (n == 4 || n == 8) {
            if (grp == (n == 4 ? 0 : 1)) {
#pragma unroll
              for (int r = 0; r < 16; ++r) s[r] = ((r & 3) + 8 * (r >> 2)) > ql ? s[r] : NEG;
            }
          }
          v4u p, p2;
          p[0] = pk(ex2(s[0]), ex2(s[1])); p[1] = pk(ex2(s[2]), ex2(s[3]));
          if constexpr (n < 8) {
            lds_wait_bits(L_RDY + L * 4, 1u << ((n < 4 ? top0 : top1) - 1 - (n & 3)), rdy, rg.bud);
            k1 = lds128(ka[n >> 2] + (3 - (n & 3)) * KV_T); k2 = lds64(kb[n >> 2] + (3 - (n & 3)) * KV_T);
          }
          p[2] = pk(ex2(s[4]), ex2(s[5])); p[3] = pk(ex2(s[6]), ex2(s[7]));
          p2[0] = pk(ex2(s[8]), ex2(s[9]));
          SCHED();
          if constexpr (n < 8) sn = mfma32(k1, q1, negm);
          p2[1] = pk(ex2(s[10]), ex2(s[11]));
          float e12 = ex2(s[12]);
          SCHED();
          if constexpr (n < 8) sn = mfma32k8(k2, q2, sn);
          p2[2] = pk(e12, ex2(s[13])); p2[3] = pk(ex2(s[14]), ex2(s[15]));
          const unsigned any = ((p[0] | p[1] | p[2]) | (p[3] | p2[0] | p2[1]) | (p2[2] | p2[3])) & 0x40004000u;
          if (__builtin_expect(__any(any != 0u), 0)) {
            const float d = fmaxf(max_halves(max16(s)) + MB, 0.f);
            const float f = ex2(-d);
#pragma unroll
            for (int r = 0; r < 16; ++r) { s[r] -= d; negm[r] -= d; o[r] *= f; if (n < 8) sn[r] -= d; }
#pragma unroll
            for (int i = 0; i < 4; ++i) { p[i] = pk(ex2(s[2 * i]), ex2(s[2 * i + 1])); p2[i] = pk(ex2(s[8 + 2 * i]), ex2(s[9 + 2 * i])); }
          }
          o = mfma32(v4u{v0[0], v0[1], v1[0], v1[1]}, p, o);
          o = mfma32(v4u{v2[0], v2[1], v3[0], v3[1]}, p2, o);
          SCHED();
          if constexpr (n == 3) STAMP2(26);
          if constexpr (n == 5) STAMP2(27);
          if constexpr (n == 7) STAMP2(28);
        });
        const float inv = __builtin_amdgcn_rcpf(o[12]);
        v4u o1;
#pragma unroll
        for (int i = 0; i < 4; ++i) o1[i] = pk(o[2 * i] * inv, o[2 * i + 1] * inv);
        const v2u o2 = {pk(o[8] * inv, o[9] * inv), pk(o[10] * inv, o[11] * inv)};
        const unsigned ob = OFF_OA + L * OA_SZ + (2 * t32i) * OA_T + (5 * (j >> 1)) * 256;
        const unsigned tv = ((lane >> 4) & 1) * OA_T + (lane & 15) * 16;
        gst128(o1, ws, tv + h * 256, ob + (3 * (j & 1)) * 256);
        gst64(o2, ws, tv + (h ? WS_USED : 0u), ob + 2 * 256 + (j & 1) * 8);
        STAMP(4 + L * 12);
        if (L == 0) lds_wait_gt(L_W2OK, 0u, rg.bud);
        ctr_arrive(wsp, 4 * L, t32i);
        if (L == 0) {
          const int g4 = lane >> 4;
          const unsigned ro = ((t16i & 1) * 16 + (lane & 15)) * 16 + (g4 & 1) * 8, rb = OFF_XA + (t16i >> 1) * XA_T + (5 * ct) * 512;
          const v2u r0 = gld64(ws, ro + (g4 >> 1) * 512, rb), r1 = gld64(ws, ro + (g4 >> 1) * 512 + 1024, rb), r2 = gld64(ws, g4 < 2 ? ro + 2048 : WS_USED, rb);
          res[0] = r0[0]; res[1] = r0[1]; res[2] = r1[0]; res[3] = r1[1]; res[4] = r2[0]; res[5] = r2[1];
        }
      }
      STAMP(5 + L * 12);
      PP((PHPRIO >> 4) & 3);
      gemm16<20, WO_, 3, 8, 9, TabO>(rg, ws, OFF_OA + L * OA_SZ + t16i * OA_T, res, OFF_XA + (2 * L + 1) * XA_SZ + (t16i >> 1) * XA_T,
                                     OFF_ST + (2 * L + 1) * ST_SZ + (t16i >> 1) * 4096, false, out, t16i, ct,
                                     [&]() INL { ctr_wait(wsp, 4 * L, t16i >> 1, rep, rg.bud); STAMP(6 + L * 12); });
      STAMP(7 + L * 12);
      ctr_arrive(wsp, 4 * L + 1, t16i >> 1);
      PP((PHPRIO >> 6) & 3);
      {
        f32x16 acc[3]; float rs;
        gemm32<3, WG_, 2, 13, 14, TabG, L_CG>(acc, rs, rg, ws, OFF_XA + (2 * L + 1) * XA_SZ + t32i * XA_T, OFF_ST + (2 * L + 1) * ST_SZ + t32i * 4096,
                                              [&]() INL {
          if (HINT && HINT < 3) {
            const int lane = fresh_lane();
            const v4u sv = {SENT, SENT, SENT, SENT};
            const unsigned hb = OFF_HA + L * HA_SZ + (2 * t32i) * HA_T + (6 * j) * 256;
            const unsigned tv = ((lane >> 4) & 1) * HA_T + (lane & 15) * 16 + (lane >> 5) * 256;
            gst128(sv, ws, tv, hb); gst128(sv, ws, tv + 512, hb); gst128(sv, ws, tv + 1024, hb);
          }
          ctr_wait(wsp, 4 * L + 1, t32i, rep, rg.bud); STAMP(8 + L * 12); STAMP1(24); });
        STAMP1(25); STAMP1(26);
        const int lane = fresh_lane(), h = lane >> 5;
        const float r2 = rs * rs;
        const unsigned hb = OFF_HA + L * HA_SZ + (2 * t32i) * HA_T + (6 * j) * 256;
        const unsigned tv = ((lane >> 4) & 1) * HA_T + (lane & 15) * 16 + h * 256;
        v4u hv;
#pragma unroll
        for (int i = 0; i < 4; ++i) hv[i] = pk(acc[0][2 * i] * r2 * acc[1][2 * i], acc[0][2 * i + 1] * r2 * acc[1][2 * i + 1]);
        gst128(hv, ws, tv, hb);
#pragma unroll
        for (int i = 0; i < 4; ++i) hv[i] = pk(acc[0][8 + 2 * i] * r2 * acc[1][8 + 2 * i], acc[0][9 + 2 * i] * r2 * acc[1][9 + 2 * i]);
        gst128(hv, ws, tv + 512, hb);
#pragma unroll
        for (int i = 0; i < 4; ++i) hv[i] = pk(acc[2][2 * i] * r2 * acc[2][8 + 2 * i], acc[2][2 * i + 1] * r2 * acc[2][9 + 2 * i]);
        gst128(hv, ws, tv + 1024, hb);
      }
      STAMP(9 + L * 12);
      ctr_arrive(wsp, 4 * L + 2, t32i);
      PP((PHPRIO >> 8) & 3);
      gemm16<48, WD_, 3, 25, 26, TabD>(rg, ws, OFF_HA + L * HA_SZ + t16i * HA_T, res, OFF_XA + 2 * XA_SZ + (t16i >> 1) * XA_T,
                                       OFF_ST + 2 * ST_SZ + (t16i >> 1) * 4096, L == 1, out, t16i, ct,
                                       [&]() INL { ctr_wait(wsp, 4 * L + 2, t16i >> 1, rep, rg.bud); STAMP(10 + L * 12); });
      STAMP(11 + L * 12);
      if (L == 0) ctr_arrive(wsp, 3, t16i >> 1);
    }
  } else {
    const int lane = fresh_lane();
    const int half = 2 - wave;
    __builtin_amdgcn_s_setprio(3);
    unsigned bud = 0;
    unsigned tag = 0;
    if (wave == 3) {
      unsigned t = __builtin_amdgcn_raw_buffer_load_b32(ws, lane * (1u << 20) + (1u << 20) - 128, 0, SC1);
      t ^= *(const GAS unsigned*)((const GAS char*)out + (lane & 7) * 327680 + j * 128);
      asm volatile("" :: "v"(t));
    }
    if (wave == 3) { lds_wait_gt(L_TAGV, 0u, bud); tag = ufl(lds_ld(L_TAG)); }
    if (wave < 3) {
      const int hx = wave - 1;
      {
        const unsigned xa = OFF_XA + (band * 8 + (j >> 2)) * XA_T + ((j & 3) * 8 + (lane & 7)) * 16 + (hx * 40 + (lane >> 3)) * 512;
        unsigned la = STG_X + hx * 5120 + (lane & 7) * 128 + (lane >> 3) * 16;
        unsigned sa = OFF_ST + (band * 8 + (j >> 2)) * 4096 + st_line(hx * 8 + (lane >> 3), (j >> 1) & 1) + (lane & 7) * 16 + (j & 1) * 8;
        unsigned fa = lane < 32 ? OFF_IN + (band * 32 + lane) * 512 + 256 + (j * 2 + hx) * 4 : OOR;
#pragma unroll 1
        for (int pass = DRYPASS ? 0 : 1; pass < 2; ++pass) {
          const unsigned off = pass ? 0u : OOR;
          if (pass) DRAIN();
          v4u xv[5];
#pragma unroll
          for (int i = 0; i < 5; ++i) xv[i] = lds128(la + i * 1024);
#pragma unroll
          for (int i = 0; i < 5; ++i) gst128(xv[i], ws, xa + off + i * 4096, 0);
          float s = 0.f, qq = 0.f;
#pragma unroll
          for (int i = 0; i < 5; ++i) {
            s = sum8(xv[i], s);
#pragma unroll
            for (int k = 0; k < 4; ++k) { const unsigned u = xv[i][k]; qq = dot2(u, u, qq); }
          }
          gst64(v2u{asu(s), asu(qq)}, ws, sa + off, 0);
          if (pass) lds_wait_gt(L_TAGV, 0u, bud);
          tag = ufl(lds_ld(L_TAG));
          if (pass) DRAIN();
          gst32(tag, ws, fa + off);
        }
      }
      lds_add1(L_CRIT);
    } else {
      for (;;) {
        const unsigned m = (unsigned)__ballot(rmw_poll_pairs((GAS unsigned*)(wsp + OFF_IN + (band * 32 + j) * 512 + 256), lane * 8, 0xffffffffu, tag));
        lds_st(L_XRDY, m);
        if (m == 0xffffffffu) break;
        SPEND(bud);
      }
      lds_wait_gt(L_WQOK, 0u, bud);
      v4u pc[10];
      sfor<0, 10>([&](auto I) INL { pc[I.v] = lda<I.v>(ws, lane * 16, OFF_CSQ + j * 10240); });
      f32x4 c = __builtin_bit_cast(f32x4, pc[0]);
#pragma unroll
      for (int i = 1; i < 10; ++i) c += __builtin_bit_cast(f32x4, pc[i]);
#pragma unroll
      for (int i = 0; i < 4; ++i) c[i] = sum_halves(sum_rows16(c[i]));
      if (lane < 16) csum_rows(c, L_CQ + lane * 32);
      CBAR();
      lds_st(L_CSUM, 1u);
    }
    __builtin_amdgcn_s_setprio(PRIO_H);
    if (wave == 3) {
      lds_wait_gt(L_PROG, F2GATE, bud);
      for (;;) {
        const v4u f0 = gld128(ws, lane * 16, OFF_F2);
        const bool ok = f0[0] == tag && f0[1] == tag && f0[2] == tag && f0[3] == tag;
        if (__all(ok)) break;
        SPEND(bud);
        __builtin_amdgcn_s_sleep(16);
        CBAR();
      }
      CBAR();
      lds_st(L_W2OK, 1u);
      if (blockIdx.x == 0) gst32(tag, ws, lane == 0 ? OFF_EP : WS_USED);
      f32x4 c = {0.f, 0.f, 0.f, 0.f};
#pragma unroll 1
      for (int r = 0; r < 2; ++r) {
        v4u pc[10];
        sfor<0, 10>([&](auto I) INL { pc[I.v] = lda<I.v>(ws, lane * 16, OFF_CSG + j * 20480 + r * 10240); });
#pragma unroll
        for (int i = 0; i < 10; ++i) c += __builtin_bit_cast(f32x4, pc[i]);
      }
#pragma unroll
      for (int i = 0; i < 4; ++i) c[i] = sum_halves(c[i]);
      if (lane < 24) csum_rows(c, L_CG + lane * 32);
      CBAR();
      lds_st(L_CSUM, 2u);
      return;
    }
    if (band == 0) {
#pragma unroll 1
      for (int L = 0; L < 2; ++L) {
        if (L > 0) prog_wait(L * NSTAGE, bud); else lds_wait_gt(L_STG, 7u, bud);
        const v4u z = {0u, 0u, 0u, 0u};
#pragma unroll 1
        for (int sl = 0; sl < 4; ++sl) kv_write(KV_OFF + (8 * half + sl) * KV_T, lane, z, v4u{lane < 32 ? 0xf149u : 0u, 0u, 0u, 0u}, z);
        CBAR();
        lds_or1(L_RDY + L * 4, 0xfu << (8 * half));
      }
    } else {
#pragma unroll 1
      for (int L = 0; L < 2; ++L) {
        lds_wait_gt(L_PROG + (1 - half) * 16, L * NSTAGE + 5, bud);
        if (L > 0) prog_wait(L * NSTAGE, bud); else lds_wait_gt(L_STG, 7u, bud);
        unsigned pend = 0xfu;
        GAS unsigned* fl = (GAS unsigned*)(wsp + OFF_HALOF + (((L * 8 + band - 1) * 32 + j) * 2 + half) * 256);
        const unsigned gb = OFF_KVH + L * KVH_SZ + (((band - 1) * 32 + j) * 8 + 4 * half) * KVH_T;
        while (pend) {
          const unsigned nw = (unsigned)__ballot(rmw_poll_lanes(fl, lane * 4, 0xfu, 0u, 0u) == tag) & pend;
          __builtin_amdgcn_fence(__ATOMIC_ACQUIRE, "wavefront");
          CBAR();
          if (!nw) {
            SPEND(bud);
            continue;
          }
          pend &= ~nw;
          v4u x[4][3];
#pragma unroll
          for (int sl = 3; sl >= 0; --sl) {
            const unsigned vo = lane * 16 + ((nw >> sl & 1) ? 0u : WS_USED);
#pragma unroll
            for (int i = 0; i < 3; ++i) x[sl][i] = gld128(ws, vo + i * 1024, gb + sl * KVH_T);
          }
#pragma unroll
          for (int sl = 3; sl >= 0; --sl)
            if (nw >> sl & 1) {
              if (HINT && __builtin_expect((stale2(x[sl][0], x[sl][1]) | stale2(x[sl][2], x[sl][2])) != 0, 0)) { if (bud < SPIN_CAP) { ++bud; pend |= 1u << sl; continue; } }
              kv_write(KV_OFF + (8 * half + sl) * KV_T, lane, x[sl][0], x[sl][1], x[sl][2]);
              CBAR();
              lds_or1(L_RDY + L * 4, 1u << (8 * half + sl));
            }
        }
      }
    }
  }
}

extern "C" void kernel_launch(void* const* d_in, const int* in_sizes, int n_in, void* d_out, int out_size, void* d_ws, size_t ws_size,
                              hipStream_t stream) {
  typedef const unsigned char* cp;
  mega_kernel<<<256, 768, 0, stream>>>((unsigned char*)d_ws, d_out, (cp)d_in[0], (cp)d_in[1], (cp)d_in[2], (cp)d_in[4], (cp)d_in[5], (cp)d_in[6]);
}
```

```cpp
#include <hip/hip_runtime.h>
#include <hip/hip_bf16.h>
#include <stdint.h>
#pragma clang diagnostic ignored "-Wint-to-pointer-cast"

typedef unsigned v4u __attribute__((ext_vector_type(4)));
typedef unsigned v2u __attribute__((ext_vector_type(2)));
typedef short s16x8 __attribute__((ext_vector_type(8)));
typedef short s16x4 __attribute__((ext_vector_type(4)));
typedef float f32x16 __attribute__((ext_vector_type(16)));
typedef float f32x4 __attribute__((ext_vector_type(4)));
typedef float f32x2 __attribute__((ext_vector_type(2)));
typedef __bf16 bf2 __attribute__((ext_vector_type(2)));
typedef unsigned short u16;
#define LAS __attribute__((address_space(3)))
#define GAS __attribute__((address_space(1)))
#define DI __device__ __forceinline__
#define INL __attribute__((always_inline))
#define RLX_AGENT __ATOMIC_RELAXED, __HIP_MEMORY_SCOPE_AGENT
#define RLX_WG __ATOMIC_RELAXED, __HIP_MEMORY_SCOPE_WORKGROUP
#define CBAR() asm volatile("" ::: "memory")
#define SCHED() __builtin_amdgcn_sched_barrier(0)

constexpr unsigned OFF_CTR = 0;
constexpr unsigned OFF_HALOF = 7 * 64 * 1024;
constexpr unsigned OFF_IN = OFF_HALOF + 2 * 8 * 32 * 512;
constexpr unsigned OFF_F2 = OFF_IN + 256 * 512;
constexpr unsigned OFF_EP = OFF_F2 + 1024;
constexpr unsigned OFF_STAMP = 64u << 20;
constexpr unsigned OFF_WQ = 1u << 20;
constexpr unsigned OFF_WO = OFF_WQ + 32 * 81920;
constexpr unsigned OFF_WG = OFF_WO + 16 * 51200;
constexpr unsigned OFF_WD = OFF_WG + 32 * 122880;
constexpr unsigned XA_T = 40960, XA_SZ = 64 * XA_T;
constexpr unsigned OFF_XA = OFF_WD + 16 * 122880;
constexpr unsigned ST_SZ = 64 * 4096;
constexpr unsigned OFF_ST = OFF_XA + 4 * XA_SZ;
constexpr unsigned OA_T = 20480, OA_SZ = 128 * OA_T;
constexpr unsigned OFF_OA = OFF_ST + 4 * ST_SZ;
constexpr unsigned HA_T = 49152, HA_SZ = 128 * HA_T;
constexpr unsigned OFF_HA = OFF_OA + 2 * OA_SZ;
constexpr unsigned KVH_T = 3072, KVH_SZ = 8 * 32 * 8 * KVH_T;
constexpr unsigned OFF_KVH = OFF_HA + 2 * HA_SZ;
constexpr unsigned OFF_CSQ = OFF_KVH + 2 * KVH_SZ;
constexpr unsigned OFF_CSG = OFF_CSQ + 32 * 40 * 256;
constexpr unsigned WS_USED = OFF_CSG + 32 * 40 * 512;
static_assert(OFF_EP + 256 <= OFF_WQ, "");

constexpr int UNIT = 10240;
constexpr int MISC = 0, ARENA = 2048;
constexpr int KV_OFF = ARENA + 8 * UNIT, KV_T = 3072;
constexpr int L_PROG = MISC, L_LANDED = MISC + 64, L_RDY = MISC + 68, L_CSUM = MISC + 76;
constexpr int L_TAG = MISC + 80, L_TAGV = MISC + 84, L_XRDY = MISC + 88, L_STG = MISC + 92, L_WQOK = MISC + 96, L_W2OK = MISC + 100, L_CRIT = MISC + 104;
constexpr int STG = ARENA + 8 * UNIT;
constexpr int STG_X = STG + 61440;
constexpr int L_CQ = MISC + 256, L_CG = MISC + 768, L_STAMP = ARENA + 15 * UNIT;
constexpr int LDS_BYTES = ARENA + 15 * UNIT + 4096;
#define LDEPTH 2
#define WQ_ 12
#define WG_ 10
#define WO_ 10
#define WD_ 10
#define PRIO_H 0
#define PHPRIO 438
#define PP(p) do { if (PHPRIO) __builtin_amdgcn_s_setprio(p); } while (0)
#define PRETOUCH 0
#define L2GATE 0
#define DRYPASS 0
#define HINT 0
#define S2GATE 4
#define F2GATE 3u
#define ANN 0
#define TURN0 3
constexpr int NFILL = 37, NSTAGE = 38;
constexpr unsigned SPIN_CAP = 1u << 18;

struct TabQ { static constexpr int u(int i) { return i; } };
struct TabO { static constexpr int u(int i) { return i + 1; } };
struct TabG { static constexpr int u(int i) { constexpr int t[12] = {13, 14, 0, 6, 7, 8, 9, 10, 11, 12, 1, 2}; return t[i]; } };
struct TabD { static constexpr int u(int i) { constexpr int t[12] = {3, 4, 5, 13, 14, 6, 7, 8, 9, 10, 11, 12}; return t[i]; } };

template <int V> struct IC { static constexpr int v = V; };
template <int I, int N, class F> DI void sfor(F&& f) { if constexpr (I < N) { f(IC<I>{}); sfor<I + 1, N>(f); } }
DI int fresh_lane() { int l; asm volatile("v_mbcnt_lo_u32_b32 %0, -1, 0\n\tv_mbcnt_hi_u32_b32 %0, -1, %0" : "=v"(l)); return l; }
DI int opq(int v) { asm volatile("" : "+s"(v)); return v; }
DI float asf(unsigned u) { return __uint_as_float(u); }
DI unsigned asu(float f) { return __float_as_uint(f); }
DI unsigned pk(float a, float b) { f32x2 t = {a, b}; bf2 p = __builtin_convertvector(t, bf2); return __builtin_bit_cast(unsigned, p); }
DI float rbf(float a) { return asf(pk(a, a) << 16); }
DI float ex2(float a) { return __builtin_amdgcn_exp2f(a); }
DI float dot2(unsigned a, unsigned b, float c) { return __builtin_amdgcn_fdot2_f32_bf16(__builtin_bit_cast(bf2, a), __builtin_bit_cast(bf2, b), c, false); }
DI f32x16 mfma32(v4u a, v4u b, f32x16 c) { return __builtin_amdgcn_mfma_f32_32x32x16_bf16(__builtin_bit_cast(s16x8, a), __builtin_bit_cast(s16x8, b), c, 0, 0, 0); }
DI f32x16 mfma32k8(v2u a, v2u b, f32x16 c) { return __builtin_amdgcn_mfma_f32_32x32x8bf16_1k(__builtin_bit_cast(s16x4, a), __builtin_bit_cast(s16x4, b), c, 0, 0, 0); }
DI f32x4 mfma16(v4u a, v4u b, f32x4 c) { return __builtin_amdgcn_mfma_f32_16x16x32_bf16(__builtin_bit_cast(s16x8, a), __builtin_bit_cast(s16x8, b), c, 0, 0, 0); }
DI void mfma16i(f32x4& c, v4u a, v4u b) { asm volatile("v_mfma_f32_16x16x32_bf16 %0, %1, %2, %0" : "+v"(c) : "v"(a), "v"(b)); }
DI void mfma_fence(f32x4& a, f32x4& b, f32x4& c) { asm volatile("s_nop 7" : "+v"(a), "+v"(b), "+v"(c)); }
DI unsigned ufl(unsigned v) { return __builtin_amdgcn_readfirstlane(v); }
DI unsigned lds_ld(unsigned a) { return __hip_atomic_load((LAS unsigned*)a, RLX_WG); }
DI void lds_st(unsigned a, unsigned v) { __hip_atomic_store((LAS unsigned*)a, v, RLX_WG); }
DI void lds_or1(unsigned a, unsigned v) { asm volatile("s_mov_b64 exec, 1\n\tds_or_b32 %0, %1\n\ts_mov_b64 exec, -1" :: "v"(a), "v"(v) : "memory"); }
DI v4u lds128(unsigned a) { return *(LAS v4u*)a; }
DI v2u lds64(unsigned a) { return *(LAS v2u*)a; }
DI v2u ldstr(unsigned a) { return __builtin_bit_cast(v2u, __builtin_amdgcn_ds_read_tr16_b64_v4i16((LAS s16x4*)a)); }
DI float sum_halves(float x) { auto r = __builtin_amdgcn_permlane32_swap(asu(x), asu(x), false, false); unsigned a = r[0], b = r[1]; return asf(a) + asf(b); }
DI float max_halves(float x) { auto r = __builtin_amdgcn_permlane32_swap(asu(x), asu(x), false, false); unsigned a = r[0], b = r[1]; return fmaxf(asf(a), asf(b)); }
DI float sum_rows16(float x) { auto r = __builtin_amdgcn_permlane16_swap(asu(x), asu(x), false, false); unsigned a = r[0], b = r[1]; return asf(a) + asf(b); }
DI float ror8(float x) { return asf(__builtin_amdgcn_update_dpp(0u, asu(x), 0x128, 0xf, 0xf, false)); }
DI unsigned st_line(int c, int hf) { return ((c >> 2) * 8 + ((c >> 1) & 1) * 4 + hf * 2 + (c & 1)) * 128; }
DI float max3(float a, float b, float c) { return __builtin_elementwise_maximum(__builtin_elementwise_maximum(a, b), c); }

typedef __amdgpu_buffer_rsrc_t rsrc_t;
constexpr int SC1 = 16;
DI v4u gld128(rsrc_t r, unsigned v, unsigned s) { return __builtin_amdgcn_raw_buffer_load_b128(r, v, s, SC1); }
DI v2u gld64(rsrc_t r, unsigned v, unsigned s) { return __builtin_amdgcn_raw_buffer_load_b64(r, v, s, SC1); }
DI void gst128(v4u x, rsrc_t r, unsigned v, unsigned s) { __builtin_amdgcn_raw_buffer_store_b128(x, r, v + s, 0, SC1); }
DI void gst64(v2u x, rsrc_t r, unsigned v, unsigned s) { __builtin_amdgcn_raw_buffer_store_b64(x, r, v, s, SC1); }
template <int S> DI v4u lda(rsrc_t r, unsigned voff, unsigned base) { return gld128(r, voff + (S & 3) * 1024, base + (S >> 2) * 4096); }

#define STAMP_HI (SPIN_CAP - rg.bud)
#define STAMP(i) do {} while (0)
#define INNER 1
#define STAMP1(i) do { if (INNER == 1) STAMP(i); } while (0)
#define STAMP2(i) do { if (INNER == 2) STAMP(i); } while (0)
#define STAMP4(i) do { if (INNER == 4) STAMP(i); } while (0)
#define STAMP3(i) do { if (INNER == 3 && rg.ls == 0) STAMP(i); } while (0)

#define SPEND(bud) { if ((bud) >= SPIN_CAP) break; ++(bud); }
DI unsigned lds_wait_gt(unsigned a, unsigned f, unsigned& bud) {
  unsigned v;
  for (;;) {
    v = lds_ld(a);
    if (ufl(v) > f) break;
    SPEND(bud);
    __builtin_amdgcn_s_sleep(1);
  }
  CBAR();
  return v;
}
DI void lds_wait_bits(unsigned a, unsigned bits, unsigned& cache, unsigned& bud) {
  if (__builtin_expect((cache & bits) == bits, 1)) return;
  for (;;) {
    cache = ufl(lds_ld(a));
    if ((cache & bits) == bits) break;
    SPEND(bud);
    __builtin_amdgcn_s_sleep(1);
  }
  CBAR();
}
DI void prog_wait(unsigned need, unsigned& bud) {
  const int lane = fresh_lane();
  for (;;) {
    unsigned v = lds_ld(L_PROG + (lane & 7) * 4);
    if (__all(v >= need)) break;
    SPEND(bud);
    __builtin_amdgcn_s_sleep(1);
  }
  CBAR();
}
DI unsigned rmw_poll(GAS unsigned* p) {
  unsigned f;
  asm volatile("s_mov_b64 exec, 1\n\ts_nop 3\n\tglobal_atomic_or %0, %1, %1, %2 sc0\n\ts_waitcnt vmcnt(0)\n\ts_mov_b64 exec, -1" : "=&v"(f) : "v"(0u), "s"(p) : "memory");
  return ufl(f);
}
DI unsigned rmw_poll_lanes(GAS unsigned* p, unsigned voff, unsigned mlo, unsigned mhi, unsigned init) {
  asm volatile("s_mov_b32 exec_lo, %3\n\ts_mov_b32 exec_hi, %4\n\ts_nop 3\n\tglobal_atomic_or %0, %1, %5, %2 sc0\n\ts_waitcnt vmcnt(0)\n\ts_mov_b64 exec, -1"
               : "+v"(init) : "v"(voff), "s"(p), "s"(mlo), "s"(mhi), "v"(0u) : "memory");
  return init;
}
DI bool rmw_poll_pairs(GAS unsigned* p, unsigned voff, unsigned mlo, unsigned tag) {
  v2u f = {0u, 0u};
  asm volatile("s_mov_b32 exec_lo, %3\n\ts_mov_b32 exec_hi, 0\n\ts_nop 3\n\tglobal_atomic_or_x2 %0, %1, %4, %2 sc0\n\ts_waitcnt vmcnt(0)\n\ts_mov_b64 exec, -1"
               : "+v"(f) : "v"(voff), "s"(p), "s"(mlo), "v"(v2u{0u, 0u}) : "memory");
  return f[0] == tag && f[1] == tag;
}
DI void gst32(unsigned x, rsrc_t r, unsigned v) { __builtin_amdgcn_raw_buffer_store_b32(x, r, v, 0, SC1); }
#define DRAIN() asm volatile("s_waitcnt vmcnt(0)" ::: "memory")
#define WG_BARRIER() do { asm volatile("s_waitcnt lgkmcnt(0)" ::: "memory"); __builtin_amdgcn_s_barrier(); CBAR(); } while (0)
DI void ctr_wait(unsigned char* ws, int kind, int t32, int rep, unsigned& bud) {
  GAS unsigned* p = (GAS unsigned*)(ws + OFF_CTR + (kind * 64 + t32) * 1024 + rep * 256);
  for (;;) {
    if (rmw_poll(p) >= 32u) break;
    SPEND(bud);
  }
  __builtin_amdgcn_fence(__ATOMIC_ACQUIRE, "wavefront");
  CBAR();
}
DI void ctr_arrive(unsigned char* ws, int kind, int t32) {
  const int lane = fresh_lane();
  GAS unsigned* p = (GAS unsigned*)(ws + OFF_CTR + (kind * 64 + t32) * 1024);
  if (!HINT) DRAIN();
  asm volatile("s_mov_b64 exec, 15\n\ts_nop 3\n\tglobal_atomic_add %0, %1, %2\n\ts_mov_b64 exec, -1" :: "v"(lane * 256), "v"(1u), "s"(p) : "memory");
}

DI void dma_fill(v4u srd, unsigned voff, unsigned soff, unsigned ldst) {
  unsigned keep, so;
  asm volatile(
      "s_mov_b32 %0, m0\n\ts_mov_b32 m0, %5\n\ts_add_u32 %1, %4, 0x1000\n\t"
      "buffer_load_dwordx4 %2, %3, %4 offen sc1 lds\n\tbuffer_load_dwordx4 %2, %3, %4 offen offset:1024 sc1 lds\n\t"
      "buffer_load_dwordx4 %2, %3, %4 offen offset:2048 sc1 lds\n\tbuffer_load_dwordx4 %2, %3, %4 offen offset:3072 sc1 lds\n\t"
      "s_add_u32 m0, m0, 0x1000\n\ts_nop 0\n\t"
      "buffer_load_dwordx4 %2, %3, %1 offen sc1 lds\n\tbuffer_load_dwordx4 %2, %3, %1 offen offset:1024 sc1 lds\n\t"
      "buffer_load_dwordx4 %2, %3, %1 offen offset:2048 sc1 lds\n\tbuffer_load_dwordx4 %2, %3, %1 offen offset:3072 sc1 lds\n\t"
      "s_add_u32 m0, m0, 0x1000\n\ts_add_u32 %1, %1, 0x1000\n\t"
      "buffer_load_dwordx4 %2, %3, %1 offen sc1 lds\n\tbuffer_load_dwordx4 %2, %3, %1 offen offset:1024 sc1 lds\n\t"
      "s_mov_b32 m0, %0"
      : "=&s"(keep), "=&s"(so) : "v"(voff), "s"(srd), "s"(soff), "s"(ldst) : "memory", "scc");
}

DI void dma_raw(const unsigned char* base, unsigned bytes, unsigned voff, unsigned soff, unsigned sstep, unsigned ldst, int n) {
  const v4u srd = {(unsigned)(uintptr_t)base, (unsigned)((uintptr_t)base >> 32) & 0xffffu, bytes, 0x00020000u};
#pragma unroll 1
  for (int i = 0; i < n; ++i) {
    unsigned keep;
    asm volatile("s_mov_b32 %0, m0\n\ts_mov_b32 m0, %4\n\ts_nop 0\n\tbuffer_load_dwordx4 %1, %2, %3 offen lds\n\ts_mov_b32 m0, %0"
                 : "=&s"(keep) : "v"(voff), "s"(srd), "s"(soff + i * sstep), "s"(ldst + i * 1024) : "memory");
  }
}
DI unsigned lds_inc1(unsigned a) {
  unsigned r;
  asm volatile("s_mov_b64 exec, 1\n\tds_add_rtn_u32 %0, %1, %2\n\ts_waitcnt lgkmcnt(0)\n\ts_mov_b64 exec, -1" : "=&v"(r) : "v"(a), "v"(1u) : "memory");
  return ufl(r);
}
DI void lds_add1(unsigned a) { asm volatile("s_mov_b64 exec, 1\n\tds_add_u32 %0, %1\n\ts_mov_b64 exec, -1" :: "v"(a), "v"(1u) : "memory"); }

constexpr unsigned SENT = 0xffffffffu;
typedef unsigned long long u64;
DI u64 stale2(v4u x, v4u y) {
  const unsigned a = x[0], b = x[2], c = y[0], d = y[2];
  u64 m = __ballot(max(max(max(a, b), c), d) == SENT);
  asm volatile("" : "+s"(m));
  return m;
}
template <int F> DI void fresh2(u64 bad, v4u& x, v4u& y, __amdgpu_buffer_rsrc_t ws, unsigned voff, unsigned base, unsigned& bud) {
  if (__builtin_expect(bad != 0, 0)) {
    for (;;) {
      CBAR();
      x = lda<F>(ws, voff, base); y = lda<F + 1>(ws, voff, base);
      if (!stale2(x, y)) break;
      SPEND(bud);
    }
  }
}

struct Ring {
  unsigned lv, lvs;
  unsigned lf, ls;
  unsigned prog;
  unsigned bud;
};
DI void ring_check(Ring& r, int f) {
  if (__builtin_expect(r.lvs <= r.lf + f, 0)) lds_wait_gt(L_LANDED, r.lf + f, r.bud);
  CBAR();
}
DI void ring_latch(Ring& r) { r.lvs = opq(ufl(r.lv)); }
DI void ring_peek(Ring& r) { r.lv = lds_ld(L_LANDED); }
constexpr int BSTEP = 61440;

template <int NB, int W, int DIST, int F0, int S0, class Tab, int CS, class Wait>
DI void gemm32(f32x16 (&acc)[NB], float& rs, Ring& rg, rsrc_t ws, unsigned abase, unsigned stbase, Wait&& wait) {
  const int lane = fresh_lane();
  const unsigned voff = lane * 16;
  v4u a[W], b[DIST + 1][NB];
  unsigned lb[3];
#pragma unroll
  for (int i = 0; i < 3; ++i) { lb[i] = voff + i * BSTEP; asm volatile("" : "+v"(lb[i])); }
  unsigned pa = rg.prog, pv = rg.ls + S0;
  asm volatile("" : "+v"(pa), "+v"(pv));
  v2u cf[NB], xm;
  v4u st[4];
  float s_ = 0.f, q_ = 0.f, s1_ = 0.f, q1_ = 0.f;
  unsigned csv = 0;
  u64 bs0 = 0, bs1 = 0;
  auto readB = [&](auto P) INL {
    constexpr int ph = P.v;
    if constexpr (ph < 40 * NB) {
      constexpr int ad = ARENA + Tab::u(ph / 10) * UNIT + (ph % 10) * 1024;
      b[(ph / NB) % (DIST + 1)][ph % NB] = lds128(lb[ad / BSTEP] + ad % BSTEP);
    }
    if constexpr (ph % 10 == 3 && ph + 7 < 40 * NB) ring_peek(rg);
    if constexpr (ph % 10 == 7 && ph + 3 < 40 * NB) ring_latch(rg);
    if constexpr (ph % 10 == 9 && ph + 1 < 40 * NB) ring_check(rg, F0 + ph / 10 + 1);
    if constexpr (ph % 10 == 0 && ph > 0 && ph <= 40 * NB) { CBAR(); ++pv; lds_st(pa, pv); CBAR(); }
  };
  wait();
  sfor<0, W>([&](auto S) INL { a[S.v] = lda<S.v>(ws, voff, abase); });
  SCHED();
  ring_peek(rg);
  ring_latch(rg);
  ring_check(rg, F0);
  sfor<0, DIST * NB>(readB);
  u64 bad = 0;
  if (HINT == 1) fresh2<0>(stale2(a[0], a[1]), a[0], a[1], ws, voff, abase, rg.bud);
  SCHED();
  constexpr int X1 = 40 * NB - 14;
  constexpr int X0 = X1 - 28;
  sfor<0, 40 * NB>([&](auto SL) INL {
    constexpr int sl = SL.v, s = sl / NB, t = sl % NB;
    if constexpr (s == 0) { f32x16 z; _Pragma("unroll") for (int r = 0; r < 16; ++r) z[r] = 0.f; acc[t] = mfma32(b[0][t], a[0], z); }
    else acc[t] = mfma32(b[s % (DIST + 1)][t], a[s % W], acc[t]);
    SCHED();
    readB(IC<sl + DIST * NB>{});
    if constexpr (t == NB - 1 && s + W < 40) a[s % W] = lda<s + W>(ws, voff, abase);
    if constexpr (HINT == 1 && t == NB - 1 && (s & 1) && s + 2 < 40) fresh2<s + 1>(bad, a[(s + 1) % W], a[(s + 2) % W], ws, voff, abase, rg.bud);
    if constexpr (HINT == 1 && t == NB - 2 && (s & 1) && s + 2 < 40) bad = stale2(a[(s + 1) % W], a[(s + 2) % W]);
    if constexpr (HINT == 1 && sl == X1 - 5) bs0 = stale2(st[0], st[1]);
    if constexpr (HINT == 1 && sl == X1 - 4) bs1 = stale2(st[2], st[3]);
    if constexpr (HINT == 1 && sl == X1 - 2) fresh2<0>(bs0, st[0], st[1], ws, voff, stbase, rg.bud);
    if constexpr (HINT == 1 && sl == X1 - 1) fresh2<2>(bs1, st[2], st[3], ws, voff, stbase, rg.bud);
    if constexpr (sl >= X0 && sl < X0 + 8 && (sl - X0) % 2 == 0) st[(sl - X0) / 2] = lda<(sl - X0) / 2>(ws, voff, stbase);
    if constexpr (sl == X1 - 10) csv = lds_ld(L_CSUM);
    if constexpr (sl == X1 - 6) csv = opq(ufl(csv));
    if constexpr (sl == X1 - 3) { if (__builtin_expect(ufl(csv) < NB - 1, 0)) lds_wait_gt(L_CSUM, NB - 2, rg.bud); CBAR(); }
    if constexpr (sl >= X1 && sl < X1 + 3) {
      constexpr int i = sl - X1 + 1;
      const unsigned u0 = st[i][0], u1 = st[i][1], u2 = st[i][2], u3 = st[i][3];
      if constexpr (i == 1) { const unsigned w0 = st[0][0], w1 = st[0][1], w2 = st[0][2], w3 = st[0][3]; s_ = asf(w0); q_ = asf(w1); s1_ = asf(w2); q1_ = asf(w3); }
      s_ += asf(u0); q_ += asf(u1); s1_ += asf(u2); q1_ += asf(u3);
    }
    if constexpr (sl == X1 + 3) { const bool up = lane & 8; s_ = (up ? s1_ : s_) + ror8(up ? s_ : s1_); }
    if constexpr (sl == X1 + 4) { const bool up = lane & 8; q_ = (up ? q1_ : q_) + ror8(up ? q_ : q1_); }
    if constexpr (sl == X1 + 5) s_ = sum_halves(s_) * (1.f / 640.f);
    if constexpr (sl == X1 + 6) q_ = sum_halves(q_) * (1.f / 640.f);
    if constexpr (sl == X1 + 7) {
      q_ = fmaxf(q_ - s_ * s_, 0.f);
      const unsigned nm = pk(-s_, -s_);
      xm[0] = lane < 32 ? nm : 0u; xm[1] = lane < 32 ? (nm & 0xffffu) : 0u;
    }
    if constexpr (sl == X1 + 8) {
      q_ = rbf(rbf(q_) + 1e-5f);
      sfor<0, NB>([&](auto T) INL { cf[T.v] = lds64(CS + T.v * 256 + (lane & 31) * 8); });
    }
    if constexpr (sl == X1 + 9) rs = __builtin_amdgcn_rcpf(rbf(__builtin_amdgcn_sqrtf(q_)));
    SCHED();
  });
  sfor<0, NB>([&](auto T) INL { acc[T.v] = mfma32k8(cf[T.v], xm, acc[T.v]); });
}

template <int NS, int W, int DIST, int F0, int S0, class Tab, class Wait>
DI void gemm16(Ring& rg, rsrc_t ws, unsigned abase, unsigned (&res)[6], unsigned xbase, unsigned stbase, bool last, void* out, int t16i, int ct, Wait&& wait) {
  const int lane = fresh_lane();
  const unsigned voff = lane * 16;
  v4u a[W], b[DIST + 1][3];
  unsigned lb[3], lc[3];
  const int g4 = lane >> 4;
#pragma unroll
  for (int i = 0; i < 3; ++i) {
    lb[i] = voff + i * BSTEP; asm volatile("" : "+v"(lb[i]));
    lc[i] = g4 * 128 + (lane & 7) * 16 + i * BSTEP + ((lane & 8) ? 0x40000 : 0); asm volatile("" : "+v"(lc[i]));
  }
  unsigned pa = rg.prog, pv = rg.ls + S0;
  asm volatile("" : "+v"(pa), "+v"(pv));
  f32x4 acc[3];
#pragma unroll
  for (int t = 0; t < 3; ++t) acc[t] = f32x4{asf(res[2 * t] << 16), asf(res[2 * t] & 0xffff0000u), asf(res[2 * t + 1] << 16), asf(res[2 * t + 1] & 0xffff0000u)};
  auto readB = [&](auto S, auto T) INL {
    constexpr int s = S.v, t = T.v;
    if constexpr (s < NS) {
      constexpr int ad = ARENA + Tab::u(s / 4) * UNIT + (s % 4) * 2560;
      b[s % (DIST + 1)][t] = lds128((t < 2 ? lb : lc)[ad / BSTEP] + ad % BSTEP + t * 1024);
    }
    if constexpr (t == 0 && s % 4 == 1 && s + 3 < NS) ring_peek(rg);
    if constexpr (t == 0 && s % 4 == 3 && s + 1 < NS) ring_latch(rg);
    if constexpr (t == 2 && s % 4 == 3 && s + 1 < NS) ring_check(rg, F0 + s / 4 + 1);
    if constexpr (t == 0 && s % 4 == 0 && s > 0 && s <= NS) { CBAR(); ++pv; lds_st(pa, pv); CBAR(); }
  };
  const unsigned t32 = (t16i & 1) * 16 + (lane & 15);
  const unsigned pi = 2 * (g4 & 1) + (g4 >> 1);
  unsigned so1 = xbase + (5 * ct) * 512 + pi * 512 + t32 * 16, so2 = xbase + (5 * ct + 4) * 512 + t32 * 16 + (lane < 16 ? 0u : WS_USED);
  unsigned so3 = stbase + st_line(ct, t16i & 1) + (lane & 7) * 16 + (lane & 8) + (lane < 16 ? 0u : WS_USED);
  asm volatile("" : "+v"(so1), "+v"(so2), "+v"(so3), "+v"(acc[0]), "+v"(acc[1]), "+v"(acc[2]));
  if (HINT && HINT < 3 && !last) { const v4u sv = {SENT, SENT, SENT, SENT}; gst128(sv, ws, so1, 0); gst128(sv, ws, so2, 0); gst64(v2u{SENT, SENT}, ws, so3, 0); }
  wait();
  sfor<0, W>([&](auto S) INL { a[S.v] = lda<S.v>(ws, voff, abase); });
  SCHED();
  ring_peek(rg);
  ring_latch(rg);
  ring_check(rg, F0);
  sfor<0, DIST * 3>([&](auto I) INL { readB(IC<I.v / 3>{}, IC<I.v % 3>{}); });
  u64 bad = 0;
  if (HINT == 1) fresh2<0>(stale2(a[0], a[1]), a[0], a[1], ws, voff, abase, rg.bud);
  SCHED();
  if constexpr (NS == 48) { STAMP1(27); }
  sfor<0, NS>([&](auto S) INL {
    constexpr int s = S.v;
    if constexpr (NS == 48 && s % 12 == 1) STAMP3(24 + s / 12);
    sfor<0, 3>([&](auto T) INL {
      constexpr int t = 2 - T.v;
      mfma16i(acc[t], b[s % (DIST + 1)][t], a[s % W]);
      SCHED();
      readB(IC<s + DIST>{}, T);
      if constexpr (T.v == 2 && s + W < NS) a[s % W] = lda<s + W>(ws, voff, abase);
      if constexpr (HINT == 1 && T.v == 0 && (s & 1) && s + 2 < NS) bad = stale2(a[(s + 1) % W], a[(s + 2) % W]);
      if constexpr (HINT == 1 && T.v == 2 && (s & 1) && s + 2 < NS) fresh2<s + 1>(bad, a[(s + 1) % W], a[(s + 2) % W], ws, voff, abase, rg.bud);
      SCHED();
    });
  });
  mfma_fence(acc[0], acc[1], acc[2]);
  if constexpr (NS == 48) { STAMP1(28); STAMP3(28); }
#pragma unroll
  for (int t = 0; t < 3; ++t) { res[2 * t] = pk(acc[t][0], acc[t][1]); res[2 * t + 1] = pk(acc[t][2], acc[t][3]); }
  auto s0 = __builtin_amdgcn_permlane16_swap(res[0], res[2], false, false);
  auto s1 = __builtin_amdgcn_permlane16_swap(res[1], res[3], false, false);
  auto s2 = __builtin_amdgcn_permlane16_swap(res[4], res[4], false, false);
  auto s3 = __builtin_amdgcn_permlane16_swap(res[5], res[5], false, false);
  const v4u o01 = {s0[0], s1[0], s0[1], s1[1]};
  const v4u o2 = {s2[0], s3[0], s2[1], s3[1]};
  if (last) {
    GAS char* ob = (GAS char*)out + (size_t)(t16i * 16 + (lane & 15)) * 1280 + ct * 80;
    *(GAS v4u*)(ob + pi * 16) = o01;
    if (lane < 16) *(GAS v4u*)(ob + 64) = o2;
  } else {
    gst128(o01, ws, so1, 0);
    gst128(o2, ws, so2, 0);
    const f32x4 sv = acc[0] + acc[1] + acc[2], qv = acc[0] * acc[0] + acc[1] * acc[1] + acc[2] * acc[2];
    float s = (sv[0] + sv[1]) + (sv[2] + sv[3]), q = (qv[0] + qv[1]) + (qv[2] + qv[3]);
    s = sum_halves(sum_rows16(s));
    q = sum_halves(sum_rows16(q));
    gst64(v2u{asu(s), asu(q)}, ws, so3, 0);
  }
}

constexpr float QSCALE = 0.22360679774997896f * 1.4426950408889634f;
constexpr float NEG = -1e30f;

DI float max16(const f32x16& s) {
  const float a = max3(s[0], s[1], s[2]), b = max3(s[3], s[4], s[5]), c = max3(s[6], s[7], s[8]);
  const float d = max3(s[9], s[10], s[11]), e = max3(s[12], s[13], s[14]);
  return __builtin_elementwise_maximum(max3(a, b, c), max3(d, e, s[15]));
}
DI void kv_write(unsigned kb, int lane, v4u k1, v4u ta, v4u tb) {
  *(LAS v4u*)(kb + lane * 16) = k1;
  *(LAS v2u*)(kb + 1024 + (lane ^ 32) * 8) = v2u{ta[0], ta[1]};
  const unsigned vb = kb + 1536 + (lane & 31) * 48 + (lane >> 5) * 8;
  *(LAS v2u*)(vb) = v2u{ta[2], ta[3]};
  *(LAS v2u*)(vb + 16) = v2u{tb[0], tb[1]};
  *(LAS v2u*)(vb + 32) = v2u{tb[2], tb[3]};
}
constexpr float MB = 16.f;

DI int kmapO4(int P, int t) {
  const int p = P / 5, r = P - 5 * p;
  if (r == 2) return 20 * (2 * p + t) + 16;
  return 20 * (2 * p + (r > 2)) + 8 * t + 4 * (r > 2 ? r - 3 : r);
}
DI v4u tr_frag(unsigned a0, unsigned a1) { const v2u x = ldstr(a0), y = ldstr(a1); return v4u{x[0], x[1], y[0], y[1]}; }
DI float sum8(v4u f, float s) {
#pragma unroll
  for (int i = 0; i < 4; ++i) { const unsigned u = f[i]; s = dot2(u, 0x3f803f80u, s); }
  return s;
}

DI void csum_rows(f32x4 c, unsigned la) {
  unsigned o[8];
#pragma unroll
  for (int i = 0; i < 4; ++i) {
    const float v = c[i], hi = rbf(v), mid = rbf(v - hi), lo = rbf(v - hi - mid);
    o[2 * i] = pk(hi, mid); o[2 * i + 1] = pk(lo, 0.f);
  }
  *(LAS v4u*)la = v4u{o[0], o[1], o[2], o[3]};
  *(LAS v4u*)(la + 16) = v4u{o[4], o[5], o[6], o[7]};
}

__global__ void __launch_bounds__(768) mega_kernel(unsigned char* wsp, void* out, const unsigned char* xin, const unsigned char* wqkv, const unsigned char* wo,
                                                   const unsigned char* wg, const unsigned char* wu, const unsigned char* wd) {
  __shared__ __attribute__((aligned(16))) char smem[LDS_BYTES];
  const int wave = ufl(threadIdx.x >> 6);
  asm volatile("" :: "v"((LAS char*)smem) : "memory");
  const int bx = blockIdx.x & 7, br = blockIdx.x >> 3;
#define GMAP 5
  const int band = (bx & 4) + (br >> 3), j = (bx & 3) * 8 + (br & 7);
  const int ct = (j >> 4) * 8 + (j & 7), rsel = (j >> 3) & 1;
  const rsrc_t ws = __builtin_amdgcn_make_buffer_rsrc(wsp, 0, WS_USED, 0x00020000);
  constexpr unsigned OOR = 0x40000000u;
  if (wave == 0) lds_st(MISC + (fresh_lane() & 31) * 4, 0u);
  WG_BARRIER();
  const int tk = (wave - 4) * 256 + bx * 32 + br;
  const unsigned sb = wave < 4 ? STG_X + (wave - 1) * 5120 : wave < 6 ? STG + (wave - 4) * 6144 : STG - 4096 + (wave - 4) * 8192;
  {
    const int lane = fresh_lane();
    const bool isx = wave < 4;
    dma_raw(isx ? xin : wqkv, isx ? 2048 * 1280 : 640 * 3840, isx || (lane & 7) < 5 ? (lane >> 3) * 1280 + (lane & 7) * 16 : OOR,
            isx ? (band * 256 + j * 8) * 1280 + (wave - 1) * 640 : (tk >> 4) * (16 * 3840) + (tk & 15) * 80, isx ? 128 : 10240, sb,
            opq(isx ? (wave == 1 || wave == 2 ? 5 : 0) : tk < 640 ? 6 : 0));
  }

  if (wave == 0) {
    const int lane = fresh_lane();
    __builtin_amdgcn_s_setprio(3);
    const v4u wsd = {(unsigned)(uintptr_t)wsp, (unsigned)((uintptr_t)wsp >> 32) & 0xffffu, WS_USED, 0x00020000u};
    unsigned bud = 0;
    const unsigned tag = ufl(__builtin_amdgcn_raw_buffer_load_b32(ws, 0, OFF_EP, SC1)) + 1u;
    lds_st(L_TAG, tag);
    CBAR();
    lds_st(L_TAGV, 1u);
    for (;;) {
      if (((unsigned)__ballot(rmw_poll_pairs((GAS unsigned*)(wsp + OFF_IN + (band * 32 + j) * 512), lane * 8, 0xfffffu, tag)) & 0xfffffu) == 0xfffffu) break;
      SPEND(bud);
    }
    lds_st(L_WQOK, 1u);
#pragma unroll 1
    for (int f = 0; f < 6; ++f) dma_fill(wsd, lane * 16, OFF_WQ + j * 81920 + f * UNIT, ARENA + f * UNIT);
    asm volatile("s_waitcnt vmcnt(50)" ::: "memory"); lds_st(L_LANDED, 1u);
    dma_fill(wsd, lane * 16, OFF_WQ + j * 81920 + 6 * UNIT, ARENA + 6 * UNIT);
    asm volatile("s_waitcnt vmcnt(50)" ::: "memory"); lds_st(L_LANDED, 2u);
    dma_fill(wsd, lane * 16, OFF_WQ + j * 81920 + 7 * UNIT, ARENA + 7 * UNIT);
    asm volatile("s_waitcnt vmcnt(50)" ::: "memory"); lds_st(L_LANDED, 3u);
    asm volatile("s_waitcnt vmcnt(40)" ::: "memory"); lds_st(L_LANDED, 4u);
    asm volatile("s_waitcnt vmcnt(30)" ::: "memory"); lds_st(L_LANDED, 5u);
    asm volatile("s_waitcnt vmcnt(20)" ::: "memory"); lds_st(L_LANDED, 6u);
    asm volatile("s_waitcnt vmcnt(10)" ::: "memory"); lds_st(L_LANDED, 7u);
    asm volatile("s_waitcnt vmcnt(0)" ::: "memory"); lds_st(L_LANDED, 8u);
    __builtin_amdgcn_s_setprio(PRIO_H);
    lds_wait_gt(L_W2OK, 0u, bud);
    unsigned issued = 8, published = 8;
#pragma unroll 1
    for (int lp = 1; lp < 8; ++lp) {
      const int L = lp >> 2, ph = lp & 3;
      unsigned long long units, needs_lo, needs_hi; int n; unsigned src;
      if (ph == 0) { units = 0x76543210ull; n = 8; src = OFF_WQ + j * 81920;
        needs_lo = 17ull | 25ull << 8 | 26ull << 16 | 27ull << 24 | 28ull << 32 | 29ull << 40 | 32ull << 48 | 33ull << 56; needs_hi = 0; }
      else if (ph == 1) { units = 0x54321ull; n = 5; src = OFF_WO + ct * 51200;
        needs_lo = 40ull | 41ull << 8 | 42ull << 16 | 43ull << 24 | 44ull << 32; needs_hi = 0; }
      else if (ph == 2) { units = 0x21CBA98760EDull; n = 12; src = OFF_WG + j * 122880;
        needs_lo = 30ull | 31ull << 8 | 39ull << 16 | 45ull << 24 | 46ull << 32 | 47ull << 40 | 47ull << 48 | 47ull << 56;
        needs_hi = 47ull | 47ull << 8 | 48ull << 16 | 49ull << 24; }
      else { units = 0xCBA9876ED543ull; n = 12; src = OFF_WD + ct * 122880;
        needs_lo = 50ull | 51ull << 8 | 52ull << 16 | 53ull << 24 | 54ull << 32 | 56ull << 40 | 57ull << 48 | 58ull << 56;
        needs_hi = 59ull | 60ull << 8 | 61ull << 16 | 62ull << 24; }
#pragma unroll 1
      for (int f = 0; f < n; ++f) {
        const int unit = (int)((units >> (4 * f)) & 15ull);
        const int nd = (int)(((f < 8 ? needs_lo >> (8 * f) : needs_hi >> (8 * (f - 8)))) & 255ull) + (L - 1) * NSTAGE;
        if (nd > 0) {
          unsigned v = lds_ld(L_PROG + (lane & 7) * 4);
          if (!__all((int)v >= nd)) {
            if (published != issued) {
              if (issued - published >= 2) { asm volatile("s_waitcnt vmcnt(10)" ::: "memory"); lds_st(L_LANDED, issued - 1); }
              asm volatile("s_waitcnt vmcnt(0)" ::: "memory");
              published = issued; lds_st(L_LANDED, published);
            }
            prog_wait((unsigned)nd, bud);
          }
        }
        dma_fill(wsd, lane * 16, src + f * UNIT, ARENA + unit * UNIT);
        ++issued;
        if (issued - published > LDEPTH) {
          asm volatile("s_waitcnt vmcnt(%0)" :: "i"(10 * LDEPTH) : "memory");
          published = issued - LDEPTH; lds_st(L_LANDED, published);
        }
      }
    }
    asm volatile("s_waitcnt vmcnt(10)" ::: "memory");
    lds_st(L_LANDED, issued - 1);
    asm volatile("s_waitcnt vmcnt(0)" ::: "memory");
    lds_st(L_LANDED, issued);
    return;
  }

  if (wave >= 4) {
    const int cw = wave - 4, grp = cw >> 2, wi = cw & 3;
    const int qt = (1 - grp) * 4 + wi;
    const int t32_ = band * 8 + qt;
    const int t16_ = band * 16 + (1 - grp) * 8 + rsel * 4 + wi;
    const int rep = j & 3;
    Ring rg;
    rg.prog = L_PROG + cw * 4;
    rg.bud = 0;
#define PRIO_E 2
#define PRIO_L 1
    if (grp == 0) __builtin_amdgcn_s_setprio(PRIO_E); else __builtin_amdgcn_s_setprio(PRIO_L);
    const int dslot = qt + (qt < 4 ? 4 : 8);
    STAMP(30);
    STAMP(0);
    unsigned res[6] = {0u, 0u, 0u, 0u, 0u, 0u};
    unsigned tag;
    {
      const int lane = fresh_lane();
      const int gam = lane >> 4, q = (lane & 15) >> 2, p = lane & 3, hh = gam >> 1, g = (gam & 1) * 4 + p;
      const v4u ones = {0x3f803f80u, 0x3f803f80u, 0x3f803f80u, 0x3f803f80u};
      f32x16 z;
#pragma unroll
      for (int i = 0; i < 16; ++i) z[i] = 0.f;
      if (tk < 640) {
        const int hp = tk & 15, ks = tk >> 4;
        const unsigned r = sb + (8 * hh + q) * 384;
        unsigned c0 = r + (g < 4 ? 0 : 128) + (g & 3) * 8, c1 = r + (g == 0 ? 32 : g == 1 ? 128 + 32 : 256 + ((g < 7 ? g : 6) - 2) * 8);
        unsigned dst = OFF_WQ + hp * 163840 + ks * 2048 + lane * 16, cs = OFF_CSQ + (hp * 80 + ks) * 256 + lane * 4;
        unsigned fa = lane < 16 ? OFF_IN + ((lane >> 1) * 32 + 2 * hp + (lane & 1)) * 512 + ks * 4 : OOR;
        STAMP4(24);
#pragma unroll 1
        for (int pass = DRYPASS ? 0 : 1; pass < 2; ++pass) {
          const unsigned off = pass ? 0u : OOR;
          if (pass) DRAIN();
          STAMP4(25);
          v4u f[2][2];
#pragma unroll
          for (int h2 = 0; h2 < 2; ++h2) {
            f[h2][0] = tr_frag(c0 + h2 * 40, c0 + h2 * 40 + 1536);
            f[h2][1] = tr_frag(c1 + h2 * 40, c1 + h2 * 40 + 1536);
          }
#pragma unroll
          for (int h2 = 0; h2 < 2; ++h2) {
            if ((lane & 31) >= 28) f[h2][1] = v4u{0u, 0u, 0u, 0u};
            gst128(f[h2][0], ws, dst + off + h2 * 81920, 0); gst128(f[h2][1], ws, dst + off + h2 * 81920 + 1024, 0);
          }
#pragma unroll
          for (int h2 = 0; h2 < 2; ++h2) {
            const float s0 = mfma32(ones, f[h2][0], z)[0], s1 = mfma32(ones, f[h2][1], z)[0];
            gst32(asu(lane < 32 ? s0 : s1), ws, cs + off + h2 * 10240);
          }
          if (pass) lds_wait_gt(L_TAGV, 0u, rg.bud);
          tag = ufl(lds_ld(L_TAG));
          STAMP4(26);
          if (pass) DRAIN();
          STAMP4(27);
          gst32(tag, ws, fa + off);
        }
        lds_add1(L_CRIT);
      } else {
        const bool gu = tk < 1280, dn = tk < 1664;
        const int i = gu ? tk - 640 : dn ? tk - 1280 : tk - 1664;
        unsigned a0, a1, a2, a3, dst, o2;
        if (L2GATE) lds_wait_gt(L_CRIT, L2GATE - 1, rg.bud);
        if (gu) {
          const unsigned vo = (lane & 15) < 12 ? gam * 3072 + (lane & 15) * 16 : OOR, so = (i >> 4) * (16 * 3072) + (i & 15) * 192;
          dma_raw(wg, 640 * 3072, vo, so, 4 * 3072, sb, 4);
          dma_raw(wu, 640 * 3072, vo, so, 4 * 3072, sb + 4096, 4);
          const unsigned r = sb + (8 * hh + q) * 256;
          a0 = r + g * 8; a1 = a0 + 4096; a2 = r + 64 + (g & 3) * 8 + (g < 4 ? 0 : 4096); a3 = 96;
          dst = OFF_WG + (i & 15) * 245760 + (i >> 4) * 3072; o2 = 2048 + lane * 16;
        } else {
          const unsigned vo = (lane & 15) < 10 ? gam * 1280 + (lane & 15) * 16 : OOR;
          if (tk < 1824) {
#pragma unroll 1
            for (int u = 0; u < 8; ++u) {
              const int P = 4 * (i >> 3) + (u >> 1);
              const int row = dn ? 16 * (P >> 1) + 4 * (P & 1) + 8 * (u & 1) : kmapO4(P, u & 1);
              dma_raw(dn ? wd : wo, dn ? 1536 * 1280 : 640 * 1280, vo, row * 1280 + (i & 7) * 160, 0, sb + u * 1024, 1);
            }
          }
          const unsigned r = sb + gam * 2048 + q * 256;
          a0 = r + p * 8; a1 = a0 + 32; a2 = r + 64 + (p & 1) * 8; a3 = 80;
          dst = tk >= 1824 ? OOR : dn ? OFF_WD + (i & 7) * 245760 + (i >> 3) * 2560 : OFF_WO + (i & 7) * 102400 + (i >> 3) * 2560;
          o2 = (lane & 15) < 8 ? 2048 + gam * 128 + (lane & 7) * 16 : OOR;
        }
        const unsigned dstep = gu || dn ? 122880 : 51200;
        STAMP4(24);
        if (cw == 7) {
          const int ci = j * 7 + lane;
          gst32(0u, ws, lane < 7 ? OFF_CTR + ((ci >> 5) * 64 + band * 8 + ((ci >> 2) & 7)) * 1024 + (ci & 3) * 256 : OOR);
        }
        asm volatile("s_waitcnt vmcnt(0)" : "+v"(a0), "+v"(a1), "+v"(a2), "+v"(o2) :: "memory");
        STAMP4(25);
        v4u f[2][3];
#pragma unroll
        for (int h2 = 0; h2 < 2; ++h2) {
          f[h2][0] = tr_frag(a0 + h2 * a3, a0 + h2 * a3 + 1024);
          f[h2][1] = tr_frag(a1 + h2 * a3, a1 + h2 * a3 + 1024);
          f[h2][2] = tr_frag(a2 + h2 * a3, a2 + h2 * a3 + 1024);
        }
        if (S2GATE) lds_wait_gt(L_CRIT, S2GATE - 1, rg.bud);
#pragma unroll
        for (int h2 = 0; h2 < 2; ++h2) {
          gst128(f[h2][0], ws, dst + h2 * dstep + lane * 16, 0); gst128(f[h2][1], ws, dst + h2 * dstep + lane * 16 + 1024, 0);
          gst128(f[h2][2], ws, dst + h2 * dstep + o2, 0);
        }
        if (gu) {
#pragma unroll
          for (int h2 = 0; h2 < 2; ++h2) {
            const float s0 = mfma32(ones, f[h2][0], z)[0], s1 = mfma32(ones, f[h2][1], z)[0], s2 = mfma32(ones, f[h2][2], z)[0];
            const unsigned cs = OFF_CSG + ((2 * (i & 15) + h2) * 40 + (i >> 4)) * 512 + lane * 4;
            gst32(asu(lane < 32 ? s0 : s1), ws, cs);
            gst32(lane < 32 ? asu(s2) : 0u, ws, cs + 256);
          }
        }
        lds_wait_gt(L_TAGV, 0u, rg.bud);
        tag = ufl(lds_ld(L_TAG));
        STAMP4(26);
        DRAIN();
        STAMP4(27);
      }
      if (lds_inc1(L_STG) == 7u) gst32(tag, ws, lane == 0 ? OFF_F2 + blockIdx.x * 4 : OOR);
    }
    STAMP4(28);
#pragma unroll 1
    for (int L = 0; L < 2; ++L) {
      rg.lf = L * NFILL; rg.ls = L * NSTAGE;
      const int t32i = opq(t32_), t16i = opq(t16_);
      PP(PHPRIO & 3);
      v4u q1; v2u q2;
      {
        f32x16 acc[2]; float rs;
        gemm32<2, WQ_, 2, 0, 0, TabQ, L_CQ>(acc, rs, rg, ws, OFF_XA + (2 * L) * XA_SZ + t32i * XA_T, OFF_ST + (2 * L) * ST_SZ + t32i * 4096,
                                            [&]() INL {
          if (HINT && HINT < 3) {
            const int lane = fresh_lane(), h = lane >> 5;
            const v4u sv = {SENT, SENT, SENT, SENT};
            if (band < 7) {
              const unsigned gb = OFF_KVH + L * KVH_SZ + ((band * 32 + j) * 8 + qt) * KVH_T;
              gst128(sv, ws, lane * 16, gb); gst128(sv, ws, lane * 16 + 1024, gb); gst128(sv, ws, lane * 16 + 2048, gb);
            }
            const unsigned ob = OFF_OA + L * OA_SZ + (2 * t32i) * OA_T + (5 * (j >> 1)) * 256;
            const unsigned tv = ((lane >> 4) & 1) * OA_T + (lane & 15) * 16;
            gst128(sv, ws, tv + h * 256, ob + (3 * (j & 1)) * 256);
            gst64(v2u{SENT, SENT}, ws, tv + (h ? WS_USED : 0u), ob + 2 * 256 + (j & 1) * 8);
          }
          if (L > 0) ctr_wait(wsp, 3, t32i, rep, rg.bud);
          else {
            DRAIN();
            unsigned xr = 0;
            lds_wait_bits(L_XRDY, 0xfu << (4 * qt), xr, rg.bud);
            if (TURN0 > 0 && grp == 1) lds_wait_gt(rg.prog - 16, TURN0 - 1, rg.bud);
          }
          STAMP(1 + L * 12); });
        STAMP(2 + L * 12);
        const int lane = fresh_lane(), h = lane >> 5;
        v4u k1, ta, tb;
        const float rq = rs * QSCALE, r0 = h ? rs : rq;
#pragma unroll
        for (int i = 0; i < 4; ++i) k1[i] = pk(acc[0][8 + 2 * i] * rs, acc[0][9 + 2 * i] * rs);
        ta[0] = pk(acc[1][0] * r0, acc[1][1] * r0); ta[1] = pk(acc[1][2] * r0, acc[1][3] * r0);
        ta[2] = pk(acc[1][4] * rs, acc[1][5] * rs); ta[3] = pk(acc[1][6] * rs, acc[1][7] * rs);
#pragma unroll
        for (int i = 0; i < 4; ++i) tb[i] = pk(acc[1][8 + 2 * i] * rs, acc[1][9 + 2 * i] * rs);
        if (h) { tb[2] = 0x3f803f80u; tb[3] = 0x3f803f80u; }
        q2[0] = h ? 0x3f80u : ta[0]; q2[1] = h ? 0u : ta[1];
        if (!h) { ta[0] = 0u; ta[1] = 0u; }
        if (band < 7) {
          const unsigned gb = OFF_KVH + L * KVH_SZ + ((band * 32 + j) * 8 + qt) * KVH_T;
          gst128(k1, ws, lane * 16, gb); gst128(ta, ws, lane * 16 + 1024, gb); gst128(tb, ws, lane * 16 + 2048, gb);
          if (HINT) gst32(tag, ws, lane == 0 ? OFF_HALOF + (((L * 8 + band) * 32 + j) * 2 + (qt >> 2)) * 256 + (qt & 3) * 4 : WS_USED);
        }
        if (L > 0) prog_wait(L * NSTAGE, rg.bud); else lds_wait_gt(L_STG, 7u, rg.bud);
        kv_write(KV_OFF + dslot * KV_T, lane, k1, ta, tb);
        CBAR();
        lds_or1(L_RDY + L * 4, 1u << dslot);
#pragma unroll
        for (int i = 0; i < 4; ++i) q1[i] = pk(acc[0][2 * i] * rq, acc[0][2 * i + 1] * rq);
      }
      STAMP(3 + L * 12);
      PP((PHPRIO >> 2) & 3);
      {
        const int lane = fresh_lane(), h = lane >> 5;
        f32x16 o, negm, sc[2];
        const int topA = dslot, topB = grp == 0 ? 8 : 12;
        const int top0 = grp == 0 ? topA : topB, top1 = grp == 0 ? topB : topA;
        const unsigned vlo = 1536 + (4 * h + ((lane & 15) >> 2)) * 48 + (((lane >> 4) & 1) == 0 ? 8 * (lane & 3) : ((lane & 3) == 0 ? 32 : 40));
        unsigned ka[2], kb[2], va[2], vd;
        ka[0] = KV_OFF + (top0 - 4) * KV_T + lane * 16; ka[1] = KV_OFF + (top1 - 4) * KV_T + lane * 16;
        kb[0] = KV_OFF + (top0 - 4) * KV_T + 1024 + lane * 8; kb[1] = KV_OFF + (top1 - 4) * KV_T + 1024 + lane * 8;
        va[0] = KV_OFF + (top0 - 4) * KV_T + vlo; va[1] = KV_OFF + (top1 - 4) * KV_T + vlo;
        vd = KV_OFF + dslot * KV_T + vlo;
        asm volatile("" : "+v"(ka[0]), "+v"(ka[1]), "+v"(kb[0]), "+v"(kb[1]), "+v"(va[0]), "+v"(va[1]), "+v"(vd));
        const int ql = (lane & 31) - 4 * h;
        unsigned rdyv = lds_ld(L_RDY + L * 4);
        {
          f32x16 z;
#pragma unroll
          for (int r = 0; r < 16; ++r) { z[r] = 0.f; o[r] = 0.f; }
          sc[0] = mfma32(lds128(KV_OFF + dslot * KV_T + lane * 16), q1, z);
          sc[0] = mfma32k8(lds64(KV_OFF + dslot * KV_T + 1024 + lane * 8), q2, sc[0]);
#pragma unroll
          for (int r = 0; r < 16; ++r) sc[0][r] = ((r & 3) + 8 * (r >> 2)) <= ql ? sc[0][r] : NEG;
          const float m = max_halves(max16(sc[0])) + MB;
#pragma unroll
          for (int r = 0; r < 16; ++r) { sc[0][r] -= m; negm[r] = -m; }
        }
        STAMP2(24);
        unsigned rdy = ufl(rdyv);
        sfor<0, 9>([&](auto N) INL {
          constexpr int n = N.v;
          f32x16& s = sc[n & 1];
          f32x16& sn = sc[(n + 1) & 1];
          v4u k1; v2u k2, v0, v1, v2, v3;
          if constexpr (n == ANN) {
            if (!HINT && band < 7) {
              DRAIN();
              gst32(tag, ws, lane == 0 ? OFF_HALOF + (((L * 8 + band) * 32 + j) * 2 + (qt >> 2)) * 256 + (qt & 3) * 4 : WS_USED);
            }
            STAMP2(25);
          }
          {
            const unsigned a = n == 0 ? vd : va[(n - 1) >> 2] + (3 - ((n - 1) & 3)) * KV_T;
            v0 = ldstr(a); v1 = ldstr(a + 384); v2 = ldstr(a + 768); v3 = ldstr(a + 1152);
          }
          if constexpr (n == 8) { CBAR(); lds_st(rg.prog, rg.ls + 9); CBAR(); }
          if constexpr (n == 4 || n == 8) {
            if (grp == (n == 4 ? 0 : 1)) {
#pragma unroll
              for (int r = 0; r < 16; ++r) s[r] = ((r & 3) + 8 * (r >> 2)) > ql ? s[r] : NEG;
            }
          }
          v4u p, p2;
          p[0] = pk(ex2(s[0]), ex2(s[1])); p[1] = pk(ex2(s[2]), ex2(s[3]));
          if constexpr (n < 8) {
            lds_wait_bits(L_RDY + L * 4, 1u << ((n < 4 ? top0 : top1) - 1 - (n & 3)), rdy, rg.bud);
            k1 = lds128(ka[n >> 2] + (3 - (n & 3)) * KV_T); k2 = lds64(kb[n >> 2] + (3 - (n & 3)) * KV_T);
          }
          p[2] = pk(ex2(s[4]), ex2(s[5])); p[3] = pk(ex2(s[6]), ex2(s[7]));
          p2[0] = pk(ex2(s[8]), ex2(s[9]));
          SCHED();
          if constexpr (n < 8) sn = mfma32(k1, q1, negm);
          p2[1] = pk(ex2(s[10]), ex2(s[11]));
          float e12 = ex2(s[12]);
          SCHED();
          if constexpr (n < 8) sn = mfma32k8(k2, q2, sn);
          p2[2] = pk(e12, ex2(s[13])); p2[3] = pk(ex2(s[14]), ex2(s[15]));
          const unsigned any = ((p[0] | p[1] | p[2]) | (p[3] | p2[0] | p2[1]) | (p2[2] | p2[3])) & 0x40004000u;
          if (__builtin_expect(__any(any != 0u), 0)) {
            const float d = fmaxf(max_halves(max16(s)) + MB, 0.f);
            const float f = ex2(-d);
#pragma unroll
            for (int r = 0; r < 16; ++r) { s[r] -= d; negm[r] -= d; o[r] *= f; if (n < 8) sn[r] -= d; }
#pragma unroll
            for (int i = 0; i < 4; ++i) { p[i] = pk(ex2(s[2 * i]), ex2(s[2 * i + 1])); p2[i] = pk(ex2(s[8 + 2 * i]), ex2(s[9 + 2 * i])); }
          }
          o = mfma32(v4u{v0[0], v0[1], v1[0], v1[1]}, p, o);
          o = mfma32(v4u{v2[0], v2[1], v3[0], v3[1]}, p2, o);
          SCHED();
          if constexpr (n == 3) STAMP2(26);
          if constexpr (n == 5) STAMP2(27);
          if constexpr (n == 7) STAMP2(28);
        });
        const float inv = __builtin_amdgcn_rcpf(o[12]);
        v4u o1;
#pragma unroll
        for (int i = 0; i < 4; ++i) o1[i] = pk(o[2 * i] * inv, o[2 * i + 1] * inv);
        const v2u o2 = {pk(o[8] * inv, o[9] * inv), pk(o[10] * inv, o[11] * inv)};
        const unsigned ob = OFF_OA + L * OA_SZ + (2 * t32i) * OA_T + (5 * (j >> 1)) * 256;
        const unsigned tv = ((lane >> 4) & 1) * OA_T + (lane & 15) * 16;
        gst128(o1, ws, tv + h * 256, ob + (3 * (j & 1)) * 256);
        gst64(o2, ws, tv + (h ? WS_USED : 0u), ob + 2 * 256 + (j & 1) * 8);
        STAMP(4 + L * 12);
        if (L == 0) lds_wait_gt(L_W2OK, 0u, rg.bud);
        ctr_arrive(wsp, 4 * L, t32i);
        if (L == 0) {
          const int g4 = lane >> 4;
          const unsigned ro = ((t16i & 1) * 16 + (lane & 15)) * 16 + (g4 & 1) * 8, rb = OFF_XA + (t16i >> 1) * XA_T + (5 * ct) * 512;
          const v2u r0 = gld64(ws, ro + (g4 >> 1) * 512, rb), r1 = gld64(ws, ro + (g4 >> 1) * 512 + 1024, rb), r2 = gld64(ws, g4 < 2 ? ro + 2048 : WS_USED, rb);
          res[0] = r0[0]; res[1] = r0[1]; res[2] = r1[0]; res[3] = r1[1]; res[4] = r2[0]; res[5] = r2[1];
        }
      }
      STAMP(5 + L * 12);
      PP((PHPRIO >> 4) & 3);
      gemm16<20, WO_, 3, 8, 9, TabO>(rg, ws, OFF_OA + L * OA_SZ + t16i * OA_T, res, OFF_XA + (2 * L + 1) * XA_SZ + (t16i >> 1) * XA_T,
                                     OFF_ST + (2 * L + 1) * ST_SZ + (t16i >> 1) * 4096, false, out, t16i, ct,
                                     [&]() INL { ctr_wait(wsp, 4 * L, t16i >> 1, rep, rg.bud); STAMP(6 + L * 12); });
      STAMP(7 + L * 12);
      ctr_arrive(wsp, 4 * L + 1, t16i >> 1);
      PP((PHPRIO >> 6) & 3);
      {
        f32x16 acc[3]; float rs;
        gemm32<3, WG_, 2, 13, 14, TabG, L_CG>(acc, rs, rg, ws, OFF_XA + (2 * L + 1) * XA_SZ + t32i * XA_T, OFF_ST + (2 * L + 1) * ST_SZ + t32i * 4096,
                                              [&]() INL {
          if (HINT && HINT < 3) {
            const int lane = fresh_lane();
            const v4u sv = {SENT, SENT, SENT, SENT};
            const unsigned hb = OFF_HA + L * HA_SZ + (2 * t32i) * HA_T + (6 * j) * 256;
            const unsigned tv = ((lane >> 4) & 1) * HA_T + (lane & 15) * 16 + (lane >> 5) * 256;
            gst128(sv, ws, tv, hb); gst128(sv, ws, tv + 512, hb); gst128(sv, ws, tv + 1024, hb);
          }
          ctr_wait(wsp, 4 * L + 1, t32i, rep, rg.bud); STAMP(8 + L * 12); STAMP1(24); });
        STAMP1(25); STAMP1(26);
        const int lane = fresh_lane(), h = lane >> 5;
        const float r2 = rs * rs;
        const unsigned hb = OFF_HA + L * HA_SZ + (2 * t32i) * HA_T + (6 * j) * 256;
        const unsigned tv = ((lane >> 4) & 1) * HA_T + (lane & 15) * 16 + h * 256;
        v4u hv;
#pragma unroll
        for (int i = 0; i < 4; ++i) hv[i] = pk(acc[0][2 * i] * r2 * acc[1][2 * i], acc[0][2 * i + 1] * r2 * acc[1][2 * i + 1]);
        gst128(hv, ws, tv, hb);
#pragma unroll
        for (int i = 0; i < 4; ++i) hv[i] = pk(acc[0][8 + 2 * i] * r2 * acc[1][8 + 2 * i], acc[0][9 + 2 * i] * r2 * acc[1][9 + 2 * i]);
        gst128(hv, ws, tv + 512, hb);
#pragma unroll
        for (int i = 0; i < 4; ++i) hv[i] = pk(acc[2][2 * i] * r2 * acc[2][8 + 2 * i], acc[2][2 * i + 1] * r2 * acc[2][9 + 2 * i]);
        gst128(hv, ws, tv + 1024, hb);
      }
      STAMP(9 + L * 12);
      ctr_arrive(wsp, 4 * L + 2, t32i);
      PP((PHPRIO >> 8) & 3);
      gemm16<48, WD_, 3, 25, 26, TabD>(rg, ws, OFF_HA + L * HA_SZ + t16i * HA_T, res, OFF_XA + 2 * XA_SZ + (t16i >> 1) * XA_T,
                                       OFF_ST + 2 * ST_SZ + (t16i >> 1) * 4096, L == 1, out, t16i, ct,
                                       [&]() INL { ctr_wait(wsp, 4 * L + 2, t16i >> 1, rep, rg.bud); STAMP(10 + L * 12); });
      STAMP(11 + L * 12);
      if (L == 0) ctr_arrive(wsp, 3, t16i >> 1);
    }
  } else {
    const int lane = fresh_lane();
    const int half = 2 - wave;
    __builtin_amdgcn_s_setprio(3);
    unsigned bud = 0;
    unsigned tag = 0;
    if (wave == 3) { lds_wait_gt(L_TAGV, 0u, bud); tag = ufl(lds_ld(L_TAG)); }
    if (wave < 3) {
      const int hx = wave - 1;
      {
        const unsigned xa = OFF_XA + (band * 8 + (j >> 2)) * XA_T + ((j & 3) * 8 + (lane & 7)) * 16 + (hx * 40 + (lane >> 3)) * 512;
        unsigned la = STG_X + hx * 5120 + (lane & 7) * 128 + (lane >> 3) * 16;
        unsigned sa = OFF_ST + (band * 8 + (j >> 2)) * 4096 + st_line(hx * 8 + (lane >> 3), (j >> 1) & 1) + (lane & 7) * 16 + (j & 1) * 8;
        unsigned fa = lane < 32 ? OFF_IN + (band * 32 + lane) * 512 + 256 + (j * 2 + hx) * 4 : OOR;
#pragma unroll 1
        for (int pass = DRYPASS ? 0 : 1; pass < 2; ++pass) {
          const unsigned off = pass ? 0u : OOR;
          if (pass) DRAIN();
          v4u xv[5];
#pragma unroll
          for (int i = 0; i < 5; ++i) xv[i] = lds128(la + i * 1024);
#pragma unroll
          for (int i = 0; i < 5; ++i) gst128(xv[i], ws, xa + off + i * 4096, 0);
          float s = 0.f, qq = 0.f;
#pragma unroll
          for (int i = 0; i < 5; ++i) {
            s = sum8(xv[i], s);
#pragma unroll
            for (int k = 0; k < 4; ++k) { const unsigned u = xv[i][k]; qq = dot2(u, u, qq); }
          }
          gst64(v2u{asu(s), asu(qq)}, ws, sa + off, 0);
          if (pass) lds_wait_gt(L_TAGV, 0u, bud);
          tag = ufl(lds_ld(L_TAG));
          if (pass) DRAIN();
          gst32(tag, ws, fa + off);
        }
      }
      lds_add1(L_CRIT);
    } else {
      for (;;) {
        const unsigned m = (unsigned)__ballot(rmw_poll_pairs((GAS unsigned*)(wsp + OFF_IN + (band * 32 + j) * 512 + 256), lane * 8, 0xffffffffu, tag));
        lds_st(L_XRDY, m);
        if (m == 0xffffffffu) break;
        SPEND(bud);
      }
      lds_wait_gt(L_WQOK, 0u, bud);
      v4u pc[10];
      sfor<0, 10>([&](auto I) INL { pc[I.v] = lda<I.v>(ws, lane * 16, OFF_CSQ + j * 10240); });
      f32x4 c = __builtin_bit_cast(f32x4, pc[0]);
#pragma unroll
      for (int i = 1; i < 10; ++i) c += __builtin_bit_cast(f32x4, pc[i]);
#pragma unroll
      for (int i = 0; i < 4; ++i) c[i] = sum_halves(sum_rows16(c[i]));
      if (lane < 16) csum_rows(c, L_CQ + lane * 32);
      CBAR();
      lds_st(L_CSUM, 1u);
    }
    __builtin_amdgcn_s_setprio(PRIO_H);
    if (wave == 3) {
      lds_wait_gt(L_PROG, F2GATE, bud);
      for (;;) {
        const v4u f0 = gld128(ws, lane * 16, OFF_F2);
        const bool ok = f0[0] == tag && f0[1] == tag && f0[2] == tag && f0[3] == tag;
        if (__all(ok)) break;
        SPEND(bud);
        __builtin_amdgcn_s_sleep(16);
        CBAR();
      }
      CBAR();
      lds_st(L_W2OK, 1u);
      if (blockIdx.x == 0) gst32(tag, ws, lane == 0 ? OFF_EP : WS_USED);
      f32x4 c = {0.f, 0.f, 0.f, 0.f};
#pragma unroll 1
      for (int r = 0; r < 2; ++r) {
        v4u pc[10];
        sfor<0, 10>([&](auto I) INL { pc[I.v] = lda<I.v>(ws, lane * 16, OFF_CSG + j * 20480 + r * 10240); });
#pragma unroll
        for (int i = 0; i < 10; ++i) c += __builtin_bit_cast(f32x4, pc[i]);
      }
#pragma unroll
      for (int i = 0; i < 4; ++i) c[i] = sum_halves(c[i]);
      if (lane < 24) csum_rows(c, L_CG + lane * 32);
      CBAR();
      lds_st(L_CSUM, 2u);
      return;
    }
    if (band == 0) {
#pragma unroll 1
      for (int L = 0; L < 2; ++L) {
        if (L > 0) prog_wait(L * NSTAGE, bud); else lds_wait_gt(L_STG, 7u, bud);
        const v4u z = {0u, 0u, 0u, 0u};
#pragma unroll 1
        for (int sl = 0; sl < 4; ++sl) kv_write(KV_OFF + (8 * half + sl) * KV_T, lane, z, v4u{lane < 32 ? 0xf149u : 0u, 0u, 0u, 0u}, z);
        CBAR();
        lds_or1(L_RDY + L * 4, 0xfu << (8 * half));
      }
    } else {
#pragma unroll 1
      for (int L = 0; L < 2; ++L) {
        lds_wait_gt(L_PROG + (1 - half) * 16, L * NSTAGE + 5, bud);
        if (L > 0) prog_wait(L * NSTAGE, bud); else lds_wait_gt(L_STG, 7u, bud);
        unsigned pend = 0xfu;
        GAS unsigned* fl = (GAS unsigned*)(wsp + OFF_HALOF + (((L * 8 + band - 1) * 32 + j) * 2 + half) * 256);
        const unsigned gb = OFF_KVH + L * KVH_SZ + (((band - 1) * 32 + j) * 8 + 4 * half) * KVH_T;
        while (pend) {
          const unsigned nw = (unsigned)__ballot(rmw_poll_lanes(fl, lane * 4, 0xfu, 0u, 0u) == tag) & pend;
          __builtin_amdgcn_fence(__ATOMIC_ACQUIRE, "wavefront");
          CBAR();
          if (!nw) {
            SPEND(bud);
            continue;
          }
          pend &= ~nw;
          v4u x[4][3];
#pragma unroll
          for (int sl = 3; sl >= 0; --sl) {
            const unsigned vo = lane * 16 + ((nw >> sl & 1) ? 0u : WS_USED);
#pragma unroll
            for (int i = 0; i < 3; ++i) x[sl][i] = gld128(ws, vo + i * 1024, gb + sl * KVH_T);
          }
#pragma unroll
          for (int sl = 3; sl >= 0; --sl)
            if (nw >> sl & 1) {
              if (HINT && __builtin_expect((stale2(x[sl][0], x[sl][1]) | stale2(x[sl][2], x[sl][2])) != 0, 0)) { if (bud < SPIN_CAP) { ++bud; pend |= 1u << sl; continue; } }
              kv_write(KV_OFF + (8 * half + sl) * KV_T, lane, x[sl][0], x[sl][1], x[sl][2]);
              CBAR();
              lds_or1(L_RDY + L * 4, 1u << (8 * half + sl));
            }
        }
      }
    }
  }
}

extern "C" void kernel_launch(void* const* d_in, const int* in_sizes, int n_in, void* d_out, int out_size, void* d_ws, size_t ws_size,
                              hipStream_t stream) {
  typedef const unsigned char* cp;
  mega_kernel<<<256, 768, 0, stream>>>((unsigned char*)d_ws + ((ws_size - WS_USED) & ~(size_t)0x1fffff), d_out, (cp)d_in[0], (cp)d_in[1], (cp)d_in[2], (cp)d_in[4], (cp)d_in[5], (cp)d_in[6]);
}
```

```cpp
#include <hip/hip_runtime.h>
#include <hip/hip_bf16.h>
#include <stdint.h>
#pragma clang diagnostic ignored "-Wint-to-pointer-cast"

typedef unsigned v4u __attribute__((ext_vector_type(4)));
typedef unsigned v2u __attribute__((ext_vector_type(2)));
typedef short s16x8 __attribute__((ext_vector_type(8)));
typedef short s16x4 __attribute__((ext_vector_type(4)));
typedef float f32x16 __attribute__((ext_vector_type(16)));
typedef float f32x4 __attribute__((ext_vector_type(4)));
typedef float f32x2 __attribute__((ext_vector_type(2)));
typedef __bf16 bf2 __attribute__((ext_vector_type(2)));
typedef unsigned short u16;
#define LAS __attribute__((address_space(3)))
#define GAS __attribute__((address_space(1)))
#define DI __device__ __forceinline__
#define INL __attribute__((always_inline))
#define RLX_AGENT __ATOMIC_RELAXED, __HIP_MEMORY_SCOPE_AGENT
#define RLX_WG __ATOMIC_RELAXED, __HIP_MEMORY_SCOPE_WORKGROUP
#define CBAR() asm volatile("" ::: "memory")
#define SCHED() __builtin_amdgcn_sched_barrier(0)

constexpr unsigned OFF_CTR = 0;
constexpr unsigned OFF_HALOF = 7 * 64 * 1024;
constexpr unsigned OFF_IN = OFF_HALOF + 2 * 8 * 32 * 512;
constexpr unsigned OFF_F2 = OFF_IN + 256 * 512;
constexpr unsigned OFF_EP = OFF_F2 + 1024;
constexpr unsigned OFF_STAMP = 64u << 20;
constexpr unsigned OFF_WQ = 1u << 20;
constexpr unsigned OFF_WO = OFF_WQ + 32 * 81920;
constexpr unsigned OFF_WG = OFF_WO + 16 * 51200;
constexpr unsigned OFF_WD = OFF_WG + 32 * 122880;
constexpr unsigned XA_T = 40960, XA_SZ = 64 * XA_T;
constexpr unsigned OFF_XA = OFF_WD + 16 * 122880;
constexpr unsigned ST_SZ = 64 * 4096;
constexpr unsigned OFF_ST = OFF_XA + 4 * XA_SZ;
constexpr unsigned OA_T = 20480, OA_SZ = 128 * OA_T;
constexpr unsigned OFF_OA = OFF_ST + 4 * ST_SZ;
constexpr unsigned HA_T = 49152, HA_SZ = 128 * HA_T;
constexpr unsigned OFF_HA = OFF_OA + 2 * OA_SZ;
constexpr unsigned KVH_T = 3072, KVH_SZ = 8 * 32 * 8 * KVH_T;
constexpr unsigned OFF_KVH = OFF_HA + 2 * HA_SZ;
constexpr unsigned OFF_CSQ = OFF_KVH + 2 * KVH_SZ;
constexpr unsigned OFF_CSG = OFF_CSQ + 32 * 40 * 256;
constexpr unsigned WS_USED = OFF_CSG + 32 * 40 * 512;
static_assert(OFF_EP + 256 <= OFF_WQ, "");

constexpr int UNIT = 10240;
constexpr int MISC = 0, ARENA = 2048;
constexpr int KV_OFF = ARENA + 8 * UNIT, KV_T = 3072;
constexpr int L_PROG = MISC, L_LANDED = MISC + 64, L_RDY = MISC + 68, L_CSUM = MISC + 76;
constexpr int L_TAG = MISC + 80, L_TAGV = MISC + 84, L_XRDY = MISC + 88, L_STG = MISC + 92, L_WQOK = MISC + 96, L_W2OK = MISC + 100, L_CRIT = MISC + 104;
constexpr int STG = ARENA + 8 * UNIT;
constexpr int STG_X = STG + 65536;
constexpr int L_CQ = MISC + 256, L_CG = MISC + 768, L_STAMP = ARENA + 15 * UNIT + 4096;
constexpr int LDS_BYTES = ARENA + 15 * UNIT + 8192;
#define LDEPTH 2
#define WQ_ 12
#define WG_ 12
#define WO_ 10
#define WD_ 10
#define PRIO_H 0
#define PHPRIO 438
#define PP(p) do { if (PHPRIO) __builtin_amdgcn_s_setprio(p); } while (0)
#define PRETOUCH 0
#define L2GATE 0
#define DRYPASS 0
#define HINT 0
#define ABL 0
#define PRIO_F 3
#define FGATE 6
#define DQ_ 4
#define DG_ 4
#define NOPK 1
#define W0_ 4
#define SL_L 1
#define SL_F 1
#define TURNQ 0
#define TKMAP 1
#define S2GATE (TKMAP ? 2 : 4)
#define F2GATE 3u
#define ANN 0
#define TURN0 0
constexpr int NFILL = 37, NSTAGE = 38;
constexpr unsigned SPIN_CAP = 1u << 18;

struct TabQ { static constexpr int u(int i) { return i; } };
struct TabO { static constexpr int u(int i) { return i + 1; } };
struct TabG { static constexpr int u(int i) { constexpr int t[12] = {13, 14, 0, 6, 7, 8, 9, 10, 11, 12, 1, 2}; return t[i]; } };
struct TabD { static constexpr int u(int i) { constexpr int t[12] = {3, 4, 5, 13, 14, 6, 7, 8, 9, 10, 11, 12}; return t[i]; } };

template <int V> struct IC { static constexpr int v = V; };
template <int I, int N, class F> DI void sfor(F&& f) { if constexpr (I < N) { f(IC<I>{}); sfor<I + 1, N>(f); } }
DI int fresh_lane() { int l; asm volatile("v_mbcnt_lo_u32_b32 %0, -1, 0\n\tv_mbcnt_hi_u32_b32 %0, -1, %0" : "=v"(l)); return l; }
DI int opq(int v) { asm volatile("" : "+s"(v)); return v; }
DI unsigned long long w_ballot(bool p) { return __builtin_amdgcn_ballot_w64(p); }
DI bool w_any(bool p) { return w_ballot(p) != 0ull; }
DI bool w_all(bool p) { return w_ballot(!p) == 0ull; }
DI float asf(unsigned u) { return __uint_as_float(u); }
DI unsigned asu(float f) { return __float_as_uint(f); }
DI unsigned pk(float a, float b) { f32x2 t = {a, b}; bf2 p = __builtin_convertvector(t, bf2); return __builtin_bit_cast(unsigned, p); }
DI float rbf(float a) { return asf(pk(a, a) << 16); }
DI float ex2(float a) { return __builtin_amdgcn_exp2f(a); }
DI float dot2(unsigned a, unsigned b, float c) { return __builtin_amdgcn_fdot2_f32_bf16(__builtin_bit_cast(bf2, a), __builtin_bit_cast(bf2, b), c, false); }
DI f32x16 mfma32(v4u a, v4u b, f32x16 c) { return __builtin_amdgcn_mfma_f32_32x32x16_bf16(__builtin_bit_cast(s16x8, a), __builtin_bit_cast(s16x8, b), c, 0, 0, 0); }
DI f32x16 mfma32k8(v2u a, v2u b, f32x16 c) { return __builtin_amdgcn_mfma_f32_32x32x8bf16_1k(__builtin_bit_cast(s16x4, a), __builtin_bit_cast(s16x4, b), c, 0, 0, 0); }
DI f32x4 mfma16(v4u a, v4u b, f32x4 c) { return __builtin_amdgcn_mfma_f32_16x16x32_bf16(__builtin_bit_cast(s16x8, a), __builtin_bit_cast(s16x8, b), c, 0, 0, 0); }
DI void mfma16i(f32x4& c, v4u a, v4u b) { asm volatile("v_mfma_f32_16x16x32_bf16 %0, %1, %2, %0" : "+v"(c) : "v"(a), "v"(b)); }
DI void mfma_fence(f32x4& a, f32x4& b, f32x4& c) { asm volatile("s_nop 7" : "+v"(a), "+v"(b), "+v"(c)); }
DI unsigned ufl(unsigned v) { return __builtin_amdgcn_readfirstlane(v); }
DI unsigned lds_ld(unsigned a) { return __hip_atomic_load((LAS unsigned*)a, RLX_WG); }
DI void lds_st(unsigned a, unsigned v) { __hip_atomic_store((LAS unsigned*)a, v, RLX_WG); }
DI void lds_or1(unsigned a, unsigned v) { asm volatile("s_mov_b64 exec, 1\n\tds_or_b32 %0, %1\n\ts_mov_b64 exec, -1" :: "v"(a), "v"(v) : "memory"); }
DI v4u lds128(unsigned a) { return *(LAS v4u*)a; }
DI v2u lds64(unsigned a) { return *(LAS v2u*)a; }
DI v2u ldstr(unsigned a) { return __builtin_bit_cast(v2u, __builtin_amdgcn_ds_read_tr16_b64_v4i16((LAS s16x4*)a)); }
DI float sum_halves(float x) { auto r = __builtin_amdgcn_permlane32_swap(asu(x), asu(x), false, false); unsigned a = r[0], b = r[1]; return asf(a) + asf(b); }
DI float max_halves(float x) { auto r = __builtin_amdgcn_permlane32_swap(asu(x), asu(x), false, false); unsigned a = r[0], b = r[1]; return fmaxf(asf(a), asf(b)); }
DI float sum_rows16(float x) { auto r = __builtin_amdgcn_permlane16_swap(asu(x), asu(x), false, false); unsigned a = r[0], b = r[1]; return asf(a) + asf(b); }
DI float ror8(float x) { return asf(__builtin_amdgcn_update_dpp(0u, asu(x), 0x128, 0xf, 0xf, false)); }
DI unsigned st_line(int c, int hf) { return ((c >> 2) * 8 + ((c >> 1) & 1) * 4 + hf * 2 + (c & 1)) * 128; }
DI float max3(float a, float b, float c) { return __builtin_elementwise_maximum(__builtin_elementwise_maximum(a, b), c); }

typedef __amdgpu_buffer_rsrc_t rsrc_t;
constexpr int SC1 = 16;
DI v4u gld128(rsrc_t r, unsigned v, unsigned s) { return __builtin_amdgcn_raw_buffer_load_b128(r, v, s, SC1); }
DI v2u gld64(rsrc_t r, unsigned v, unsigned s) { return __builtin_amdgcn_raw_buffer_load_b64(r, v, s, SC1); }
DI void gst128(v4u x, rsrc_t r, unsigned v, unsigned s) { __builtin_amdgcn_raw_buffer_store_b128(x, r, v + s, 0, SC1); }
DI void gst64(v2u x, rsrc_t r, unsigned v, unsigned s) { __builtin_amdgcn_raw_buffer_store_b64(x, r, v, s, SC1); }
template <int S> DI v4u lda(rsrc_t r, unsigned voff, unsigned base) { return gld128(r, voff + (S & 3) * 1024, base + (S >> 2) * 4096); }

#define STAMP_HI (SPIN_CAP - rg.bud)
#define STAMP(i) do {} while (0)
#define INNER 1
#define STAMP1(i) do { if (INNER == 1) STAMP(i); } while (0)
#define STAMP2(i) do { if (INNER == 2) STAMP(i); } while (0)
#define STAMP4(i) do { if (INNER == 4) STAMP(i); } while (0)
#define STAMP5(i) do { if (INNER == 5 && rg.ls == 0) STAMP(i); } while (0)
#define STAMP3(i) do { if (INNER == 3 && rg.ls == 0) STAMP(i); } while (0)

#define SPEND(bud) { if ((bud) >= SPIN_CAP) break; ++(bud); }
template <int SL = 1> DI unsigned lds_wait_gt(unsigned a, unsigned f, unsigned& bud) {
  unsigned v;
  for (;;) {
    v = lds_ld(a);
    if (ufl(v) > f) break;
    SPEND(bud);
    __builtin_amdgcn_s_sleep(SL);
  }
  CBAR();
  return v;
}
DI void lds_wait_bits(unsigned a, unsigned bits, unsigned& cache, unsigned& bud) {
  if (__builtin_expect((cache & bits) == bits, 1)) return;
  for (;;) {
    cache = ufl(lds_ld(a));
    if ((cache & bits) == bits) break;
    SPEND(bud);
    __builtin_amdgcn_s_sleep(1);
  }
  CBAR();
}
template <int SL = 1> DI void prog_wait(unsigned need, unsigned& bud) {
  const int lane = fresh_lane();
  for (;;) {
    unsigned v = lds_ld(L_PROG + (lane & 7) * 4);
    if (w_all(v >= need)) break;
    SPEND(bud);
    __builtin_amdgcn_s_sleep(SL);
  }
  CBAR();
}
DI unsigned rmw_poll(GAS unsigned* p) {
  unsigned f;
  asm volatile("s_mov_b64 exec, 1\n\ts_nop 3\n\tglobal_atomic_or %0, %1, %1, %2 sc0\n\ts_waitcnt vmcnt(0)\n\ts_mov_b64 exec, -1" : "=&v"(f) : "v"(0u), "s"(p) : "memory");
  return ufl(f);
}
DI unsigned rmw_poll_lanes(GAS unsigned* p, unsigned voff, unsigned mlo, unsigned mhi, unsigned init) {
  asm volatile("s_mov_b32 exec_lo, %3\n\ts_mov_b32 exec_hi, %4\n\ts_nop 3\n\tglobal_atomic_or %0, %1, %5, %2 sc0\n\ts_waitcnt vmcnt(0)\n\ts_mov_b64 exec, -1"
               : "+v"(init) : "v"(voff), "s"(p), "s"(mlo), "s"(mhi), "v"(0u) : "memory");
  return init;
}
DI bool rmw_poll_pairs(GAS unsigned* p, unsigned voff, unsigned mlo, unsigned tag) {
  v2u f = {0u, 0u};
  asm volatile("s_mov_b32 exec_lo, %3\n\ts_mov_b32 exec_hi, 0\n\ts_nop 3\n\tglobal_atomic_or_x2 %0, %1, %4, %2 sc0\n\ts_waitcnt vmcnt(0)\n\ts_mov_b64 exec, -1"
               : "+v"(f) : "v"(voff), "s"(p), "s"(mlo), "v"(v2u{0u, 0u}) : "memory");
  return f[0] == tag && f[1] == tag;
}
DI void gst32(unsigned x, rsrc_t r, unsigned v) { __builtin_amdgcn_raw_buffer_store_b32(x, r, v, 0, SC1); }
#define DRAIN() asm volatile("s_waitcnt vmcnt(0)" ::: "memory")
#define WG_BARRIER() do { asm volatile("s_waitcnt lgkmcnt(0)" ::: "memory"); __builtin_amdgcn_s_barrier(); CBAR(); } while (0)
DI void ctr_wait(unsigned char* ws, int kind, int t32, int rep, unsigned& bud) {
  GAS unsigned* p = (GAS unsigned*)(ws + OFF_CTR + (kind * 64 + t32) * 1024 + rep * 256);
  if (ABL & 8) return;
  for (;;) {
    if (rmw_poll(p) >= 32u) break;
    SPEND(bud);
  }
  __builtin_amdgcn_fence(__ATOMIC_ACQUIRE, "wavefront");
  CBAR();
}
DI void ctr_arrive(unsigned char* ws, int kind, int t32) {
  const int lane = fresh_lane();
  GAS unsigned* p = (GAS unsigned*)(ws + OFF_CTR + (kind * 64 + t32) * 1024);
  if (ABL & 8) return;
  if (!HINT) DRAIN();
  asm volatile("s_mov_b64 exec, 15\n\ts_nop 3\n\tglobal_atomic_add %0, %1, %2\n\ts_mov_b64 exec, -1" :: "v"(lane * 256), "v"(1u), "s"(p) : "memory");
}

DI void dma_fill(v4u srd, unsigned voff, unsigned soff, unsigned ldst) {
  unsigned keep, so;
  asm volatile(
      "s_mov_b32 %0, m0\n\ts_mov_b32 m0, %5\n\ts_add_u32 %1, %4, 0x1000\n\t"
      "buffer_load_dwordx4 %2, %3, %4 offen sc1 lds\n\tbuffer_load_dwordx4 %2, %3, %4 offen offset:1024 sc1 lds\n\t"
      "buffer_load_dwordx4 %2, %3, %4 offen offset:2048 sc1 lds\n\tbuffer_load_dwordx4 %2, %3, %4 offen offset:3072 sc1 lds\n\t"
      "s_add_u32 m0, m0, 0x1000\n\ts_nop 0\n\t"
      "buffer_load_dwordx4 %2, %3, %1 offen sc1 lds\n\tbuffer_load_dwordx4 %2, %3, %1 offen offset:1024 sc1 lds\n\t"
      "buffer_load_dwordx4 %2, %3, %1 offen offset:2048 sc1 lds\n\tbuffer_load_dwordx4 %2, %3, %1 offen offset:3072 sc1 lds\n\t"
      "s_add_u32 m0, m0, 0x1000\n\ts_add_u32 %1, %1, 0x1000\n\t"
      "buffer_load_dwordx4 %2, %3, %1 offen sc1 lds\n\tbuffer_load_dwordx4 %2, %3, %1 offen offset:1024 sc1 lds\n\t"
      "s_mov_b32 m0, %0"
      : "=&s"(keep), "=&s"(so) : "v"(voff), "s"(srd), "s"(soff), "s"(ldst) : "memory", "scc");
}

DI void dma_raw(const unsigned char* base, unsigned bytes, unsigned voff, unsigned soff, unsigned sstep, unsigned ldst, int n) {
  const v4u srd = {(unsigned)(uintptr_t)base, (unsigned)((uintptr_t)base >> 32) & 0xffffu, bytes, 0x00020000u};
#pragma unroll 1
  for (int i = 0; i < n; ++i) {
    unsigned keep;
    asm volatile("s_mov_b32 %0, m0\n\ts_mov_b32 m0, %4\n\ts_nop 0\n\tbuffer_load_dwordx4 %1, %2, %3 offen lds\n\ts_mov_b32 m0, %0"
                 : "=&s"(keep) : "v"(voff), "s"(srd), "s"(soff + i * sstep), "s"(ldst + i * 1024) : "memory");
  }
}
DI unsigned lds_inc1(unsigned a) {
  unsigned r;
  asm volatile("s_mov_b64 exec, 1\n\tds_add_rtn_u32 %0, %1, %2\n\ts_waitcnt lgkmcnt(0)\n\ts_mov_b64 exec, -1" : "=&v"(r) : "v"(a), "v"(1u) : "memory");
  return ufl(r);
}
DI void lds_add1(unsigned a) { asm volatile("s_mov_b64 exec, 1\n\tds_add_u32 %0, %1\n\ts_mov_b64 exec, -1" :: "v"(a), "v"(1u) : "memory"); }

constexpr unsigned SENT = 0xffffffffu;
typedef unsigned long long u64;
DI u64 stale2(v4u x, v4u y) {
  const unsigned a = x[0], b = x[2], c = y[0], d = y[2];
  u64 m = w_ballot(max(max(max(a, b), c), d) == SENT);
  asm volatile("" : "+s"(m));
  return m;
}
template <int F> DI void fresh2(u64 bad, v4u& x, v4u& y, __amdgpu_buffer_rsrc_t ws, unsigned voff, unsigned base, unsigned& bud) {
  if (__builtin_expect(bad != 0, 0)) {
    for (;;) {
      CBAR();
      x = lda<F>(ws, voff, base); y = lda<F + 1>(ws, voff, base);
      if (!stale2(x, y)) break;
      SPEND(bud);
    }
  }
}

struct Ring {
  unsigned lv, lvs;
  unsigned lf, ls;
  unsigned prog;
  unsigned bud;
};
DI void ring_check(Ring& r, int f) {
  if (__builtin_expect(r.lvs <= r.lf + f, 0)) lds_wait_gt(L_LANDED, r.lf + f, r.bud);
  CBAR();
}
DI void ring_latch(Ring& r) { r.lvs = opq(ufl(r.lv)); }
DI void ring_peek(Ring& r) { r.lv = lds_ld(L_LANDED); }
constexpr int BSTEP = 61440;

template <int NB, int W, int DIST, int F0, int S0, class Tab, int CS, class Wait, class Hook>
DI void gemm32(f32x16 (&acc)[NB], float& rs, Ring& rg, rsrc_t ws, unsigned abase, unsigned stbase, Wait&& wait, Hook&& hook) {
  const int lane = fresh_lane();
  const unsigned voff = lane * 16;
  v4u a[W], b[DIST + 1];
  unsigned lb[3];
#pragma unroll
  for (int i = 0; i < 3; ++i) { lb[i] = voff + i * BSTEP; asm volatile("" : "+v"(lb[i])); }
  unsigned pa = rg.prog, pv = rg.ls + S0;
  asm volatile("" : "+v"(pa), "+v"(pv));
  v2u cf[NB], xm;
  v4u st[4];
  float s_ = 0.f, q_ = 0.f, s1_ = 0.f, q1_ = 0.f;
  unsigned csv = 0;
  u64 bs0 = 0, bs1 = 0;
  auto readB = [&](auto P) INL {
    constexpr int ph = P.v;
    if constexpr (ph < 40 * NB) {
      constexpr int ad = ARENA + Tab::u(ph / 10) * UNIT + (ph % 10) * 1024;
      b[ph % (DIST + 1)] = lds128(lb[ad / BSTEP] + ad % BSTEP);
    }
    if constexpr (ph % 10 == 3 && ph + 7 < 40 * NB) ring_peek(rg);
    if constexpr (ph % 10 == 7 && ph + 3 < 40 * NB) ring_latch(rg);
    if constexpr (ph % 10 == 9 && ph + 1 < 40 * NB) ring_check(rg, F0 + ph / 10 + 1);
    if constexpr (ph % 10 == 0 && ph > 0 && ph <= 40 * NB) { CBAR(); ++pv; lds_st(pa, pv); CBAR(); }
  };
  wait();
  constexpr int W0 = W0_ < W ? W0_ : W;
  sfor<0, W0>([&](auto S) INL { a[S.v] = lda<S.v>(ws, voff, abase); });
  SCHED();
  ring_peek(rg);
  ring_latch(rg);
  ring_check(rg, F0);
  sfor<0, DIST>(readB);
  u64 bad = 0;
  if (HINT == 1) fresh2<0>(stale2(a[0], a[1]), a[0], a[1], ws, voff, abase, rg.bud);
  SCHED();
  constexpr int X1 = 38 * NB - 12;
  constexpr int X0 = X1 - 28;
  sfor<0, 40 * NB>([&](auto SL) INL {
    constexpr int sl = SL.v, s = sl / NB, t = sl % NB;
    if constexpr (NB == 3 && t == 0 && (s == 1 || s == 11 || s == 21 || s == 31)) STAMP5(24 + s / 10);
    if constexpr (s == 0) { f32x16 z; _Pragma("unroll") for (int r = 0; r < 16; ++r) z[r] = 0.f; acc[t] = mfma32(b[sl % (DIST + 1)], a[0], z); }
    else acc[t] = mfma32(b[sl % (DIST + 1)], a[s % W], acc[t]);
    SCHED();
    readB(IC<sl + DIST>{});
    if constexpr (s < W - W0) {
      if constexpr (t == 0) a[(W0 + 2 * s) % W] = lda<W0 + 2 * s>(ws, voff, abase);
      if constexpr (t == NB - 1) a[(W0 + 2 * s + 1) % W] = lda<W0 + 2 * s + 1>(ws, voff, abase);
    } else if constexpr (t == NB - 1 && s + W < 40) a[s % W] = lda<s + W>(ws, voff, abase);
    if constexpr (HINT == 1 && t == NB - 1 && (s & 1) && s + 2 < 40) fresh2<s + 1>(bad, a[(s + 1) % W], a[(s + 2) % W], ws, voff, abase, rg.bud);
    if constexpr (HINT == 1 && t == NB - 2 && (s & 1) && s + 2 < 40) bad = stale2(a[(s + 1) % W], a[(s + 2) % W]);
    if constexpr (HINT == 1 && sl == X1 - 5) bs0 = stale2(st[0], st[1]);
    if constexpr (HINT == 1 && sl == X1 - 4) bs1 = stale2(st[2], st[3]);
    if constexpr (HINT == 1 && sl == X1 - 2) fresh2<0>(bs0, st[0], st[1], ws, voff, stbase, rg.bud);
    if constexpr (HINT == 1 && sl == X1 - 1) fresh2<2>(bs1, st[2], st[3], ws, voff, stbase, rg.bud);
    if constexpr (sl >= X0 && sl < X0 + 8 && (sl - X0) % 2 == 0) st[(sl - X0) / 2] = (ABL & 2) ? v4u{0u, 0u, 0u, 0u} : lda<(sl - X0) / 2>(ws, voff, stbase);
    if constexpr (sl == X1 - 10) csv = lds_ld(L_CSUM);
    if constexpr (sl == X1 - 6) csv = opq(ufl(csv));
    if constexpr (sl == X1 - 3) { if (__builtin_expect(ufl(csv) < NB - 1, 0)) lds_wait_gt(L_CSUM, NB - 2, rg.bud); CBAR(); }
    if constexpr (sl >= X1 && sl < X1 + 3) {
      constexpr int i = sl - X1 + 1;
      const unsigned u0 = st[i][0], u1 = st[i][1], u2 = st[i][2], u3 = st[i][3];
      if constexpr (i == 1) { const unsigned w0 = st[0][0], w1 = st[0][1], w2 = st[0][2], w3 = st[0][3]; s_ = asf(w0); q_ = asf(w1); s1_ = asf(w2); q1_ = asf(w3); }
      s_ += asf(u0); q_ += asf(u1); s1_ += asf(u2); q1_ += asf(u3);
    }
    if constexpr (sl == X1 + 3) { const bool up = lane & 8; s_ = (up ? s1_ : s_) + ror8(up ? s_ : s1_); }
    if constexpr (sl == X1 + 4) { const bool up = lane & 8; q_ = (up ? q1_ : q_) + ror8(up ? q_ : q1_); }
    if constexpr (sl == X1 + 5) s_ = sum_halves(s_) * (1.f / 640.f);
    if constexpr (sl == X1 + 6) q_ = sum_halves(q_) * (1.f / 640.f);
    if constexpr (sl == X1 + 7) {
      q_ = fmaxf(q_ - s_ * s_, 0.f);
      const unsigned nm = pk(-s_, -s_);
      xm[0] = lane < 32 ? nm : 0u; xm[1] = lane < 32 ? (nm & 0xffffu) : 0u;
    }
    if constexpr (sl == X1 + 8) {
      q_ = rbf(rbf(q_) + 1e-5f);
      sfor<0, NB>([&](auto T) INL { cf[T.v] = lds64(CS + T.v * 256 + (lane & 31) * 8); });
    }
    if constexpr (sl == X1 + 9) rs = __builtin_amdgcn_rcpf(rbf(__builtin_amdgcn_sqrtf(q_)));
    if constexpr (sl == X1 + 10) hook();
    SCHED();
    if constexpr (s == 38) { acc[t] = mfma32k8(cf[t], xm, acc[t]); SCHED(); }
  });
}

template <int NS, int W, int DIST, int F0, int S0, class Tab, class Wait>
DI void gemm16(Ring& rg, rsrc_t ws, unsigned abase, unsigned (&res)[6], unsigned xbase, unsigned stbase, bool last, void* out, int t16i, int ct, Wait&& wait) {
  const int lane = fresh_lane();
  const unsigned voff = lane * 16;
  v4u a[W], b[DIST + 1][3];
  unsigned lb[3], lc[3];
  const int g4 = lane >> 4;
#pragma unroll
  for (int i = 0; i < 3; ++i) {
    lb[i] = voff + i * BSTEP; asm volatile("" : "+v"(lb[i]));
    lc[i] = g4 * 128 + (lane & 7) * 16 + i * BSTEP + ((lane & 8) ? 0x40000 : 0); asm volatile("" : "+v"(lc[i]));
  }
  unsigned pa = rg.prog, pv = rg.ls + S0;
  asm volatile("" : "+v"(pa), "+v"(pv));
  f32x4 acc[3];
#pragma unroll
  for (int t = 0; t < 3; ++t) acc[t] = f32x4{asf(res[2 * t] << 16), asf(res[2 * t] & 0xffff0000u), asf(res[2 * t + 1] << 16), asf(res[2 * t + 1] & 0xffff0000u)};
  auto readB = [&](auto S, auto T) INL {
    constexpr int s = S.v, t = T.v;
    if constexpr (s < NS) {
      constexpr int ad = ARENA + Tab::u(s / 4) * UNIT + (s % 4) * 2560;
      b[s % (DIST + 1)][t] = lds128((t < 2 ? lb : lc)[ad / BSTEP] + ad % BSTEP + t * 1024);
    }
    if constexpr (t == 0 && s % 4 == 1 && s + 3 < NS) ring_peek(rg);
    if constexpr (t == 0 && s % 4 == 3 && s + 1 < NS) ring_latch(rg);
    if constexpr (t == 2 && s % 4 == 3 && s + 1 < NS) ring_check(rg, F0 + s / 4 + 1);
    if constexpr (t == 0 && s % 4 == 0 && s > 0 && s <= NS) { CBAR(); ++pv; lds_st(pa, pv); CBAR(); }
  };
  const unsigned t32 = (t16i & 1) * 16 + (lane & 15);
  const unsigned pi = 2 * (g4 & 1) + (g4 >> 1);
  unsigned so1 = xbase + (5 * ct) * 512 + pi * 512 + t32 * 16, so2 = xbase + (5 * ct + 4) * 512 + t32 * 16 + (lane < 16 ? 0u : WS_USED);
  unsigned so3 = stbase + st_line(ct, t16i & 1) + (lane & 7) * 16 + (lane & 8) + (lane < 16 ? 0u : WS_USED);
  asm volatile("" : "+v"(so1), "+v"(so2), "+v"(so3), "+v"(acc[0]), "+v"(acc[1]), "+v"(acc[2]));
  if (HINT && HINT < 3 && !last) { const v4u sv = {SENT, SENT, SENT, SENT}; gst128(sv, ws, so1, 0); gst128(sv, ws, so2, 0); gst64(v2u{SENT, SENT}, ws, so3, 0); }
  wait();
  sfor<0, W>([&](auto S) INL { a[S.v] = lda<S.v>(ws, voff, abase); });
  SCHED();
  ring_peek(rg);
  ring_latch(rg);
  ring_check(rg, F0);
  sfor<0, DIST * 3>([&](auto I) INL { readB(IC<I.v / 3>{}, IC<I.v % 3>{}); });
  u64 bad = 0;
  if (HINT == 1) fresh2<0>(stale2(a[0], a[1]), a[0], a[1], ws, voff, abase, rg.bud);
  SCHED();
  if constexpr (NS == 48) { STAMP1(27); }
  sfor<0, NS>([&](auto S) INL {
    constexpr int s = S.v;
    if constexpr (NS == 48 && s % 12 == 1) STAMP3(24 + s / 12);
    sfor<0, 3>([&](auto T) INL {
      constexpr int t = 2 - T.v;
      mfma16i(acc[t], b[s % (DIST + 1)][t], a[s % W]);
      SCHED();
      readB(IC<s + DIST>{}, T);
      if constexpr (T.v == 2 && s + W < NS) a[s % W] = lda<s + W>(ws, voff, abase);
      if constexpr (HINT == 1 && T.v == 0 && (s & 1) && s + 2 < NS) bad = stale2(a[(s + 1) % W], a[(s + 2) % W]);
      if constexpr (HINT == 1 && T.v == 2 && (s & 1) && s + 2 < NS) fresh2<s + 1>(bad, a[(s + 1) % W], a[(s + 2) % W], ws, voff, abase, rg.bud);
      SCHED();
    });
  });
  mfma_fence(acc[0], acc[1], acc[2]);
  if constexpr (NS == 48) { STAMP1(28); STAMP3(28); }
#pragma unroll
  for (int t = 0; t < 3; ++t) { res[2 * t] = pk(acc[t][0], acc[t][1]); res[2 * t + 1] = pk(acc[t][2], acc[t][3]); }
  auto s0 = __builtin_amdgcn_permlane16_swap(res[0], res[2], false, false);
  auto s1 = __builtin_amdgcn_permlane16_swap(res[1], res[3], false, false);
  auto s2 = __builtin_amdgcn_permlane16_swap(res[4], res[4], false, false);
  auto s3 = __builtin_amdgcn_permlane16_swap(res[5], res[5], false, false);
  const v4u o01 = {s0[0], s1[0], s0[1], s1[1]};
  const v4u o2 = {s2[0], s3[0], s2[1], s3[1]};
  if (last) {
    GAS char* ob = (GAS char*)out + (size_t)(t16i * 16 + (lane & 15)) * 1280 + ct * 80;
    *(GAS v4u*)(ob + pi * 16) = o01;
    if (lane < 16) *(GAS v4u*)(ob + 64) = o2;
  } else {
    gst128(o01, ws, so1, 0);
    gst128(o2, ws, so2, 0);
    const f32x4 sv = acc[0] + acc[1] + acc[2], qv = acc[0] * acc[0] + acc[1] * acc[1] + acc[2] * acc[2];
    float s = (sv[0] + sv[1]) + (sv[2] + sv[3]), q = (qv[0] + qv[1]) + (qv[2] + qv[3]);
    s = sum_halves(sum_rows16(s));
    q = sum_halves(sum_rows16(q));
    if (!(ABL & 2)) gst64(v2u{asu(s), asu(q)}, ws, so3, 0);
  }
}

constexpr float QSCALE = 0.22360679774997896f * 1.4426950408889634f;
constexpr float NEG = -1e30f;

DI float max16(const f32x16& s) {
  const float a = max3(s[0], s[1], s[2]), b = max3(s[3], s[4], s[5]), c = max3(s[6], s[7], s[8]);
  const float d = max3(s[9], s[10], s[11]), e = max3(s[12], s[13], s[14]);
  return __builtin_elementwise_maximum(max3(a, b, c), max3(d, e, s[15]));
}
DI void kv_write(unsigned kb, int lane, v4u k1, v4u ta, v4u tb) {
  *(LAS v4u*)(kb + lane * 16) = k1;
  *(LAS v2u*)(kb + 1024 + (lane ^ 32) * 8) = v2u{ta[0], ta[1]};
  const unsigned vb = kb + 1536 + (lane & 31) * 48 + (lane >> 5) * 8;
  *(LAS v2u*)(vb) = v2u{ta[2], ta[3]};
  *(LAS v2u*)(vb + 16) = v2u{tb[0], tb[1]};
  *(LAS v2u*)(vb + 32) = v2u{tb[2], tb[3]};
}

DI int kmapO4(int P, int t) {
  const int p = P / 5, r = P - 5 * p;
  if (r == 2) return 20 * (2 * p + t) + 16;
  return 20 * (2 * p + (r > 2)) + 8 * t + 4 * (r > 2 ? r - 3 : r);
}
DI v4u tr_frag(unsigned a0, unsigned a1) { const v2u x = ldstr(a0), y = ldstr(a1); return v4u{x[0], x[1], y[0], y[1]}; }
DI float sum8(v4u f, float s) {
#pragma unroll
  for (int i = 0; i < 4; ++i) { const unsigned u = f[i]; s = dot2(u, 0x3f803f80u, s); }
  return s;
}

DI void csum_rows(f32x4 c, unsigned la) {
  unsigned o[8];
#pragma unroll
  for (int i = 0; i < 4; ++i) {
    const float v = c[i], hi = rbf(v), mid = rbf(v - hi), lo = rbf(v - hi - mid);
    o[2 * i] = pk(hi, mid); o[2 * i + 1] = pk(lo, 0.f);
  }
  *(LAS v4u*)la = v4u{o[0], o[1], o[2], o[3]};
  *(LAS v4u*)(la + 16) = v4u{o[4], o[5], o[6], o[7]};
}

#if defined(__HIP_DEVICE_COMPILE__) && NOPK
#define NO_PK __attribute__((target("no-packed-fp32-ops")))
#else
#define NO_PK
#endif
__global__ void NO_PK __launch_bounds__(768) mega_kernel(unsigned char* wsp, void* out, const unsigned char* xin, const unsigned char* wqkv, const unsigned char* wo,
                                                   const unsigned char* wg, const unsigned char* wu, const unsigned char* wd) {
  __shared__ __attribute__((aligned(16))) char smem[LDS_BYTES];
  const int wave = ufl(threadIdx.x >> 6);
  asm volatile("" :: "v"((LAS char*)smem) : "memory");
  const int bx = blockIdx.x & 7, br = blockIdx.x >> 3;
#define GMAP 5
  const int band = (bx & 4) + (br >> 3), j = (bx & 3) * 8 + (br & 7);
  const int ct = (j >> 4) * 8 + (j & 7), rsel = (j >> 3) & 1;
  const rsrc_t ws = __builtin_amdgcn_make_buffer_rsrc(wsp, 0, WS_USED, 0x00020000);
  constexpr unsigned OOR = 0x40000000u;
  if (wave == 0) lds_st(MISC + (fresh_lane() & 31) * 4, 0u);
  WG_BARRIER();
  const int tk = blockIdx.x * 8 + ((wave - 4) & 7);
  const unsigned sb = wave < 4 ? STG_X + (wave - 1) * 5120 : STG + (wave - 4) * 8192;
  {
    const int lane = fresh_lane();
    const bool isx = wave < 4;
    dma_raw(isx ? xin : wqkv, isx ? 2048 * 1280 : 640 * 3840, isx || (lane & 7) < 5 ? (lane >> 3) * 1280 + (lane & 7) * 16 : OOR,
            isx ? (band * 256 + j * 8) * 1280 + (wave - 1) * 640 : (tk >> 4) * (16 * 3840) + (tk & 15) * 80, isx ? 128 : 10240, sb,
            opq(isx ? (wave == 1 || wave == 2 ? 5 : 0) : tk < 640 ? 6 : 0));
  }

  if (wave == 0) {
    const int lane = fresh_lane();
    __builtin_amdgcn_s_setprio(3);
    const v4u wsd = {(unsigned)(uintptr_t)wsp, (unsigned)((uintptr_t)wsp >> 32) & 0xffffu, WS_USED, 0x00020000u};
    unsigned bud = 0;
    const unsigned tag = ufl(__builtin_amdgcn_raw_buffer_load_b32(ws, 0, OFF_EP, SC1)) + 1u;
    lds_st(L_TAG, tag);
    CBAR();
    lds_st(L_TAGV, 1u);
    for (;;) {
      if (((unsigned)w_ballot(rmw_poll_pairs((GAS unsigned*)(wsp + OFF_IN + (band * 32 + j) * 512), lane * 8, 0xfffffu, tag)) & 0xfffffu) == 0xfffffu) break;
      SPEND(bud);
    }
    lds_st(L_WQOK, 1u);
#pragma unroll 1
    for (int f = 0; f < 6; ++f) dma_fill(wsd, lane * 16, OFF_WQ + j * 81920 + f * UNIT, ARENA + f * UNIT);
    asm volatile("s_waitcnt vmcnt(50)" ::: "memory"); lds_st(L_LANDED, 1u);
    dma_fill(wsd, lane * 16, OFF_WQ + j * 81920 + 6 * UNIT, ARENA + 6 * UNIT);
    asm volatile("s_waitcnt vmcnt(50)" ::: "memory"); lds_st(L_LANDED, 2u);
    dma_fill(wsd, lane * 16, OFF_WQ + j * 81920 + 7 * UNIT, ARENA + 7 * UNIT);
    asm volatile("s_waitcnt vmcnt(50)" ::: "memory"); lds_st(L_LANDED, 3u);
    asm volatile("s_waitcnt vmcnt(40)" ::: "memory"); lds_st(L_LANDED, 4u);
    asm volatile("s_waitcnt vmcnt(30)" ::: "memory"); lds_st(L_LANDED, 5u);
    asm volatile("s_waitcnt vmcnt(20)" ::: "memory"); lds_st(L_LANDED, 6u);
    asm volatile("s_waitcnt vmcnt(10)" ::: "memory"); lds_st(L_LANDED, 7u);
    asm volatile("s_waitcnt vmcnt(0)" ::: "memory"); lds_st(L_LANDED, 8u);
    __builtin_amdgcn_s_setprio(PRIO_H);
    lds_wait_gt(L_W2OK, 0u, bud);
    unsigned issued = 8, published = 8;
#pragma unroll 1
    for (int lp = 1; lp < 8; ++lp) {
      const int L = lp >> 2, ph = lp & 3;
      unsigned long long units, needs_lo, needs_hi; int n; unsigned src;
      if (ph == 0) { units = 0x76543210ull; n = 8; src = OFF_WQ + j * 81920;
        needs_lo = 17ull | 25ull << 8 | 26ull << 16 | 27ull << 24 | 28ull << 32 | 29ull << 40 | 32ull << 48 | 33ull << 56; needs_hi = 0; }
      else if (ph == 1) { units = 0x54321ull; n = 5; src = OFF_WO + ct * 51200;
        needs_lo = 40ull | 41ull << 8 | 42ull << 16 | 43ull << 24 | 44ull << 32; needs_hi = 0; }
      else if (ph == 2) { units = 0x21CBA98760EDull; n = 12; src = OFF_WG + j * 122880;
        needs_lo = 30ull | 31ull << 8 | 39ull << 16 | 45ull << 24 | 46ull << 32 | 47ull << 40 | 47ull << 48 | 47ull << 56;
        needs_hi = 47ull | 47ull << 8 | 48ull << 16 | 49ull << 24; }
      else { units = 0xCBA9876ED543ull; n = 12; src = OFF_WD + ct * 122880;
        needs_lo = 50ull | 51ull << 8 | 52ull << 16 | 53ull << 24 | 54ull << 32 | 56ull << 40 | 57ull << 48 | 58ull << 56;
        needs_hi = 59ull | 60ull << 8 | 61ull << 16 | 62ull << 24; }
#pragma unroll 1
      for (int f = 0; f < n; ++f) {
        const int unit = (int)((units >> (4 * f)) & 15ull);
        const int nd = (int)(((f < 8 ? needs_lo >> (8 * f) : needs_hi >> (8 * (f - 8)))) & 255ull) + (L - 1) * NSTAGE;
        if (nd > 0) {
          unsigned v = lds_ld(L_PROG + (lane & 7) * 4);
          if (!w_all((int)v >= nd)) {
            if (published != issued) {
              if (issued - published >= 2) { asm volatile("s_waitcnt vmcnt(10)" ::: "memory"); lds_st(L_LANDED, issued - 1); }
              asm volatile("s_waitcnt vmcnt(0)" ::: "memory");
              published = issued; lds_st(L_LANDED, published);
            }
            prog_wait<SL_L>((unsigned)nd, bud);
          }
        }
        dma_fill(wsd, lane * 16, src + f * UNIT, ARENA + unit * UNIT);
        ++issued;
        if (issued - published > LDEPTH) {
          asm volatile("s_waitcnt vmcnt(%0)" :: "i"(10 * LDEPTH) : "memory");
          published = issued - LDEPTH; lds_st(L_LANDED, published);
        }
      }
    }
    asm volatile("s_waitcnt vmcnt(10)" ::: "memory");
    lds_st(L_LANDED, issued - 1);
    asm volatile("s_waitcnt vmcnt(0)" ::: "memory");
    lds_st(L_LANDED, issued);
    return;
  }

  if (wave >= 4) {
    const int cw = wave - 4, grp = cw >> 2, wi = cw & 3;
    const int qt = (1 - grp) * 4 + wi;
    const int t32_ = band * 8 + qt;
    const int t16_ = band * 16 + (1 - grp) * 8 + rsel * 4 + wi;
    const int rep = j & 3;
    Ring rg;
    rg.prog = L_PROG + cw * 4;
    rg.bud = 0;
#define PRIO_E 2
#define PRIO_L 1
    if (grp == 0) __builtin_amdgcn_s_setprio(PRIO_E); else __builtin_amdgcn_s_setprio(PRIO_L);
    const int dslot = qt + (qt < 4 ? 4 : 8);
    STAMP(30);
    STAMP(0);
    unsigned res[6] = {0u, 0u, 0u, 0u, 0u, 0u};
    unsigned tag;
    {
      const int lane = fresh_lane();
      const int gam = lane >> 4, q = (lane & 15) >> 2, p = lane & 3, hh = gam >> 1, g = (gam & 1) * 4 + p;
      const v4u ones = {0x3f803f80u, 0x3f803f80u, 0x3f803f80u, 0x3f803f80u};
      f32x16 z;
#pragma unroll
      for (int i = 0; i < 16; ++i) z[i] = 0.f;
      if (cw == 7) {
        const int ci = j * 7 + lane;
        gst32(0u, ws, lane < 7 ? OFF_CTR + ((ci >> 5) * 64 + band * 8 + ((ci >> 2) & 7)) * 1024 + (ci & 3) * 256 : OOR);
      }
      if (tk < 640) {
        const int hp = tk & 15, ks = tk >> 4;
        const unsigned r = sb + (8 * hh + q) * 384;
        unsigned c0 = r + (g < 4 ? 0 : 128) + (g & 3) * 8, c1 = r + (g == 0 ? 32 : g == 1 ? 128 + 32 : 256 + ((g < 7 ? g : 6) - 2) * 8);
        unsigned dst = OFF_WQ + hp * 163840 + ks * 2048 + lane * 16, cs = OFF_CSQ + (hp * 80 + ks) * 256 + lane * 4;
        unsigned fa = lane < 16 ? OFF_IN + ((lane >> 1) * 32 + 2 * hp + (lane & 1)) * 512 + ks * 4 : OOR;
        STAMP4(24);
#pragma unroll 1
        for (int pass = DRYPASS ? 0 : 1; pass < 2; ++pass) {
          const unsigned off = pass ? 0u : OOR;
          if (pass) DRAIN();
          STAMP4(25);
          v4u f[2][2];
#pragma unroll
          for (int h2 = 0; h2 < 2; ++h2) {
            f[h2][0] = tr_frag(c0 + h2 * 40, c0 + h2 * 40 + 1536);
            f[h2][1] = tr_frag(c1 + h2 * 40, c1 + h2 * 40 + 1536);
          }
#pragma unroll
          for (int h2 = 0; h2 < 2; ++h2) {
            if ((lane & 31) >= 28) f[h2][1] = v4u{0u, 0u, 0u, 0u};
            gst128(f[h2][0], ws, dst + off + h2 * 81920, 0); gst128(f[h2][1], ws, dst + off + h2 * 81920 + 1024, 0);
          }
#pragma unroll
          for (int h2 = 0; h2 < 2; ++h2) {
            const float s0 = mfma32(ones, f[h2][0], z)[0], s1 = mfma32(ones, f[h2][1], z)[0];
            gst32(asu(lane < 32 ? s0 : s1), ws, cs + off + h2 * 10240);
          }
          if (pass) lds_wait_gt(L_TAGV, 0u, rg.bud);
          tag = ufl(lds_ld(L_TAG));
          STAMP4(26);
          if (pass) DRAIN();
          STAMP4(27);
          gst32(tag, ws, fa + off);
        }
        lds_add1(L_CRIT);
      } else {
        const bool gu = tk < 1280, dn = tk < 1664;
        const int i = gu ? tk - 640 : dn ? tk - 1280 : tk - 1664;
        unsigned a0, a1, a2, a3, dst, o2;
        if (L2GATE) lds_wait_gt(L_CRIT, L2GATE - 1, rg.bud);
        if (gu) {
          const unsigned vo = (lane & 15) < 12 ? gam * 3072 + (lane & 15) * 16 : OOR, so = (i >> 4) * (16 * 3072) + (i & 15) * 192;
          dma_raw(wg, 640 * 3072, vo, so, 4 * 3072, sb, 4);
          dma_raw(wu, 640 * 3072, vo, so, 4 * 3072, sb + 4096, 4);
          const unsigned r = sb + (8 * hh + q) * 256;
          a0 = r + g * 8; a1 = a0 + 4096; a2 = r + 64 + (g & 3) * 8 + (g < 4 ? 0 : 4096); a3 = 96;
          dst = OFF_WG + (i & 15) * 245760 + (i >> 4) * 3072; o2 = 2048 + lane * 16;
        } else {
          const unsigned vo = (lane & 15) < 10 ? gam * 1280 + (lane & 15) * 16 : OOR;
          if (tk < 1824) {
#pragma unroll 1
            for (int u = 0; u < 8; ++u) {
              const int P = 4 * (i >> 3) + (u >> 1);
              const int row = dn ? 16 * (P >> 1) + 4 * (P & 1) + 8 * (u & 1) : kmapO4(P, u & 1);
              dma_raw(dn ? wd : wo, dn ? 1536 * 1280 : 640 * 1280, vo, row * 1280 + (i & 7) * 160, 0, sb + u * 1024, 1);
            }
          }
          const unsigned r = sb + gam * 2048 + q * 256;
          a0 = r + p * 8; a1 = a0 + 32; a2 = r + 64 + (p & 1) * 8; a3 = 80;
          dst = tk >= 1824 ? OOR : dn ? OFF_WD + (i & 7) * 245760 + (i >> 3) * 2560 : OFF_WO + (i & 7) * 102400 + (i >> 3) * 2560;
          o2 = (lane & 15) < 8 ? 2048 + gam * 128 + (lane & 7) * 16 : OOR;
        }
        const unsigned dstep = gu || dn ? 122880 : 51200;
        STAMP4(24);
        asm volatile("s_waitcnt vmcnt(0)" : "+v"(a0), "+v"(a1), "+v"(a2), "+v"(o2) :: "memory");
        STAMP4(25);
        v4u f[2][3];
#pragma unroll
        for (int h2 = 0; h2 < 2; ++h2) {
          f[h2][0] = tr_frag(a0 + h2 * a3, a0 + h2 * a3 + 1024);
          f[h2][1] = tr_frag(a1 + h2 * a3, a1 + h2 * a3 + 1024);
          f[h2][2] = tr_frag(a2 + h2 * a3, a2 + h2 * a3 + 1024);
        }
        if (S2GATE) lds_wait_gt(L_CRIT, S2GATE - 1, rg.bud);
#pragma unroll
        for (int h2 = 0; h2 < 2; ++h2) {
          gst128(f[h2][0], ws, dst + h2 * dstep + lane * 16, 0); gst128(f[h2][1], ws, dst + h2 * dstep + lane * 16 + 1024, 0);
          gst128(f[h2][2], ws, dst + h2 * dstep + o2, 0);
        }
        if (gu) {
#pragma unroll
          for (int h2 = 0; h2 < 2; ++h2) {
            const float s0 = mfma32(ones, f[h2][0], z)[0], s1 = mfma32(ones, f[h2][1], z)[0], s2 = mfma32(ones, f[h2][2], z)[0];
            const unsigned cs = OFF_CSG + ((2 * (i & 15) + h2) * 40 + (i >> 4)) * 512 + lane * 4;
            gst32(asu(lane < 32 ? s0 : s1), ws, cs);
            gst32(lane < 32 ? asu(s2) : 0u, ws, cs + 256);
          }
        }
        lds_wait_gt(L_TAGV, 0u, rg.bud);
        tag = ufl(lds_ld(L_TAG));
        STAMP4(26);
        DRAIN();
        STAMP4(27);
      }
      if (lds_inc1(L_STG) == 7u) gst32(tag, ws, lane == 0 ? OFF_F2 + blockIdx.x * 4 : OOR);
    }
    STAMP4(28);
#pragma unroll 1
    for (int L = 0; L < 2; ++L) {
      rg.lf = L * NFILL; rg.ls = L * NSTAGE;
      const int t32i = opq(t32_), t16i = opq(t16_);
      PP(PHPRIO & 3);
      v4u q1; v2u q2;
      {
        f32x16 acc[2]; float rs;
        gemm32<2, WQ_, DQ_, 0, 0, TabQ, L_CQ>(acc, rs, rg, ws, OFF_XA + (2 * L) * XA_SZ + t32i * XA_T, OFF_ST + (2 * L) * ST_SZ + t32i * 4096,
                                            [&]() INL {
          if (HINT && HINT < 3) {
            const int lane = fresh_lane(), h = lane >> 5;
            const v4u sv = {SENT, SENT, SENT, SENT};
            if (band < 7) {
              const unsigned gb = OFF_KVH + L * KVH_SZ + ((band * 32 + j) * 8 + qt) * KVH_T;
              gst128(sv, ws, lane * 16, gb); gst128(sv, ws, lane * 16 + 1024, gb); gst128(sv, ws, lane * 16 + 2048, gb);
            }
            const unsigned ob = OFF_OA + L * OA_SZ + (2 * t32i) * OA_T + (5 * (j >> 1)) * 256;
            const unsigned tv = ((lane >> 4) & 1) * OA_T + (lane & 15) * 16;
            gst128(sv, ws, tv + h * 256, ob + (3 * (j & 1)) * 256);
            gst64(v2u{SENT, SENT}, ws, tv + (h ? WS_USED : 0u), ob + 2 * 256 + (j & 1) * 8);
          }
          if (L > 0) {
            ctr_wait(wsp, 3, t32i, rep, rg.bud);
            if (TURNQ && grp == 0) lds_wait_gt(rg.prog + 16, 25 + TURNQ, rg.bud);
          } else {
            DRAIN();
            unsigned xr = 0;
            lds_wait_bits(L_XRDY, 0xfu << (4 * qt), xr, rg.bud);
            if (TURN0 > 0 && grp == 1) lds_wait_gt(rg.prog - 16, TURN0 - 1, rg.bud);
          }
          STAMP(1 + L * 12); },
          [&]() INL { if (L > 0) prog_wait(L * NSTAGE, rg.bud); else lds_wait_gt(L_STG, 7u, rg.bud); });
        STAMP(2 + L * 12);
        const int lane = fresh_lane(), h = lane >> 5;
        v4u k1, ta, tb;
        const float rq = rs * QSCALE, r0 = h ? rs : rq;
#pragma unroll
        for (int i = 0; i < 4; ++i) k1[i] = pk(acc[0][8 + 2 * i] * rs, acc[0][9 + 2 * i] * rs);
        ta[0] = pk(acc[1][0] * r0, acc[1][1] * r0); ta[1] = pk(acc[1][2] * r0, acc[1][3] * r0);
        ta[2] = pk(acc[1][4] * rs, acc[1][5] * rs); ta[3] = pk(acc[1][6] * rs, acc[1][7] * rs);
#pragma unroll
        for (int i = 0; i < 4; ++i) tb[i] = pk(acc[1][8 + 2 * i] * rs, acc[1][9 + 2 * i] * rs);
        if (h) { tb[2] = 0x3f803f80u; tb[3] = 0x3f803f80u; }
        q2[0] = h ? 0x3f80u : ta[0]; q2[1] = h ? 0u : ta[1];
        if (!h) { ta[0] = 0u; ta[1] = 0u; }
        if (band < 7 && !(ABL & 1)) {
          const unsigned gb = OFF_KVH + L * KVH_SZ + ((band * 32 + j) * 8 + qt) * KVH_T;
          gst128(k1, ws, lane * 16, gb); gst128(ta, ws, lane * 16 + 1024, gb); gst128(tb, ws, lane * 16 + 2048, gb);
          if (HINT) gst32(tag, ws, lane == 0 ? OFF_HALOF + (((L * 8 + band) * 32 + j) * 2 + (qt >> 2)) * 256 + (qt & 3) * 4 : WS_USED);
        }
        kv_write(KV_OFF + dslot * KV_T, lane, k1, ta, tb);
        CBAR();
        lds_or1(L_RDY + L * 4, 1u << dslot);
#pragma unroll
        for (int i = 0; i < 4; ++i) q1[i] = pk(acc[0][2 * i] * rq, acc[0][2 * i + 1] * rq);
      }
      STAMP(3 + L * 12);
      PP((PHPRIO >> 2) & 3);
      {
        const int lane = fresh_lane(), h = lane >> 5;
        f32x16 o, negm, sc[2];
        const int topA = dslot, topB = grp == 0 ? 8 : 12;
        const int top0 = grp == 0 ? topA : topB, top1 = grp == 0 ? topB : topA;
        const unsigned vlo = 1536 + (4 * h + ((lane & 15) >> 2)) * 48 + (((lane >> 4) & 1) == 0 ? 8 * (lane & 3) : ((lane & 3) == 0 ? 32 : 40));
        unsigned ka[2], kb[2], va[2], vd;
        ka[0] = KV_OFF + (top0 - 4) * KV_T + lane * 16; ka[1] = KV_OFF + (top1 - 4) * KV_T + lane * 16;
        kb[0] = KV_OFF + (top0 - 4) * KV_T + 1024 + lane * 8; kb[1] = KV_OFF + (top1 - 4) * KV_T + 1024 + lane * 8;
        va[0] = KV_OFF + (top0 - 4) * KV_T + vlo; va[1] = KV_OFF + (top1 - 4) * KV_T + vlo;
        vd = KV_OFF + dslot * KV_T + vlo;
        asm volatile("" : "+v"(ka[0]), "+v"(ka[1]), "+v"(kb[0]), "+v"(kb[1]), "+v"(va[0]), "+v"(va[1]), "+v"(vd));
        const int ql = (lane & 31) - 4 * h;
        unsigned rdyv = lds_ld(L_RDY + L * 4);
        {
          f32x16 z;
#pragma unroll
          for (int r = 0; r < 16; ++r) { z[r] = 0.f; o[r] = 0.f; }
          sc[0] = mfma32(lds128(KV_OFF + dslot * KV_T + lane * 16), q1, z);
          sc[0] = mfma32k8(lds64(KV_OFF + dslot * KV_T + 1024 + lane * 8), q2, sc[0]);
#pragma unroll
          for (int r = 0; r < 16; ++r) sc[0][r] = ((r & 3) + 8 * (r >> 2)) <= ql ? sc[0][r] : NEG;
          const float m = max_halves(max16(sc[0]));
#pragma unroll
          for (int r = 0; r < 16; ++r) { sc[0][r] -= m; negm[r] = -m; }
        }
        STAMP2(24);
        unsigned rdy = ufl(rdyv);
        sfor<0, 9>([&](auto N) INL {
          if constexpr ((ABL & 4) && N.v > 0 && N.v < 8) return;
          constexpr int n = N.v;
          f32x16& s = sc[n & 1];
          f32x16& sn = sc[(n + 1) & 1];
          v4u k1; v2u k2, v0, v1, v2, v3;
          if constexpr (n == ANN) {
            if (!HINT && band < 7 && !(ABL & 1)) {
              DRAIN();
              gst32(tag, ws, lane == 0 ? OFF_HALOF + (((L * 8 + band) * 32 + j) * 2 + (qt >> 2)) * 256 + (qt & 3) * 4 : WS_USED);
            }
            STAMP2(25);
          }
          {
            const unsigned a = n == 0 ? vd : va[(n - 1) >> 2] + (3 - ((n - 1) & 3)) * KV_T;
            v0 = ldstr(a); v1 = ldstr(a + 384); v2 = ldstr(a + 768); v3 = ldstr(a + 1152);
          }
          if constexpr (n < 8) {
            lds_wait_bits(L_RDY + L * 4, 1u << ((n < 4 ? top0 : top1) - 1 - (n & 3)), rdy, rg.bud);
            k1 = lds128(ka[n >> 2] + (3 - (n & 3)) * KV_T); k2 = lds64(kb[n >> 2] + (3 - (n & 3)) * KV_T);
          }
          if constexpr (n == 4 || n == 8) {
            if (grp == (n == 4 ? 0 : 1)) {
#pragma unroll
              for (int r = 0; r < 16; ++r) s[r] = ((r & 3) + 8 * (r >> 2)) > ql ? s[r] : NEG;
            }
          }
          SCHED();
          v4u p, p2;
          float e[16];
          e[0] = ex2(s[0]); e[1] = ex2(s[1]); e[2] = ex2(s[2]); SCHED();
          p[0] = pk(e[0], e[1]); e[3] = ex2(s[3]); e[4] = ex2(s[4]); SCHED();
          p[1] = pk(e[2], e[3]); e[5] = ex2(s[5]); e[6] = ex2(s[6]); SCHED();
          if constexpr (n < 8) sn = mfma32(k1, q1, negm);
          SCHED();
          p[2] = pk(e[4], e[5]); e[7] = ex2(s[7]); e[8] = ex2(s[8]); SCHED();
          p[3] = pk(e[6], e[7]); SCHED();
          if constexpr (n < 8) sn = mfma32k8(k2, q2, sn);
          SCHED();
          e[9] = ex2(s[9]); e[10] = ex2(s[10]); SCHED();
          p2[0] = pk(e[8], e[9]); e[11] = ex2(s[11]); SCHED();
          o = mfma32(v4u{v0[0], v0[1], v1[0], v1[1]}, p, o);
          SCHED();
          e[12] = ex2(s[12]); SCHED();
          p2[1] = pk(e[10], e[11]); e[13] = ex2(s[13]); e[14] = ex2(s[14]); e[15] = ex2(s[15]); SCHED();
          p2[2] = pk(e[12], e[13]); SCHED();
          p2[3] = pk(e[14], e[15]); SCHED();
          o = mfma32(v4u{v2[0], v2[1], v3[0], v3[1]}, p2, o);
          SCHED();
          if constexpr (n == 3) STAMP2(26);
          if constexpr (n == 5) STAMP2(27);
          if constexpr (n == 7) STAMP2(28);
        });
        if (__builtin_expect(w_any(!(o[12] < 1e18f)), 0)) {
          float m = NEG;
#pragma unroll
          for (int r = 0; r < 16; ++r) { o[r] = 0.f; negm[r] = 0.f; }
#pragma unroll 1
          for (int n = 0; n < 9; ++n) {
            const int sl = n < 5 ? dslot - n : topB + 4 - n;
            const unsigned kt = KV_OFF + sl * KV_T;
            f32x16 s = mfma32(lds128(kt + lane * 16), q1, negm);
            s = mfma32k8(lds64(kt + 1024 + lane * 8), q2, s);
#pragma unroll
            for (int r = 0; r < 16; ++r) {
              const bool lo = ((r & 3) + 8 * (r >> 2)) <= ql;
              s[r] = (n == 0 && !lo) || (n == 4 && lo) ? NEG : s[r];
            }
            const float mn = fmaxf(m, max_halves(max16(s))), f = ex2(m - mn);
            m = mn;
            v4u p, p2;
#pragma unroll
            for (int i = 0; i < 4; ++i) { p[i] = pk(ex2(s[2 * i] - m), ex2(s[2 * i + 1] - m)); p2[i] = pk(ex2(s[8 + 2 * i] - m), ex2(s[9 + 2 * i] - m)); }
#pragma unroll
            for (int r = 0; r < 16; ++r) o[r] *= f;
            const v2u v0 = ldstr(kt + vlo), v1 = ldstr(kt + vlo + 384), v2 = ldstr(kt + vlo + 768), v3 = ldstr(kt + vlo + 1152);
            o = mfma32(v4u{v0[0], v0[1], v1[0], v1[1]}, p, o);
            o = mfma32(v4u{v2[0], v2[1], v3[0], v3[1]}, p2, o);
          }
        }
        CBAR();
        lds_st(rg.prog, rg.ls + 9);
        CBAR();
        const float inv = __builtin_amdgcn_rcpf(o[12]);
        v4u o1;
#pragma unroll
        for (int i = 0; i < 4; ++i) o1[i] = pk(o[2 * i] * inv, o[2 * i + 1] * inv);
        const v2u o2 = {pk(o[8] * inv, o[9] * inv), pk(o[10] * inv, o[11] * inv)};
        const unsigned ob = OFF_OA + L * OA_SZ + (2 * t32i) * OA_T + (5 * (j >> 1)) * 256;
        const unsigned tv = ((lane >> 4) & 1) * OA_T + (lane & 15) * 16;
        gst128(o1, ws, tv + h * 256, ob + (3 * (j & 1)) * 256);
        gst64(o2, ws, tv + (h ? WS_USED : 0u), ob + 2 * 256 + (j & 1) * 8);
        STAMP(4 + L * 12);
        if (L == 0) lds_wait_gt(L_W2OK, 0u, rg.bud);
        ctr_arrive(wsp, 4 * L, t32i);
        if (L == 0) {
          const int g4 = lane >> 4;
          const unsigned ro = ((t16i & 1) * 16 + (lane & 15)) * 16 + (g4 & 1) * 8, rb = OFF_XA + (t16i >> 1) * XA_T + (5 * ct) * 512;
          const v2u r0 = gld64(ws, ro + (g4 >> 1) * 512, rb), r1 = gld64(ws, ro + (g4 >> 1) * 512 + 1024, rb), r2 = gld64(ws, g4 < 2 ? ro + 2048 : WS_USED, rb);
          res[0] = r0[0]; res[1] = r0[1]; res[2] = r1[0]; res[3] = r1[1]; res[4] = r2[0]; res[5] = r2[1];
        }
      }
      STAMP(5 + L * 12);
      PP((PHPRIO >> 4) & 3);
      gemm16<20, WO_, 3, 8, 9, TabO>(rg, ws, OFF_OA + L * OA_SZ + t16i * OA_T, res, OFF_XA + (2 * L + 1) * XA_SZ + (t16i >> 1) * XA_T,
                                     OFF_ST + (2 * L + 1) * ST_SZ + (t16i >> 1) * 4096, false, out, t16i, ct,
                                     [&]() INL { ctr_wait(wsp, 4 * L, t16i >> 1, rep, rg.bud); STAMP(6 + L * 12); });
      STAMP(7 + L * 12);
      ctr_arrive(wsp, 4 * L + 1, t16i >> 1);
      PP((PHPRIO >> 6) & 3);
      {
        f32x16 acc[3]; float rs;
        gemm32<3, WG_, DG_, 13, 14, TabG, L_CG>(acc, rs, rg, ws, OFF_XA + (2 * L + 1) * XA_SZ + t32i * XA_T, OFF_ST + (2 * L + 1) * ST_SZ + t32i * 4096,
                                              [&]() INL {
          if (HINT && HINT < 3) {
            const int lane = fresh_lane();
            const v4u sv = {SENT, SENT, SENT, SENT};
            const unsigned hb = OFF_HA + L * HA_SZ + (2 * t32i) * HA_T + (6 * j) * 256;
            const unsigned tv = ((lane >> 4) & 1) * HA_T + (lane & 15) * 16 + (lane >> 5) * 256;
            gst128(sv, ws, tv, hb); gst128(sv, ws, tv + 512, hb); gst128(sv, ws, tv + 1024, hb);
          }
          ctr_wait(wsp, 4 * L + 1, t32i, rep, rg.bud); STAMP(8 + L * 12); STAMP1(24); }, []() INL {});
        STAMP1(25); STAMP1(26); STAMP5(28);
        const int lane = fresh_lane(), h = lane >> 5;
        const float r2 = rs * rs;
        const unsigned hb = OFF_HA + L * HA_SZ + (2 * t32i) * HA_T + (6 * j) * 256;
        const unsigned tv = ((lane >> 4) & 1) * HA_T + (lane & 15) * 16 + h * 256;
        v4u hv;
#pragma unroll
        for (int i = 0; i < 4; ++i) hv[i] = pk(acc[0][2 * i] * r2 * acc[1][2 * i], acc[0][2 * i + 1] * r2 * acc[1][2 * i + 1]);
        gst128(hv, ws, tv, hb);
#pragma unroll
        for (int i = 0; i < 4; ++i) hv[i] = pk(acc[0][8 + 2 * i] * r2 * acc[1][8 + 2 * i], acc[0][9 + 2 * i] * r2 * acc[1][9 + 2 * i]);
        gst128(hv, ws, tv + 512, hb);
#pragma unroll
        for (int i = 0; i < 4; ++i) hv[i] = pk(acc[2][2 * i] * r2 * acc[2][8 + 2 * i], acc[2][2 * i + 1] * r2 * acc[2][9 + 2 * i]);
        gst128(hv, ws, tv + 1024, hb);
      }
      STAMP(9 + L * 12);
      ctr_arrive(wsp, 4 * L + 2, t32i);
      PP((PHPRIO >> 8) & 3);
      gemm16<48, WD_, 3, 25, 26, TabD>(rg, ws, OFF_HA + L * HA_SZ + t16i * HA_T, res, OFF_XA + 2 * XA_SZ + (t16i >> 1) * XA_T,
                                       OFF_ST + 2 * ST_SZ + (t16i >> 1) * 4096, L == 1, out, t16i, ct,
                                       [&]() INL { ctr_wait(wsp, 4 * L + 2, t16i >> 1, rep, rg.bud); STAMP(10 + L * 12); });
      STAMP(11 + L * 12);
      if (L == 0) ctr_arrive(wsp, 3, t16i >> 1);
    }
  } else {
    const int lane = fresh_lane();
    const int half = 2 - wave;
    __builtin_amdgcn_s_setprio(3);
    unsigned bud = 0;
    unsigned tag = 0;
    if (wave == 3) { lds_wait_gt(L_TAGV, 0u, bud); tag = ufl(lds_ld(L_TAG)); }
    if (wave < 3) {
      const int hx = wave - 1;
      {
        const unsigned xa = OFF_XA + (band * 8 + (j >> 2)) * XA_T + ((j & 3) * 8 + (lane & 7)) * 16 + (hx * 40 + (lane >> 3)) * 512;
        unsigned la = STG_X + hx * 5120 + (lane & 7) * 128 + (lane >> 3) * 16;
        unsigned sa = OFF_ST + (band * 8 + (j >> 2)) * 4096 + st_line(hx * 8 + (lane >> 3), (j >> 1) & 1) + (lane & 7) * 16 + (j & 1) * 8;
        unsigned fa = lane < 32 ? OFF_IN + (band * 32 + lane) * 512 + 256 + (j * 2 + hx) * 4 : OOR;
#pragma unroll 1
        for (int pass = DRYPASS ? 0 : 1; pass < 2; ++pass) {
          const unsigned off = pass ? 0u : OOR;
          if (pass) DRAIN();
          v4u xv[5];
#pragma unroll
          for (int i = 0; i < 5; ++i) xv[i] = lds128(la + i * 1024);
#pragma unroll
          for (int i = 0; i < 5; ++i) gst128(xv[i], ws, xa + off + i * 4096, 0);
          float s = 0.f, qq = 0.f;
#pragma unroll
          for (int i = 0; i < 5; ++i) {
            s = sum8(xv[i], s);
#pragma unroll
            for (int k = 0; k < 4; ++k) { const unsigned u = xv[i][k]; qq = dot2(u, u, qq); }
          }
          gst64(v2u{asu(s), asu(qq)}, ws, sa + off, 0);
          if (pass) lds_wait_gt(L_TAGV, 0u, bud);
          tag = ufl(lds_ld(L_TAG));
          if (pass) DRAIN();
          gst32(tag, ws, fa + off);
        }
      }
      lds_add1(L_CRIT);
    } else {
      for (;;) {
        const unsigned m = (unsigned)w_ballot(rmw_poll_pairs((GAS unsigned*)(wsp + OFF_IN + (band * 32 + j) * 512 + 256), lane * 8, 0xffffffffu, tag));
        lds_st(L_XRDY, m);
        if (m == 0xffffffffu) break;
        SPEND(bud);
      }
      lds_wait_gt(L_WQOK, 0u, bud);
      v4u pc[10];
      sfor<0, 10>([&](auto I) INL { pc[I.v] = lda<I.v>(ws, lane * 16, OFF_CSQ + j * 10240); });
      f32x4 c = __builtin_bit_cast(f32x4, pc[0]);
#pragma unroll
      for (int i = 1; i < 10; ++i) c += __builtin_bit_cast(f32x4, pc[i]);
#pragma unroll
      for (int i = 0; i < 4; ++i) c[i] = sum_halves(sum_rows16(c[i]));
      if (lane < 16) csum_rows(c, L_CQ + lane * 32);
      CBAR();
      lds_st(L_CSUM, 1u);
    }
    __builtin_amdgcn_s_setprio(PRIO_H);
    if (wave == 3) {
      lds_wait_gt(L_PROG, F2GATE, bud);
      for (;;) {
        const v4u f0 = gld128(ws, lane * 16, OFF_F2);
        const bool ok = f0[0] == tag && f0[1] == tag && f0[2] == tag && f0[3] == tag;
        if (w_all(ok)) break;
        SPEND(bud);
        __builtin_amdgcn_s_sleep(16);
        CBAR();
      }
      CBAR();
      lds_st(L_W2OK, 1u);
      if (blockIdx.x == 0) gst32(tag, ws, lane == 0 ? OFF_EP : WS_USED);
      f32x4 c = {0.f, 0.f, 0.f, 0.f};
#pragma unroll 1
      for (int r = 0; r < 2; ++r) {
        v4u pc[10];
        sfor<0, 10>([&](auto I) INL { pc[I.v] = lda<I.v>(ws, lane * 16, OFF_CSG + j * 20480 + r * 10240); });
#pragma unroll
        for (int i = 0; i < 10; ++i) c += __builtin_bit_cast(f32x4, pc[i]);
      }
#pragma unroll
      for (int i = 0; i < 4; ++i) c[i] = sum_halves(c[i]);
      if (lane < 24) csum_rows(c, L_CG + lane * 32);
      CBAR();
      lds_st(L_CSUM, 2u);
      return;
    }
    if (band == 0 || (ABL & 1)) {
#pragma unroll 1
      for (int L = 0; L < 2; ++L) {
        if (L > 0) prog_wait<SL_F>(L * NSTAGE, bud); else lds_wait_gt(L_STG, 7u, bud);
        const v4u z = {0u, 0u, 0u, 0u};
#pragma unroll 1
        for (int sl = 0; sl < 4; ++sl) kv_write(KV_OFF + (8 * half + sl) * KV_T, lane, z, v4u{lane < 32 ? 0xf149u : 0u, 0u, 0u, 0u}, z);
        CBAR();
        lds_or1(L_RDY + L * 4, 0xfu << (8 * half));
      }
    } else {
#pragma unroll 1
      for (int it = 0; it < 4; ++it) {
        const int L = it >> 1, hf = 1 - (it & 1);
        __builtin_amdgcn_s_setprio(PRIO_H);
        lds_wait_gt<SL_F>(L_PROG + (1 - hf) * 16, L * NSTAGE + FGATE, bud);
        if (L > 0) prog_wait(L * NSTAGE, bud); else lds_wait_gt(L_STG, 7u, bud);
        __builtin_amdgcn_s_setprio(PRIO_F);
        unsigned pend = 3u;
        GAS unsigned* fl = (GAS unsigned*)(wsp + OFF_HALOF + (((L * 8 + band - 1) * 32 + j) * 2 + hf) * 256 + half * 8);
        const unsigned gb = OFF_KVH + L * KVH_SZ + (((band - 1) * 32 + j) * 8 + 4 * hf + 2 * half) * KVH_T;
        while (pend) {
          const unsigned nw = (unsigned)w_ballot(rmw_poll_lanes(fl, lane * 4, 3u, 0u, 0u) == tag) & pend;
          __builtin_amdgcn_fence(__ATOMIC_ACQUIRE, "wavefront");
          CBAR();
          if (!nw) {
            SPEND(bud);
            continue;
          }
          pend &= ~nw;
          v4u x[2][3];
#pragma unroll
          for (int sl = 1; sl >= 0; --sl) {
            const unsigned vo = lane * 16 + ((nw >> sl & 1) ? 0u : WS_USED);
#pragma unroll
            for (int i = 0; i < 3; ++i) x[sl][i] = gld128(ws, vo + i * 1024, gb + sl * KVH_T);
          }
#pragma unroll
          for (int sl = 1; sl >= 0; --sl)
            if (nw >> sl & 1) {
              kv_write(KV_OFF + (8 * hf + 2 * half + sl) * KV_T, lane, x[sl][0], x[sl][1], x[sl][2]);
              CBAR();
              lds_or1(L_RDY + L * 4, 1u << (8 * hf + 2 * half + sl));
            }
        }
      }
    }
  }
}

extern "C" void kernel_launch(void* const* d_in, const int* in_sizes, int n_in, void* d_out, int out_size, void* d_ws, size_t ws_size,
                              hipStream_t stream) {
  typedef const unsigned char* cp;
  mega_kernel<<<256, 768, 0, stream>>>((unsigned char*)d_ws, d_out, (cp)d_in[0], (cp)d_in[1], (cp)d_in[2], (cp)d_in[4], (cp)d_in[5], (cp)d_in[6]);
}
```

```cpp
#include <hip/hip_runtime.h>
#include <hip/hip_bf16.h>
#include <stdint.h>
#pragma clang diagnostic ignored "-Wint-to-pointer-cast"

typedef unsigned v4u __attribute__((ext_vector_type(4)));
typedef unsigned v2u __attribute__((ext_vector_type(2)));
typedef short s16x8 __attribute__((ext_vector_type(8)));
typedef short s16x4 __attribute__((ext_vector_type(4)));
typedef float f32x16 __attribute__((ext_vector_type(16)));
typedef float f32x4 __attribute__((ext_vector_type(4)));
typedef float f32x2 __attribute__((ext_vector_type(2)));
typedef __bf16 bf2 __attribute__((ext_vector_type(2)));
typedef unsigned short u16;
#define LAS __attribute__((address_space(3)))
#define GAS __attribute__((address_space(1)))
#define DI __device__ __forceinline__
#define INL __attribute__((always_inline))
#define RLX_AGENT __ATOMIC_RELAXED, __HIP_MEMORY_SCOPE_AGENT
#define RLX_WG __ATOMIC_RELAXED, __HIP_MEMORY_SCOPE_WORKGROUP
#define CBAR() asm volatile("" ::: "memory")
#define SCHED() __builtin_amdgcn_sched_barrier(0)

constexpr unsigned OFF_CTR = 0;
constexpr unsigned OFF_HALOF = 7 * 64 * 1024;
constexpr unsigned OFF_IN = OFF_HALOF + 2 * 8 * 32 * 512;
constexpr unsigned OFF_F2 = OFF_IN + 256 * 512;
constexpr unsigned OFF_EP = OFF_F2 + 1024;
constexpr unsigned OFF_STAMP = 64u << 20;
constexpr unsigned OFF_WQ = 1u << 20;
constexpr unsigned OFF_WO = OFF_WQ + 32 * 81920;
constexpr unsigned OFF_WG = OFF_WO + 16 * 51200;
constexpr unsigned OFF_WD = OFF_WG + 32 * 122880;
constexpr unsigned XA_T = 40960, XA_SZ = 64 * XA_T;
constexpr unsigned OFF_XA = OFF_WD + 16 * 122880;
constexpr unsigned ST_SZ = 64 * 4096;
constexpr unsigned OFF_ST = OFF_XA + 4 * XA_SZ;
constexpr unsigned OA_T = 20480, OA_SZ = 128 * OA_T;
constexpr unsigned OFF_OA = OFF_ST + 4 * ST_SZ;
constexpr unsigned HA_T = 49152, HA_SZ = 128 * HA_T;
constexpr unsigned OFF_HA = OFF_OA + 2 * OA_SZ;
constexpr unsigned KVH_T = 3072, KVH_SZ = 8 * 32 * 8 * KVH_T;
constexpr unsigned OFF_KVH = OFF_HA + 2 * HA_SZ;
constexpr unsigned OFF_CSQ = OFF_KVH + 2 * KVH_SZ;
constexpr unsigned OFF_CSG = OFF_CSQ + 32 * 40 * 256;
constexpr unsigned WS_USED = OFF_CSG + 32 * 40 * 512;
static_assert(OFF_EP + 256 <= OFF_WQ, "");

constexpr int UNIT = 10240;
constexpr int MISC = 0, ARENA = 2048;
constexpr int KV_OFF = ARENA + 8 * UNIT, KV_T = 3072;
constexpr int L_PROG = MISC, L_LANDED = MISC + 64, L_RDY = MISC + 68, L_CSUM = MISC + 76;
constexpr int L_TAG = MISC + 80, L_TAGV = MISC + 84, L_XRDY = MISC + 88, L_STG = MISC + 92, L_WQOK = MISC + 96, L_W2OK = MISC + 100, L_CRIT = MISC + 104;
constexpr int STG = ARENA + 8 * UNIT;
constexpr int STG_X = STG + 65536;
constexpr int L_CQ = MISC + 256, L_CG = MISC + 768, L_STAMP = ARENA + 15 * UNIT + 4096;
constexpr int LDS_BYTES = ARENA + 15 * UNIT + 8192;
#define LDEPTH 2
#define WQ_ 12
#define WG_ 12
#define WO_ 10
#define WD_ 10
#define PRIO_H 0
#define PHPRIO 438
#define PP(p) do { if (PHPRIO) __builtin_amdgcn_s_setprio(p); } while (0)
#define PRETOUCH 0
#define L2GATE 0
#define DRYPASS 0
#define HINT 0
#define ABL 0
#define PRIO_F 3
#define FGATE 6
#define DQ_ 4
#define DG_ 4
#define NOPK 1
#define W0_ 4
#define SL_L 1
#define SL_F 1
#define TURNQ 0
#define HOLD 1
#define HGATE 8
#define TKMAP 3
#define S2GATE (TKMAP ? 2 : 4)
#define F2GATE 3u
#define ANN 0
#define TURN0 0
constexpr int NFILL = 37, NSTAGE = 38;
constexpr unsigned SPIN_CAP = 1u << 18;

struct TabQ { static constexpr int u(int i) { return i; } };
struct TabO { static constexpr int u(int i) { return i + 1; } };
struct TabG { static constexpr int u(int i) { constexpr int t[12] = {13, 14, 0, 6, 7, 8, 9, 10, 11, 12, 1, 2}; return t[i]; } };
struct TabD { static constexpr int u(int i) { constexpr int t[12] = {3, 4, 5, 13, 14, 6, 7, 8, 9, 10, 11, 12}; return t[i]; } };

template <int V> struct IC { static constexpr int v = V; };
template <int I, int N, class F> DI void sfor(F&& f) { if constexpr (I < N) { f(IC<I>{}); sfor<I + 1, N>(f); } }
DI int fresh_lane() { int l; asm volatile("v_mbcnt_lo_u32_b32 %0, -1, 0\n\tv_mbcnt_hi_u32_b32 %0, -1, %0" : "=v"(l)); return l; }
DI int opq(int v) { asm volatile("" : "+s"(v)); return v; }
DI unsigned long long w_ballot(bool p) { return __builtin_amdgcn_ballot_w64(p); }
DI bool w_any(bool p) { return w_ballot(p) != 0ull; }
DI bool w_all(bool p) { return w_ballot(!p) == 0ull; }
DI float asf(unsigned u) { return __uint_as_float(u); }
DI unsigned asu(float f) { return __float_as_uint(f); }
DI unsigned pk(float a, float b) { f32x2 t = {a, b}; bf2 p = __builtin_convertvector(t, bf2); return __builtin_bit_cast(unsigned, p); }
DI float rbf(float a) { return asf(pk(a, a) << 16); }
DI float ex2(float a) { return __builtin_amdgcn_exp2f(a); }
DI float dot2(unsigned a, unsigned b, float c) { return __builtin_amdgcn_fdot2_f32_bf16(__builtin_bit_cast(bf2, a), __builtin_bit_cast(bf2, b), c, false); }
DI f32x16 mfma32(v4u a, v4u b, f32x16 c) { return __builtin_amdgcn_mfma_f32_32x32x16_bf16(__builtin_bit_cast(s16x8, a), __builtin_bit_cast(s16x8, b), c, 0, 0, 0); }
DI f32x16 mfma32k8(v2u a, v2u b, f32x16 c) { return __builtin_amdgcn_mfma_f32_32x32x8bf16_1k(__builtin_bit_cast(s16x4, a), __builtin_bit_cast(s16x4, b), c, 0, 0, 0); }
DI f32x4 mfma16(v4u a, v4u b, f32x4 c) { return __builtin_amdgcn_mfma_f32_16x16x32_bf16(__builtin_bit_cast(s16x8, a), __builtin_bit_cast(s16x8, b), c, 0, 0, 0); }
DI void mfma16i(f32x4& c, v4u a, v4u b) { asm volatile("v_mfma_f32_16x16x32_bf16 %0, %1, %2, %0" : "+v"(c) : "v"(a), "v"(b)); }
DI void mfma_fence(f32x4& a, f32x4& b, f32x4& c) { asm volatile("s_nop 7" : "+v"(a), "+v"(b), "+v"(c)); }
DI unsigned ufl(unsigned v) { return __builtin_amdgcn_readfirstlane(v); }
DI unsigned lds_ld(unsigned a) { return __hip_atomic_load((LAS unsigned*)a, RLX_WG); }
DI void lds_st(unsigned a, unsigned v) { __hip_atomic_store((LAS unsigned*)a, v, RLX_WG); }
DI void lds_or1(unsigned a, unsigned v) { asm volatile("s_mov_b64 exec, 1\n\tds_or_b32 %0, %1\n\ts_mov_b64 exec, -1" :: "v"(a), "v"(v) : "memory"); }
DI v4u lds128(unsigned a) { return *(LAS v4u*)a; }
DI v2u lds64(unsigned a) { return *(LAS v2u*)a; }
DI v2u ldstr(unsigned a) { return __builtin_bit_cast(v2u, __builtin_amdgcn_ds_read_tr16_b64_v4i16((LAS s16x4*)a)); }
DI float sum_halves(float x) { auto r = __builtin_amdgcn_permlane32_swap(asu(x), asu(x), false, false); unsigned a = r[0], b = r[1]; return asf(a) + asf(b); }
DI float max_halves(float x) { auto r = __builtin_amdgcn_permlane32_swap(asu(x), asu(x), false, false); unsigned a = r[0], b = r[1]; return fmaxf(asf(a), asf(b)); }
DI float sum_rows16(float x) { auto r = __builtin_amdgcn_permlane16_swap(asu(x), asu(x), false, false); unsigned a = r[0], b = r[1]; return asf(a) + asf(b); }
DI float ror8(float x) { return asf(__builtin_amdgcn_update_dpp(0u, asu(x), 0x128, 0xf, 0xf, false)); }
DI unsigned st_line(int c, int hf) { return ((c >> 2) * 8 + ((c >> 1) & 1) * 4 + hf * 2 + (c & 1)) * 128; }
DI float max3(float a, float b, float c) { return __builtin_elementwise_maximum(__builtin_elementwise_maximum(a, b), c); }

typedef __amdgpu_buffer_rsrc_t rsrc_t;
constexpr int SC1 = 16;
DI v4u gld128(rsrc_t r, unsigned v, unsigned s) { return __builtin_amdgcn_raw_buffer_load_b128(r, v, s, SC1); }
DI v2u gld64(rsrc_t r, unsigned v, unsigned s) { return __builtin_amdgcn_raw_buffer_load_b64(r, v, s, SC1); }
DI void gst128(v4u x, rsrc_t r, unsigned v, unsigned s) { __builtin_amdgcn_raw_buffer_store_b128(x, r, v + s, 0, SC1); }
DI void gst64(v2u x, rsrc_t r, unsigned v, unsigned s) { __builtin_amdgcn_raw_buffer_store_b64(x, r, v, s, SC1); }
template <int S> DI v4u lda(rsrc_t r, unsigned voff, unsigned base) { return gld128(r, voff + (S & 3) * 1024, base + (S >> 2) * 4096); }

#define STAMP_HI (SPIN_CAP - rg.bud)
#define STAMP(i) do {} while (0)
#define INNER 1
#define STAMP1(i) do { if (INNER == 1) STAMP(i); } while (0)
#define STAMP2(i) do { if (INNER == 2) STAMP(i); } while (0)
#define STAMP4(i) do { if (INNER == 4) STAMP(i); } while (0)
#define STAMP5(i) do { if (INNER == 5 && rg.ls == 0) STAMP(i); } while (0)
#define STAMP3(i) do { if (INNER == 3 && rg.ls == 0) STAMP(i); } while (0)

#define SPEND(bud) { if ((bud) >= SPIN_CAP) break; ++(bud); }
template <int SL = 1> DI unsigned lds_wait_gt(unsigned a, unsigned f, unsigned& bud) {
  unsigned v;
  for (;;) {
    v = lds_ld(a);
    if (ufl(v) > f) break;
    SPEND(bud);
    __builtin_amdgcn_s_sleep(SL);
  }
  CBAR();
  return v;
}
DI void lds_wait_bits(unsigned a, unsigned bits, unsigned& cache, unsigned& bud) {
  if (__builtin_expect((cache & bits) == bits, 1)) return;
  for (;;) {
    cache = ufl(lds_ld(a));
    if ((cache & bits) == bits) break;
    SPEND(bud);
    __builtin_amdgcn_s_sleep(1);
  }
  CBAR();
}
template <int SL = 1> DI void prog_wait(unsigned need, unsigned& bud) {
  const int lane = fresh_lane();
  for (;;) {
    unsigned v = lds_ld(L_PROG + (lane & 7) * 4);
    if (w_all(v >= need)) break;
    SPEND(bud);
    __builtin_amdgcn_s_sleep(SL);
  }
  CBAR();
}
DI unsigned rmw_poll(GAS unsigned* p) {
  unsigned f;
  asm volatile("s_mov_b64 exec, 1\n\ts_nop 3\n\tglobal_atomic_or %0, %1, %1, %2 sc0\n\ts_waitcnt vmcnt(0)\n\ts_mov_b64 exec, -1" : "=&v"(f) : "v"(0u), "s"(p) : "memory");
  return ufl(f);
}
DI unsigned rmw_poll_lanes(GAS unsigned* p, unsigned voff, unsigned mlo, unsigned mhi, unsigned init) {
  asm volatile("s_mov_b32 exec_lo, %3\n\ts_mov_b32 exec_hi, %4\n\ts_nop 3\n\tglobal_atomic_or %0, %1, %5, %2 sc0\n\ts_waitcnt vmcnt(0)\n\ts_mov_b64 exec, -1"
               : "+v"(init) : "v"(voff), "s"(p), "s"(mlo), "s"(mhi), "v"(0u) : "memory");
  return init;
}
DI bool rmw_poll_pairs(GAS unsigned* p, unsigned voff, unsigned mlo, unsigned tag) {
  v2u f = {0u, 0u};
  asm volatile("s_mov_b32 exec_lo, %3\n\ts_mov_b32 exec_hi, 0\n\ts_nop 3\n\tglobal_atomic_or_x2 %0, %1, %4, %2 sc0\n\ts_waitcnt vmcnt(0)\n\ts_mov_b64 exec, -1"
               : "+v"(f) : "v"(voff), "s"(p), "s"(mlo), "v"(v2u{0u, 0u}) : "memory");
  return f[0] == tag && f[1] == tag;
}
DI void gst32(unsigned x, rsrc_t r, unsigned v) { __builtin_amdgcn_raw_buffer_store_b32(x, r, v, 0, SC1); }
#define DRAIN() asm volatile("s_waitcnt vmcnt(0)" ::: "memory")
#define WG_BARRIER() do { asm volatile("s_waitcnt lgkmcnt(0)" ::: "memory"); __builtin_amdgcn_s_barrier(); CBAR(); } while (0)
DI void ctr_wait(unsigned char* ws, int kind, int t32, int rep, unsigned& bud) {
  GAS unsigned* p = (GAS unsigned*)(ws + OFF_CTR + (kind * 64 + t32) * 1024 + rep * 256);
  if (ABL & 8) return;
  for (;;) {
    if (rmw_poll(p) >= 32u) break;
    SPEND(bud);
  }
  __builtin_amdgcn_fence(__ATOMIC_ACQUIRE, "wavefront");
  CBAR();
}
DI void ctr_arrive(unsigned char* ws, int kind, int t32) {
  const int lane = fresh_lane();
  GAS unsigned* p = (GAS unsigned*)(ws + OFF_CTR + (kind * 64 + t32) * 1024);
  if (ABL & 8) return;
  if (!HINT) DRAIN();
  asm volatile("s_mov_b64 exec, 15\n\ts_nop 3\n\tglobal_atomic_add %0, %1, %2\n\ts_mov_b64 exec, -1" :: "v"(lane * 256), "v"(1u), "s"(p) : "memory");
}

DI void dma_fill(v4u srd, unsigned voff, unsigned soff, unsigned ldst) {
  unsigned keep, so;
  asm volatile(
      "s_mov_b32 %0, m0\n\ts_mov_b32 m0, %5\n\ts_add_u32 %1, %4, 0x1000\n\t"
      "buffer_load_dwordx4 %2, %3, %4 offen sc1 lds\n\tbuffer_load_dwordx4 %2, %3, %4 offen offset:1024 sc1 lds\n\t"
      "buffer_load_dwordx4 %2, %3, %4 offen offset:2048 sc1 lds\n\tbuffer_load_dwordx4 %2, %3, %4 offen offset:3072 sc1 lds\n\t"
      "s_add_u32 m0, m0, 0x1000\n\ts_nop 0\n\t"
      "buffer_load_dwordx4 %2, %3, %1 offen sc1 lds\n\tbuffer_load_dwordx4 %2, %3, %1 offen offset:1024 sc1 lds\n\t"
      "buffer_load_dwordx4 %2, %3, %1 offen offset:2048 sc1 lds\n\tbuffer_load_dwordx4 %2, %3, %1 offen offset:3072 sc1 lds\n\t"
      "s_add_u32 m0, m0, 0x1000\n\ts_add_u32 %1, %1, 0x1000\n\t"
      "buffer_load_dwordx4 %2, %3, %1 offen sc1 lds\n\tbuffer_load_dwordx4 %2, %3, %1 offen offset:1024 sc1 lds\n\t"
      "s_mov_b32 m0, %0"
      : "=&s"(keep), "=&s"(so) : "v"(voff), "s"(srd), "s"(soff), "s"(ldst) : "memory", "scc");
}

DI void dma_raw(const unsigned char* base, unsigned bytes, unsigned voff, unsigned soff, unsigned sstep, unsigned ldst, int n) {
  const v4u srd = {(unsigned)(uintptr_t)base, (unsigned)((uintptr_t)base >> 32) & 0xffffu, bytes, 0x00020000u};
#pragma unroll 1
  for (int i = 0; i < n; ++i) {
    unsigned keep;
    asm volatile("s_mov_b32 %0, m0\n\ts_mov_b32 m0, %4\n\ts_nop 0\n\tbuffer_load_dwordx4 %1, %2, %3 offen lds\n\ts_mov_b32 m0, %0"
                 : "=&s"(keep) : "v"(voff), "s"(srd), "s"(soff + i * sstep), "s"(ldst + i * 1024) : "memory");
  }
}
DI unsigned lds_inc1(unsigned a) {
  unsigned r;
  asm volatile("s_mov_b64 exec, 1\n\tds_add_rtn_u32 %0, %1, %2\n\ts_waitcnt lgkmcnt(0)\n\ts_mov_b64 exec, -1" : "=&v"(r) : "v"(a), "v"(1u) : "memory");
  return ufl(r);
}
DI void lds_add1(unsigned a) { asm volatile("s_mov_b64 exec, 1\n\tds_add_u32 %0, %1\n\ts_mov_b64 exec, -1" :: "v"(a), "v"(1u) : "memory"); }

constexpr unsigned SENT = 0xffffffffu;
typedef unsigned long long u64;
DI u64 stale2(v4u x, v4u y) {
  const unsigned a = x[0], b = x[2], c = y[0], d = y[2];
  u64 m = w_ballot(max(max(max(a, b), c), d) == SENT);
  asm volatile("" : "+s"(m));
  return m;
}
template <int F> DI void fresh2(u64 bad, v4u& x, v4u& y, __amdgpu_buffer_rsrc_t ws, unsigned voff, unsigned base, unsigned& bud) {
  if (__builtin_expect(bad != 0, 0)) {
    for (;;) {
      CBAR();
      x = lda<F>(ws, voff, base); y = lda<F + 1>(ws, voff, base);
      if (!stale2(x, y)) break;
      SPEND(bud);
    }
  }
}

struct Ring {
  unsigned lv, lvs;
  unsigned lf, ls;
  unsigned prog;
  unsigned bud;
};
DI void ring_check(Ring& r, int f) {
  if (__builtin_expect(r.lvs <= r.lf + f, 0)) lds_wait_gt(L_LANDED, r.lf + f, r.bud);
  CBAR();
}
DI void ring_latch(Ring& r) { r.lvs = opq(ufl(r.lv)); }
DI void ring_peek(Ring& r) { r.lv = lds_ld(L_LANDED); }
constexpr int BSTEP = 61440;

template <int NB, int W, int DIST, int F0, int S0, class Tab, int CS, class Wait, class Hook>
DI void gemm32(f32x16 (&acc)[NB], float& rs, Ring& rg, rsrc_t ws, unsigned abase, unsigned stbase, Wait&& wait, Hook&& hook) {
  const int lane = fresh_lane();
  const unsigned voff = lane * 16;
  v4u a[W], b[DIST + 1];
  unsigned lb[3];
#pragma unroll
  for (int i = 0; i < 3; ++i) { lb[i] = voff + i * BSTEP; asm volatile("" : "+v"(lb[i])); }
  unsigned pa = rg.prog, pv = rg.ls + S0;
  asm volatile("" : "+v"(pa), "+v"(pv));
  v2u cf[NB], xm;
  v4u st[4];
  float s_ = 0.f, q_ = 0.f, s1_ = 0.f, q1_ = 0.f;
  unsigned csv = 0;
  u64 bs0 = 0, bs1 = 0;
  auto readB = [&](auto P) INL {
    constexpr int ph = P.v;
    if constexpr (ph < 40 * NB) {
      constexpr int ad = ARENA + Tab::u(ph / 10) * UNIT + (ph % 10) * 1024;
      b[ph % (DIST + 1)] = lds128(lb[ad / BSTEP] + ad % BSTEP);
    }
    if constexpr (ph % 10 == 3 && ph + 7 < 40 * NB) ring_peek(rg);
    if constexpr (ph % 10 == 7 && ph + 3 < 40 * NB) ring_latch(rg);
    if constexpr (ph % 10 == 9 && ph + 1 < 40 * NB) ring_check(rg, F0 + ph / 10 + 1);
    if constexpr (ph % 10 == 0 && ph > 0 && ph <= 40 * NB) { CBAR(); ++pv; lds_st(pa, pv); CBAR(); }
  };
  wait();
  constexpr int W0 = W0_ < W ? W0_ : W;
  sfor<0, W0>([&](auto S) INL { a[S.v] = lda<S.v>(ws, voff, abase); });
  SCHED();
  ring_peek(rg);
  ring_latch(rg);
  ring_check(rg, F0);
  sfor<0, DIST>(readB);
  u64 bad = 0;
  if (HINT == 1) fresh2<0>(stale2(a[0], a[1]), a[0], a[1], ws, voff, abase, rg.bud);
  SCHED();
  constexpr int X1 = 38 * NB - 12;
  constexpr int X0 = X1 - 28;
  sfor<0, 40 * NB>([&](auto SL) INL {
    constexpr int sl = SL.v, s = sl / NB, t = sl % NB;
    if constexpr (NB == 3 && t == 0 && (s == 1 || s == 11 || s == 21 || s == 31)) STAMP5(24 + s / 10);
    if constexpr (s == 0) { f32x16 z; _Pragma("unroll") for (int r = 0; r < 16; ++r) z[r] = 0.f; acc[t] = mfma32(b[sl % (DIST + 1)], a[0], z); }
    else acc[t] = mfma32(b[sl % (DIST + 1)], a[s % W], acc[t]);
    SCHED();
    readB(IC<sl + DIST>{});
    if constexpr (s < W - W0) {
      if constexpr (t == 0) a[(W0 + 2 * s) % W] = lda<W0 + 2 * s>(ws, voff, abase);
      if constexpr (t == NB - 1) a[(W0 + 2 * s + 1) % W] = lda<W0 + 2 * s + 1>(ws, voff, abase);
    } else if constexpr (t == NB - 1 && s + W < 40) a[s % W] = lda<s + W>(ws, voff, abase);
    if constexpr (HINT == 1 && t == NB - 1 && (s & 1) && s + 2 < 40) fresh2<s + 1>(bad, a[(s + 1) % W], a[(s + 2) % W], ws, voff, abase, rg.bud);
    if constexpr (HINT == 1 && t == NB - 2 && (s & 1) && s + 2 < 40) bad = stale2(a[(s + 1) % W], a[(s + 2) % W]);
    if constexpr (HINT == 1 && sl == X1 - 5) bs0 = stale2(st[0], st[1]);
    if constexpr (HINT == 1 && sl == X1 - 4) bs1 = stale2(st[2], st[3]);
    if constexpr (HINT == 1 && sl == X1 - 2) fresh2<0>(bs0, st[0], st[1], ws, voff, stbase, rg.bud);
    if constexpr (HINT == 1 && sl == X1 - 1) fresh2<2>(bs1, st[2], st[3], ws, voff, stbase, rg.bud);
    if constexpr (sl >= X0 && sl < X0 + 8 && (sl - X0) % 2 == 0) st[(sl - X0) / 2] = (ABL & 2) ? v4u{0u, 0u, 0u, 0u} : lda<(sl - X0) / 2>(ws, voff, stbase);
    if constexpr (sl == X1 - 10) csv = lds_ld(L_CSUM);
    if constexpr (sl == X1 - 6) csv = opq(ufl(csv));
    if constexpr (sl == X1 - 3) { if (__builtin_expect(ufl(csv) < NB - 1, 0)) lds_wait_gt(L_CSUM, NB - 2, rg.bud); CBAR(); }
    if constexpr (sl >= X1 && sl < X1 + 3) {
      constexpr int i = sl - X1 + 1;
      const unsigned u0 = st[i][0], u1 = st[i][1], u2 = st[i][2], u3 = st[i][3];
      if constexpr (i == 1) { const unsigned w0 = st[0][0], w1 = st[0][1], w2 = st[0][2], w3 = st[0][3]; s_ = asf(w0); q_ = asf(w1); s1_ = asf(w2); q1_ = asf(w3); }
      s_ += asf(u0); q_ += asf(u1); s1_ += asf(u2); q1_ += asf(u3);
    }
    if constexpr (sl == X1 + 3) { const bool up = lane & 8; s_ = (up ? s1_ : s_) + ror8(up ? s_ : s1_); }
    if constexpr (sl == X1 + 4) { const bool up = lane & 8; q_ = (up ? q1_ : q_) + ror8(up ? q_ : q1_); }
    if constexpr (sl == X1 + 5) s_ = sum_halves(s_) * (1.f / 640.f);
    if constexpr (sl == X1 + 6) q_ = sum_halves(q_) * (1.f / 640.f);
    if constexpr (sl == X1 + 7) {
      q_ = fmaxf(q_ - s_ * s_, 0.f);
      const unsigned nm = pk(-s_, -s_);
      xm[0] = lane < 32 ? nm : 0u; xm[1] = lane < 32 ? (nm & 0xffffu) : 0u;
    }
    if constexpr (sl == X1 + 8) {
      q_ = rbf(rbf(q_) + 1e-5f);
      sfor<0, NB>([&](auto T) INL { cf[T.v] = lds64(CS + T.v * 256 + (lane & 31) * 8); });
    }
    if constexpr (sl == X1 + 9) rs = __builtin_amdgcn_rcpf(rbf(__builtin_amdgcn_sqrtf(q_)));
    if constexpr (sl == X1 + 10) hook();
    SCHED();
    if constexpr (s == 38) { acc[t] = mfma32k8(cf[t], xm, acc[t]); SCHED(); }
  });
}

template <int NS, int W, int DIST, int F0, int S0, class Tab, class Wait>
DI void gemm16(Ring& rg, rsrc_t ws, unsigned abase, unsigned (&res)[6], unsigned xbase, unsigned stbase, bool last, void* out, int t16i, int ct, Wait&& wait) {
  const int lane = fresh_lane();
  const unsigned voff = lane * 16;
  v4u a[W], b[DIST + 1][3];
  unsigned lb[3], lc[3];
  const int g4 = lane >> 4;
#pragma unroll
  for (int i = 0; i < 3; ++i) {
    lb[i] = voff + i * BSTEP; asm volatile("" : "+v"(lb[i]));
    lc[i] = g4 * 128 + (lane & 7) * 16 + i * BSTEP + ((lane & 8) ? 0x40000 : 0); asm volatile("" : "+v"(lc[i]));
  }
  unsigned pa = rg.prog, pv = rg.ls + S0;
  asm volatile("" : "+v"(pa), "+v"(pv));
  f32x4 acc[3];
#pragma unroll
  for (int t = 0; t < 3; ++t) acc[t] = f32x4{asf(res[2 * t] << 16), asf(res[2 * t] & 0xffff0000u), asf(res[2 * t + 1] << 16), asf(res[2 * t + 1] & 0xffff0000u)};
  auto readB = [&](auto S, auto T) INL {
    constexpr int s = S.v, t = T.v;
    if constexpr (s < NS) {
      constexpr int ad = ARENA + Tab::u(s / 4) * UNIT + (s % 4) * 2560;
      b[s % (DIST + 1)][t] = lds128((t < 2 ? lb : lc)[ad / BSTEP] + ad % BSTEP + t * 1024);
    }
    if constexpr (t == 0 && s % 4 == 1 && s + 3 < NS) ring_peek(rg);
    if constexpr (t == 0 && s % 4 == 3 && s + 1 < NS) ring_latch(rg);
    if constexpr (t == 2 && s % 4 == 3 && s + 1 < NS) ring_check(rg, F0 + s / 4 + 1);
    if constexpr (t == 0 && s % 4 == 0 && s > 0 && s <= NS) { CBAR(); ++pv; lds_st(pa, pv); CBAR(); }
  };
  const unsigned t32 = (t16i & 1) * 16 + (lane & 15);
  const unsigned pi = 2 * (g4 & 1) + (g4 >> 1);
  unsigned so1 = xbase + (5 * ct) * 512 + pi * 512 + t32 * 16, so2 = xbase + (5 * ct + 4) * 512 + t32 * 16 + (lane < 16 ? 0u : WS_USED);
  unsigned so3 = stbase + st_line(ct, t16i & 1) + (lane & 7) * 16 + (lane & 8) + (lane < 16 ? 0u : WS_USED);
  asm volatile("" : "+v"(so1), "+v"(so2), "+v"(so3), "+v"(acc[0]), "+v"(acc[1]), "+v"(acc[2]));
  if (HINT && HINT < 3 && !last) { const v4u sv = {SENT, SENT, SENT, SENT}; gst128(sv, ws, so1, 0); gst128(sv, ws, so2, 0); gst64(v2u{SENT, SENT}, ws, so3, 0); }
  wait();
  sfor<0, W>([&](auto S) INL { a[S.v] = lda<S.v>(ws, voff, abase); });
  SCHED();
  ring_peek(rg);
  ring_latch(rg);
  ring_check(rg, F0);
  sfor<0, DIST * 3>([&](auto I) INL { readB(IC<I.v / 3>{}, IC<I.v % 3>{}); });
  u64 bad = 0;
  if (HINT == 1) fresh2<0>(stale2(a[0], a[1]), a[0], a[1], ws, voff, abase, rg.bud);
  SCHED();
  if constexpr (NS == 48) { STAMP1(27); }
  sfor<0, NS>([&](auto S) INL {
    constexpr int s = S.v;
    if constexpr (NS == 48 && s % 12 == 1) STAMP3(24 + s / 12);
    sfor<0, 3>([&](auto T) INL {
      constexpr int t = 2 - T.v;
      mfma16i(acc[t], b[s % (DIST + 1)][t], a[s % W]);
      SCHED();
      readB(IC<s + DIST>{}, T);
      if constexpr (T.v == 2 && s + W < NS) a[s % W] = lda<s + W>(ws, voff, abase);
      if constexpr (HINT == 1 && T.v == 0 && (s & 1) && s + 2 < NS) bad = stale2(a[(s + 1) % W], a[(s + 2) % W]);
      if constexpr (HINT == 1 && T.v == 2 && (s & 1) && s + 2 < NS) fresh2<s + 1>(bad, a[(s + 1) % W], a[(s + 2) % W], ws, voff, abase, rg.bud);
      SCHED();
    });
  });
  mfma_fence(acc[0], acc[1], acc[2]);
  if constexpr (NS == 48) { STAMP1(28); STAMP3(28); }
#pragma unroll
  for (int t = 0; t < 3; ++t) { res[2 * t] = pk(acc[t][0], acc[t][1]); res[2 * t + 1] = pk(acc[t][2], acc[t][3]); }
  auto s0 = __builtin_amdgcn_permlane16_swap(res[0], res[2], false, false);
  auto s1 = __builtin_amdgcn_permlane16_swap(res[1], res[3], false, false);
  auto s2 = __builtin_amdgcn_permlane16_swap(res[4], res[4], false, false);
  auto s3 = __builtin_amdgcn_permlane16_swap(res[5], res[5], false, false);
  const v4u o01 = {s0[0], s1[0], s0[1], s1[1]};
  const v4u o2 = {s2[0], s3[0], s2[1], s3[1]};
  if (last) {
    GAS char* ob = (GAS char*)out + (size_t)(t16i * 16 + (lane & 15)) * 1280 + ct * 80;
    *(GAS v4u*)(ob + pi * 16) = o01;
    if (lane < 16) *(GAS v4u*)(ob + 64) = o2;
  } else {
    gst128(o01, ws, so1, 0);
    gst128(o2, ws, so2, 0);
    const f32x4 sv = acc[0] + acc[1] + acc[2], qv = acc[0] * acc[0] + acc[1] * acc[1] + acc[2] * acc[2];
    float s = (sv[0] + sv[1]) + (sv[2] + sv[3]), q = (qv[0] + qv[1]) + (qv[2] + qv[3]);
    s = sum_halves(sum_rows16(s));
    q = sum_halves(sum_rows16(q));
    if (!(ABL & 2)) gst64(v2u{asu(s), asu(q)}, ws, so3, 0);
  }
}

constexpr float QSCALE = 0.22360679774997896f * 1.4426950408889634f;
constexpr float NEG = -1e30f;

DI float max16(const f32x16& s) {
  const float a = max3(s[0], s[1], s[2]), b = max3(s[3], s[4], s[5]), c = max3(s[6], s[7], s[8]);
  const float d = max3(s[9], s[10], s[11]), e = max3(s[12], s[13], s[14]);
  return __builtin_elementwise_maximum(max3(a, b, c), max3(d, e, s[15]));
}
DI void kv_write(unsigned kb, int lane, v4u k1, v4u ta, v4u tb) {
  *(LAS v4u*)(kb + lane * 16) = k1;
  *(LAS v2u*)(kb + 1024 + (lane ^ 32) * 8) = v2u{ta[0], ta[1]};
  const unsigned vb = kb + 1536 + (lane & 31) * 48 + (lane >> 5) * 8;
  *(LAS v2u*)(vb) = v2u{ta[2], ta[3]};
  *(LAS v2u*)(vb + 16) = v2u{tb[0], tb[1]};
  *(LAS v2u*)(vb + 32) = v2u{tb[2], tb[3]};
}

DI int kmapO4(int P, int t) {
  const int p = P / 5, r = P - 5 * p;
  if (r == 2) return 20 * (2 * p + t) + 16;
  return 20 * (2 * p + (r > 2)) + 8 * t + 4 * (r > 2 ? r - 3 : r);
}
DI v4u tr_frag(unsigned a0, unsigned a1) { const v2u x = ldstr(a0), y = ldstr(a1); return v4u{x[0], x[1], y[0], y[1]}; }
DI float sum8(v4u f, float s) {
#pragma unroll
  for (int i = 0; i < 4; ++i) { const unsigned u = f[i]; s = dot2(u, 0x3f803f80u, s); }
  return s;
}

DI void csum_rows(f32x4 c, unsigned la) {
  unsigned o[8];
#pragma unroll
  for (int i = 0; i < 4; ++i) {
    const float v = c[i], hi = rbf(v), mid = rbf(v - hi), lo = rbf(v - hi - mid);
    o[2 * i] = pk(hi, mid); o[2 * i + 1] = pk(lo, 0.f);
  }
  *(LAS v4u*)la = v4u{o[0], o[1], o[2], o[3]};
  *(LAS v4u*)(la + 16) = v4u{o[4], o[5], o[6], o[7]};
}

#if defined(__HIP_DEVICE_COMPILE__) && NOPK
#define NO_PK __attribute__((target("no-packed-fp32-ops")))
#else
#define NO_PK
#endif
__global__ void NO_PK __launch_bounds__(768) mega_kernel(unsigned char* wsp, void* out, const unsigned char* xin, const unsigned char* wqkv, const unsigned char* wo,
                                                   const unsigned char* wg, const unsigned char* wu, const unsigned char* wd) {
  __shared__ __attribute__((aligned(16))) char smem[LDS_BYTES];
  const int wave = ufl(threadIdx.x >> 6);
  asm volatile("" :: "v"((LAS char*)smem) : "memory");
  const int bx = blockIdx.x & 7, br = blockIdx.x >> 3;
#define GMAP 5
  const int band = (bx & 4) + (br >> 3), j = (bx & 3) * 8 + (br & 7);
  const int ct = (j >> 4) * 8 + (j & 7), rsel = (j >> 3) & 1;
  const rsrc_t ws = __builtin_amdgcn_make_buffer_rsrc(wsp, 0, WS_USED, 0x00020000);
  constexpr unsigned OOR = 0x40000000u;
  if (wave == 0) lds_st(MISC + (fresh_lane() & 31) * 4, 0u);
  WG_BARRIER();
  const int tk = ((7 - (bx ^ ((bx >> 1) & 2))) * 32 + br) * 8 + ((wave - 4) & 7);
  const unsigned sb = wave < 4 ? STG_X + (wave - 1) * 5120 : STG + (wave - 4) * 8192;
  {
    const int lane = fresh_lane();
    const bool isx = wave < 4;
    dma_raw(isx ? xin : wqkv, isx ? 2048 * 1280 : 640 * 3840, isx || (lane & 7) < 5 ? (lane >> 3) * 1280 + (lane & 7) * 16 : OOR,
            isx ? (band * 256 + j * 8) * 1280 + (wave - 1) * 640 : (tk >> 4) * (16 * 3840) + (tk & 15) * 80, isx ? 128 : 10240, sb,
            opq(isx ? (wave == 1 || wave == 2 ? 5 : 0) : tk < 640 ? 6 : 0));
  }

  if (wave == 0) {
    const int lane = fresh_lane();
    __builtin_amdgcn_s_setprio(3);
    const v4u wsd = {(unsigned)(uintptr_t)wsp, (unsigned)((uintptr_t)wsp >> 32) & 0xffffu, WS_USED, 0x00020000u};
    unsigned bud = 0;
    const unsigned tag = ufl(__builtin_amdgcn_raw_buffer_load_b32(ws, 0, OFF_EP, SC1)) + 1u;
    lds_st(L_TAG, tag);
    CBAR();
    lds_st(L_TAGV, 1u);
    for (;;) {
      if (((unsigned)w_ballot(rmw_poll_pairs((GAS unsigned*)(wsp + OFF_IN + (band * 32 + j) * 512), lane * 8, 0xfffffu, tag)) & 0xfffffu) == 0xfffffu) break;
      SPEND(bud);
    }
    lds_st(L_WQOK, 1u);
#pragma unroll 1
    for (int f = 0; f < 6; ++f) dma_fill(wsd, lane * 16, OFF_WQ + j * 81920 + f * UNIT, ARENA + f * UNIT);
    asm volatile("s_waitcnt vmcnt(50)" ::: "memory"); lds_st(L_LANDED, 1u);
    dma_fill(wsd, lane * 16, OFF_WQ + j * 81920 + 6 * UNIT, ARENA + 6 * UNIT);
    asm volatile("s_waitcnt vmcnt(50)" ::: "memory"); lds_st(L_LANDED, 2u);
    dma_fill(wsd, lane * 16, OFF_WQ + j * 81920 + 7 * UNIT, ARENA + 7 * UNIT);
    asm volatile("s_waitcnt vmcnt(50)" ::: "memory"); lds_st(L_LANDED, 3u);
    asm volatile("s_waitcnt vmcnt(40)" ::: "memory"); lds_st(L_LANDED, 4u);
    asm volatile("s_waitcnt vmcnt(30)" ::: "memory"); lds_st(L_LANDED, 5u);
    asm volatile("s_waitcnt vmcnt(20)" ::: "memory"); lds_st(L_LANDED, 6u);
    asm volatile("s_waitcnt vmcnt(10)" ::: "memory"); lds_st(L_LANDED, 7u);
    asm volatile("s_waitcnt vmcnt(0)" ::: "memory"); lds_st(L_LANDED, 8u);
    __builtin_amdgcn_s_setprio(PRIO_H);
    lds_wait_gt(L_W2OK, 0u, bud);
    unsigned issued = 8, published = 8;
#pragma unroll 1
    for (int lp = 1; lp < 8; ++lp) {
      const int L = lp >> 2, ph = lp & 3;
      unsigned long long units, needs_lo, needs_hi; int n; unsigned src;
      if (ph == 0) { units = 0x76543210ull; n = 8; src = OFF_WQ + j * 81920;
        needs_lo = 17ull | 25ull << 8 | 26ull << 16 | 27ull << 24 | 28ull << 32 | 29ull << 40 | 32ull << 48 | 33ull << 56; needs_hi = 0; }
      else if (ph == 1) { units = 0x54321ull; n = 5; src = OFF_WO + ct * 51200;
        needs_lo = 40ull | 41ull << 8 | 42ull << 16 | 43ull << 24 | 44ull << 32; needs_hi = 0; }
      else if (ph == 2) { units = 0x21CBA98760EDull; n = 12; src = OFF_WG + j * 122880;
        needs_lo = 30ull | 31ull << 8 | 39ull << 16 | 45ull << 24 | 46ull << 32 | 47ull << 40 | 47ull << 48 | 47ull << 56;
        needs_hi = 47ull | 47ull << 8 | 48ull << 16 | 49ull << 24; }
      else { units = 0xCBA9876ED543ull; n = HOLD ? 3 : 12; src = OFF_WD + ct * 122880;
        needs_lo = 50ull | 51ull << 8 | 52ull << 16 | 53ull << 24 | 54ull << 32 | 56ull << 40 | 57ull << 48 | 58ull << 56;
        needs_hi = 59ull | 60ull << 8 | 61ull << 16 | 62ull << 24; }
#pragma unroll 1
      for (int f = 0; f < n; ++f) {
        const int unit = (int)((units >> (4 * f)) & 15ull);
        const int nd = (int)(((f < 8 ? needs_lo >> (8 * f) : needs_hi >> (8 * (f - 8)))) & 255ull) + (L - 1) * NSTAGE;
        if (nd > 0) {
          unsigned v = lds_ld(L_PROG + (lane & 7) * 4);
          if (!w_all((int)v >= nd)) {
            if (published != issued) {
              if (issued - published >= 2) { asm volatile("s_waitcnt vmcnt(10)" ::: "memory"); lds_st(L_LANDED, issued - 1); }
              asm volatile("s_waitcnt vmcnt(0)" ::: "memory");
              published = issued; lds_st(L_LANDED, published);
            }
            prog_wait<SL_L>((unsigned)nd, bud);
          }
        }
        dma_fill(wsd, lane * 16, src + f * UNIT, ARENA + unit * UNIT);
        ++issued;
        if (issued - published > LDEPTH) {
          asm volatile("s_waitcnt vmcnt(%0)" :: "i"(10 * LDEPTH) : "memory");
          published = issued - LDEPTH; lds_st(L_LANDED, published);
        }
      }
      if (HOLD && ph == 3) {
        asm volatile("s_waitcnt vmcnt(10)" ::: "memory"); lds_st(L_LANDED, issued - 1);
        asm volatile("s_waitcnt vmcnt(0)" ::: "memory"); lds_st(L_LANDED, issued);
        if (L == 1) return;
        issued += 9; published = issued;
        lds_wait_gt<SL_L>(L_LANDED, issued - 1, bud);
      }
    }
    asm volatile("s_waitcnt vmcnt(10)" ::: "memory");
    lds_st(L_LANDED, issued - 1);
    asm volatile("s_waitcnt vmcnt(0)" ::: "memory");
    lds_st(L_LANDED, issued);
    return;
  }

  if (wave >= 4) {
    const int cw = wave - 4, grp = cw >> 2, wi = cw & 3;
    const int qt = (1 - grp) * 4 + wi;
    const int t32_ = band * 8 + qt;
    const int t16_ = band * 16 + (1 - grp) * 8 + rsel * 4 + wi;
    const int rep = j & 3;
    Ring rg;
    rg.prog = L_PROG + cw * 4;
    rg.bud = 0;
#define PRIO_E 2
#define PRIO_L 1
    if (grp == 0) __builtin_amdgcn_s_setprio(PRIO_E); else __builtin_amdgcn_s_setprio(PRIO_L);
    const int dslot = qt + (qt < 4 ? 4 : 8);
    STAMP(30);
    STAMP(0);
    unsigned res[6] = {0u, 0u, 0u, 0u, 0u, 0u};
    unsigned tag;
    {
      const int lane = fresh_lane();
      const int gam = lane >> 4, q = (lane & 15) >> 2, p = lane & 3, hh = gam >> 1, g = (gam & 1) * 4 + p;
      const v4u ones = {0x3f803f80u, 0x3f803f80u, 0x3f803f80u, 0x3f803f80u};
      f32x16 z;
#pragma unroll
      for (int i = 0; i < 16; ++i) z[i] = 0.f;
      if (cw == 7) {
        const int ci = j * 7 + lane;
        gst32(0u, ws, lane < 7 ? OFF_CTR + ((ci >> 5) * 64 + band * 8 + ((ci >> 2) & 7)) * 1024 + (ci & 3) * 256 : OOR);
      }
      if (tk < 640) {
        const int hp = tk & 15, ks = tk >> 4;
        const unsigned r = sb + (8 * hh + q) * 384;
        unsigned c0 = r + (g < 4 ? 0 : 128) + (g & 3) * 8, c1 = r + (g == 0 ? 32 : g == 1 ? 128 + 32 : 256 + ((g < 7 ? g : 6) - 2) * 8);
        unsigned dst = OFF_WQ + hp * 163840 + ks * 2048 + lane * 16, cs = OFF_CSQ + (hp * 80 + ks) * 256 + lane * 4;
        unsigned fa = lane < 16 ? OFF_IN + ((lane >> 1) * 32 + 2 * hp + (lane & 1)) * 512 + ks * 4 : OOR;
        STAMP4(24);
#pragma unroll 1
        for (int pass = DRYPASS ? 0 : 1; pass < 2; ++pass) {
          const unsigned off = pass ? 0u : OOR;
          if (pass) DRAIN();
          STAMP4(25);
          v4u f[2][2];
#pragma unroll
          for (int h2 = 0; h2 < 2; ++h2) {
            f[h2][0] = tr_frag(c0 + h2 * 40, c0 + h2 * 40 + 1536);
            f[h2][1] = tr_frag(c1 + h2 * 40, c1 + h2 * 40 + 1536);
          }
#pragma unroll
          for (int h2 = 0; h2 < 2; ++h2) {
            if ((lane & 31) >= 28) f[h2][1] = v4u{0u, 0u, 0u, 0u};
            gst128(f[h2][0], ws, dst + off + h2 * 81920, 0); gst128(f[h2][1], ws, dst + off + h2 * 81920 + 1024, 0);
          }
#pragma unroll
          for (int h2 = 0; h2 < 2; ++h2) {
            const float s0 = mfma32(ones, f[h2][0], z)[0], s1 = mfma32(ones, f[h2][1], z)[0];
            gst32(asu(lane < 32 ? s0 : s1), ws, cs + off + h2 * 10240);
          }
          if (pass) lds_wait_gt(L_TAGV, 0u, rg.bud);
          tag = ufl(lds_ld(L_TAG));
          STAMP4(26);
          if (pass) DRAIN();
          STAMP4(27);
          gst32(tag, ws, fa + off);
        }
        lds_add1(L_CRIT);
      } else {
        const bool gu = tk < 1280, dn = tk < 1664;
        const int i = gu ? tk - 640 : dn ? tk - 1280 : tk - 1664;
        unsigned a0, a1, a2, a3, dst, o2;
        if (L2GATE) lds_wait_gt(L_CRIT, L2GATE - 1, rg.bud);
        if (gu) {
          const unsigned vo = (lane & 15) < 12 ? gam * 3072 + (lane & 15) * 16 : OOR, so = (i >> 4) * (16 * 3072) + (i & 15) * 192;
          dma_raw(wg, 640 * 3072, vo, so, 4 * 3072, sb, 4);
          dma_raw(wu, 640 * 3072, vo, so, 4 * 3072, sb + 4096, 4);
          const unsigned r = sb + (8 * hh + q) * 256;
          a0 = r + g * 8; a1 = a0 + 4096; a2 = r + 64 + (g & 3) * 8 + (g < 4 ? 0 : 4096); a3 = 96;
          dst = OFF_WG + (i & 15) * 245760 + (i >> 4) * 3072; o2 = 2048 + lane * 16;
        } else {
          const unsigned vo = (lane & 15) < 10 ? gam * 1280 + (lane & 15) * 16 : OOR;
          if (tk < 1824) {
#pragma unroll 1
            for (int u = 0; u < 8; ++u) {
              const int P = 4 * (i >> 3) + (u >> 1);
              const int row = dn ? 16 * (P >> 1) + 4 * (P & 1) + 8 * (u & 1) : kmapO4(P, u & 1);
              dma_raw(dn ? wd : wo, dn ? 1536 * 1280 : 640 * 1280, vo, row * 1280 + (i & 7) * 160, 0, sb + u * 1024, 1);
            }
          }
          const unsigned r = sb + gam * 2048 + q * 256;
          a0 = r + p * 8; a1 = a0 + 32; a2 = r + 64 + (p & 1) * 8; a3 = 80;
          dst = tk >= 1824 ? OOR : dn ? OFF_WD + (i & 7) * 245760 + (i >> 3) * 2560 : OFF_WO + (i & 7) * 102400 + (i >> 3) * 2560;
          o2 = (lane & 15) < 8 ? 2048 + gam * 128 + (lane & 7) * 16 : OOR;
        }
        const unsigned dstep = gu || dn ? 122880 : 51200;
        STAMP4(24);
        asm volatile("s_waitcnt vmcnt(0)" : "+v"(a0), "+v"(a1), "+v"(a2), "+v"(o2) :: "memory");
        STAMP4(25);
        v4u f[2][3];
#pragma unroll
        for (int h2 = 0; h2 < 2; ++h2) {
          f[h2][0] = tr_frag(a0 + h2 * a3, a0 + h2 * a3 + 1024);
          f[h2][1] = tr_frag(a1 + h2 * a3, a1 + h2 * a3 + 1024);
          f[h2][2] = tr_frag(a2 + h2 * a3, a2 + h2 * a3 + 1024);
        }
        if (S2GATE) lds_wait_gt(L_CRIT, S2GATE - 1, rg.bud);
#pragma unroll
        for (int h2 = 0; h2 < 2; ++h2) {
          gst128(f[h2][0], ws, dst + h2 * dstep + lane * 16, 0); gst128(f[h2][1], ws, dst + h2 * dstep + lane * 16 + 1024, 0);
          gst128(f[h2][2], ws, dst + h2 * dstep + o2, 0);
        }
        if (gu) {
#pragma unroll
          for (int h2 = 0; h2 < 2; ++h2) {
            const float s0 = mfma32(ones, f[h2][0], z)[0], s1 = mfma32(ones, f[h2][1], z)[0], s2 = mfma32(ones, f[h2][2], z)[0];
            const unsigned cs = OFF_CSG + ((2 * (i & 15) + h2) * 40 + (i >> 4)) * 512 + lane * 4;
            gst32(asu(lane < 32 ? s0 : s1), ws, cs);
            gst32(lane < 32 ? asu(s2) : 0u, ws, cs + 256);
          }
        }
        lds_wait_gt(L_TAGV, 0u, rg.bud);
        tag = ufl(lds_ld(L_TAG));
        STAMP4(26);
        DRAIN();
        STAMP4(27);
      }
      if (lds_inc1(L_STG) == 7u) gst32(tag, ws, lane == 0 ? OFF_F2 + blockIdx.x * 4 : OOR);
    }
    STAMP4(28);
#pragma unroll 1
    for (int L = 0; L < 2; ++L) {
      rg.lf = L * NFILL; rg.ls = L * NSTAGE;
      const int t32i = opq(t32_), t16i = opq(t16_);
      PP(PHPRIO & 3);
      v4u q1; v2u q2;
      {
        f32x16 acc[2]; float rs;
        gemm32<2, WQ_, DQ_, 0, 0, TabQ, L_CQ>(acc, rs, rg, ws, OFF_XA + (2 * L) * XA_SZ + t32i * XA_T, OFF_ST + (2 * L) * ST_SZ + t32i * 4096,
                                            [&]() INL {
          if (HINT && HINT < 3) {
            const int lane = fresh_lane(), h = lane >> 5;
            const v4u sv = {SENT, SENT, SENT, SENT};
            if (band < 7) {
              const unsigned gb = OFF_KVH + L * KVH_SZ + ((band * 32 + j) * 8 + qt) * KVH_T;
              gst128(sv, ws, lane * 16, gb); gst128(sv, ws, lane * 16 + 1024, gb); gst128(sv, ws, lane * 16 + 2048, gb);
            }
            const unsigned ob = OFF_OA + L * OA_SZ + (2 * t32i) * OA_T + (5 * (j >> 1)) * 256;
            const unsigned tv = ((lane >> 4) & 1) * OA_T + (lane & 15) * 16;
            gst128(sv, ws, tv + h * 256, ob + (3 * (j & 1)) * 256);
            gst64(v2u{SENT, SENT}, ws, tv + (h ? WS_USED : 0u), ob + 2 * 256 + (j & 1) * 8);
          }
          if (L > 0) {
            ctr_wait(wsp, 3, t32i, rep, rg.bud);
            if (TURNQ && grp == 0) lds_wait_gt(rg.prog + 16, 25 + TURNQ, rg.bud);
          } else {
            DRAIN();
            unsigned xr = 0;
            lds_wait_bits(L_XRDY, 0xfu << (4 * qt), xr, rg.bud);
            if (TURN0 > 0 && grp == 1) lds_wait_gt(rg.prog - 16, TURN0 - 1, rg.bud);
          }
          STAMP(1 + L * 12); },
          [&]() INL { if (L > 0) prog_wait(L * NSTAGE, rg.bud); else lds_wait_gt(L_STG, 7u, rg.bud); });
        STAMP(2 + L * 12);
        const int lane = fresh_lane(), h = lane >> 5;
        v4u k1, ta, tb;
        const float rq = rs * QSCALE, r0 = h ? rs : rq;
#pragma unroll
        for (int i = 0; i < 4; ++i) k1[i] = pk(acc[0][8 + 2 * i] * rs, acc[0][9 + 2 * i] * rs);
        ta[0] = pk(acc[1][0] * r0, acc[1][1] * r0); ta[1] = pk(acc[1][2] * r0, acc[1][3] * r0);
        ta[2] = pk(acc[1][4] * rs, acc[1][5] * rs); ta[3] = pk(acc[1][6] * rs, acc[1][7] * rs);
#pragma unroll
        for (int i = 0; i < 4; ++i) tb[i] = pk(acc[1][8 + 2 * i] * rs, acc[1][9 + 2 * i] * rs);
        if (h) { tb[2] = 0x3f803f80u; tb[3] = 0x3f803f80u; }
        q2[0] = h ? 0x3f80u : ta[0]; q2[1] = h ? 0u : ta[1];
        if (!h) { ta[0] = 0u; ta[1] = 0u; }
        if (band < 7 && !(ABL & 1)) {
          const unsigned gb = OFF_KVH + L * KVH_SZ + ((band * 32 + j) * 8 + qt) * KVH_T;
          gst128(k1, ws, lane * 16, gb); gst128(ta, ws, lane * 16 + 1024, gb); gst128(tb, ws, lane * 16 + 2048, gb);
          if (HINT) gst32(tag, ws, lane == 0 ? OFF_HALOF + (((L * 8 + band) * 32 + j) * 2 + (qt >> 2)) * 256 + (qt & 3) * 4 : WS_USED);
        }
        kv_write(KV_OFF + dslot * KV_T, lane, k1, ta, tb);
        CBAR();
        lds_or1(L_RDY + L * 4, 1u << dslot);
#pragma unroll
        for (int i = 0; i < 4; ++i) q1[i] = pk(acc[0][2 * i] * rq, acc[0][2 * i + 1] * rq);
      }
      STAMP(3 + L * 12);
      PP((PHPRIO >> 2) & 3);
      {
        const int lane = fresh_lane(), h = lane >> 5;
        f32x16 o, negm, sc[2];
        const int topA = dslot, topB = grp == 0 ? 8 : 12;
        const int top0 = grp == 0 ? topA : topB, top1 = grp == 0 ? topB : topA;
        const unsigned vlo = 1536 + (4 * h + ((lane & 15) >> 2)) * 48 + (((lane >> 4) & 1) == 0 ? 8 * (lane & 3) : ((lane & 3) == 0 ? 32 : 40));
        unsigned ka[2], kb[2], va[2], vd;
        ka[0] = KV_OFF + (top0 - 4) * KV_T + lane * 16; ka[1] = KV_OFF + (top1 - 4) * KV_T + lane * 16;
        kb[0] = KV_OFF + (top0 - 4) * KV_T + 1024 + lane * 8; kb[1] = KV_OFF + (top1 - 4) * KV_T + 1024 + lane * 8;
        va[0] = KV_OFF + (top0 - 4) * KV_T + vlo; va[1] = KV_OFF + (top1 - 4) * KV_T + vlo;
        vd = KV_OFF + dslot * KV_T + vlo;
        asm volatile("" : "+v"(ka[0]), "+v"(ka[1]), "+v"(kb[0]), "+v"(kb[1]), "+v"(va[0]), "+v"(va[1]), "+v"(vd));
        const int ql = (lane & 31) - 4 * h;
        unsigned rdyv = lds_ld(L_RDY + L * 4);
        {
          f32x16 z;
#pragma unroll
          for (int r = 0; r < 16; ++r) { z[r] = 0.f; o[r] = 0.f; }
          sc[0] = mfma32(lds128(KV_OFF + dslot * KV_T + lane * 16), q1, z);
          sc[0] = mfma32k8(lds64(KV_OFF + dslot * KV_T + 1024 + lane * 8), q2, sc[0]);
#pragma unroll
          for (int r = 0; r < 16; ++r) sc[0][r] = ((r & 3) + 8 * (r >> 2)) <= ql ? sc[0][r] : NEG;
          const float m = max_halves(max16(sc[0]));
#pragma unroll
          for (int r = 0; r < 16; ++r) { sc[0][r] -= m; negm[r] = -m; }
        }
        STAMP2(24);
        unsigned rdy = ufl(rdyv);
        sfor<0, 9>([&](auto N) INL {
          if constexpr ((ABL & 4) && N.v > 0 && N.v < 8) return;
          constexpr int n = N.v;
          f32x16& s = sc[n & 1];
          f32x16& sn = sc[(n + 1) & 1];
          v4u k1; v2u k2, v0, v1, v2, v3;
          if constexpr (n == ANN) {
            if (!HINT && band < 7 && !(ABL & 1)) {
              DRAIN();
              gst32(tag, ws, lane == 0 ? OFF_HALOF + (((L * 8 + band) * 32 + j) * 2 + (qt >> 2)) * 256 + (qt & 3) * 4 : WS_USED);
            }
            STAMP2(25);
          }
          {
            const unsigned a = n == 0 ? vd : va[(n - 1) >> 2] + (3 - ((n - 1) & 3)) * KV_T;
            v0 = ldstr(a); v1 = ldstr(a + 384); v2 = ldstr(a + 768); v3 = ldstr(a + 1152);
          }
          if constexpr (n < 8) {
            lds_wait_bits(L_RDY + L * 4, 1u << ((n < 4 ? top0 : top1) - 1 - (n & 3)), rdy, rg.bud);
            k1 = lds128(ka[n >> 2] + (3 - (n & 3)) * KV_T); k2 = lds64(kb[n >> 2] + (3 - (n & 3)) * KV_T);
          }
          if constexpr (n == 4 || n == 8) {
            if (grp == (n == 4 ? 0 : 1)) {
#pragma unroll
              for (int r = 0; r < 16; ++r) s[r] = ((r & 3) + 8 * (r >> 2)) > ql ? s[r] : NEG;
            }
          }
          SCHED();
          v4u p, p2;
          float e[16];
          e[0] = ex2(s[0]); e[1] = ex2(s[1]); e[2] = ex2(s[2]); SCHED();
          p[0] = pk(e[0], e[1]); e[3] = ex2(s[3]); e[4] = ex2(s[4]); SCHED();
          p[1] = pk(e[2], e[3]); e[5] = ex2(s[5]); e[6] = ex2(s[6]); SCHED();
          if constexpr (n < 8) sn = mfma32(k1, q1, negm);
          SCHED();
          p[2] = pk(e[4], e[5]); e[7] = ex2(s[7]); e[8] = ex2(s[8]); SCHED();
          p[3] = pk(e[6], e[7]); SCHED();
          if constexpr (n < 8) sn = mfma32k8(k2, q2, sn);
          SCHED();
          e[9] = ex2(s[9]); e[10] = ex2(s[10]); SCHED();
          p2[0] = pk(e[8], e[9]); e[11] = ex2(s[11]); SCHED();
          o = mfma32(v4u{v0[0], v0[1], v1[0], v1[1]}, p, o);
          SCHED();
          e[12] = ex2(s[12]); SCHED();
          p2[1] = pk(e[10], e[11]); e[13] = ex2(s[13]); e[14] = ex2(s[14]); e[15] = ex2(s[15]); SCHED();
          p2[2] = pk(e[12], e[13]); SCHED();
          p2[3] = pk(e[14], e[15]); SCHED();
          o = mfma32(v4u{v2[0], v2[1], v3[0], v3[1]}, p2, o);
          SCHED();
          if constexpr (n == 3) STAMP2(26);
          if constexpr (n == 5) STAMP2(27);
          if constexpr (n == 7) STAMP2(28);
        });
        if (__builtin_expect(w_any(!(o[12] < 1e18f)), 0)) {
          float m = NEG;
#pragma unroll
          for (int r = 0; r < 16; ++r) { o[r] = 0.f; negm[r] = 0.f; }
#pragma unroll 1
          for (int n = 0; n < 9; ++n) {
            const int sl = n < 5 ? dslot - n : topB + 4 - n;
            const unsigned kt = KV_OFF + sl * KV_T;
            f32x16 s = mfma32(lds128(kt + lane * 16), q1, negm);
            s = mfma32k8(lds64(kt + 1024 + lane * 8), q2, s);
#pragma unroll
            for (int r = 0; r < 16; ++r) {
              const bool lo = ((r & 3) + 8 * (r >> 2)) <= ql;
              s[r] = (n == 0 && !lo) || (n == 4 && lo) ? NEG : s[r];
            }
            const float mn = fmaxf(m, max_halves(max16(s))), f = ex2(m - mn);
            m = mn;
            v4u p, p2;
#pragma unroll
            for (int i = 0; i < 4; ++i) { p[i] = pk(ex2(s[2 * i] - m), ex2(s[2 * i + 1] - m)); p2[i] = pk(ex2(s[8 + 2 * i] - m), ex2(s[9 + 2 * i] - m)); }
#pragma unroll
            for (int r = 0; r < 16; ++r) o[r] *= f;
            const v2u v0 = ldstr(kt + vlo), v1 = ldstr(kt + vlo + 384), v2 = ldstr(kt + vlo + 768), v3 = ldstr(kt + vlo + 1152);
            o = mfma32(v4u{v0[0], v0[1], v1[0], v1[1]}, p, o);
            o = mfma32(v4u{v2[0], v2[1], v3[0], v3[1]}, p2, o);
          }
        }
        CBAR();
        lds_st(rg.prog, rg.ls + 9);
        CBAR();
        const float inv = __builtin_amdgcn_rcpf(o[12]);
        v4u o1;
#pragma unroll
        for (int i = 0; i < 4; ++i) o1[i] = pk(o[2 * i] * inv, o[2 * i + 1] * inv);
        const v2u o2 = {pk(o[8] * inv, o[9] * inv), pk(o[10] * inv, o[11] * inv)};
        const unsigned ob = OFF_OA + L * OA_SZ + (2 * t32i) * OA_T + (5 * (j >> 1)) * 256;
        const unsigned tv = ((lane >> 4) & 1) * OA_T + (lane & 15) * 16;
        gst128(o1, ws, tv + h * 256, ob + (3 * (j & 1)) * 256);
        gst64(o2, ws, tv + (h ? WS_USED : 0u), ob + 2 * 256 + (j & 1) * 8);
        STAMP(4 + L * 12);
        if (L == 0) lds_wait_gt(L_W2OK, 0u, rg.bud);
        ctr_arrive(wsp, 4 * L, t32i);
        if (L == 0) {
          const int g4 = lane >> 4;
          const unsigned ro = ((t16i & 1) * 16 + (lane & 15)) * 16 + (g4 & 1) * 8, rb = OFF_XA + (t16i >> 1) * XA_T + (5 * ct) * 512;
          const v2u r0 = gld64(ws, ro + (g4 >> 1) * 512, rb), r1 = gld64(ws, ro + (g4 >> 1) * 512 + 1024, rb), r2 = gld64(ws, g4 < 2 ? ro + 2048 : WS_USED, rb);
          res[0] = r0[0]; res[1] = r0[1]; res[2] = r1[0]; res[3] = r1[1]; res[4] = r2[0]; res[5] = r2[1];
        }
      }
      STAMP(5 + L * 12);
      PP((PHPRIO >> 4) & 3);
      gemm16<20, WO_, 3, 8, 9, TabO>(rg, ws, OFF_OA + L * OA_SZ + t16i * OA_T, res, OFF_XA + (2 * L + 1) * XA_SZ + (t16i >> 1) * XA_T,
                                     OFF_ST + (2 * L + 1) * ST_SZ + (t16i >> 1) * 4096, false, out, t16i, ct,
                                     [&]() INL { ctr_wait(wsp, 4 * L, t16i >> 1, rep, rg.bud); STAMP(6 + L * 12); });
      STAMP(7 + L * 12);
      ctr_arrive(wsp, 4 * L + 1, t16i >> 1);
      PP((PHPRIO >> 6) & 3);
      {
        f32x16 acc[3]; float rs;
        gemm32<3, WG_, DG_, 13, 14, TabG, L_CG>(acc, rs, rg, ws, OFF_XA + (2 * L + 1) * XA_SZ + t32i * XA_T, OFF_ST + (2 * L + 1) * ST_SZ + t32i * 4096,
                                              [&]() INL {
          if (HINT && HINT < 3) {
            const int lane = fresh_lane();
            const v4u sv = {SENT, SENT, SENT, SENT};
            const unsigned hb = OFF_HA + L * HA_SZ + (2 * t32i) * HA_T + (6 * j) * 256;
            const unsigned tv = ((lane >> 4) & 1) * HA_T + (lane & 15) * 16 + (lane >> 5) * 256;
            gst128(sv, ws, tv, hb); gst128(sv, ws, tv + 512, hb); gst128(sv, ws, tv + 1024, hb);
          }
          ctr_wait(wsp, 4 * L + 1, t32i, rep, rg.bud); STAMP(8 + L * 12); STAMP1(24); }, []() INL {});
        STAMP1(25); STAMP1(26); STAMP5(28);
        const int lane = fresh_lane(), h = lane >> 5;
        const float r2 = rs * rs;
        const unsigned hb = OFF_HA + L * HA_SZ + (2 * t32i) * HA_T + (6 * j) * 256;
        const unsigned tv = ((lane >> 4) & 1) * HA_T + (lane & 15) * 16 + h * 256;
        v4u hv;
#pragma unroll
        for (int i = 0; i < 4; ++i) hv[i] = pk(acc[0][2 * i] * r2 * acc[1][2 * i], acc[0][2 * i + 1] * r2 * acc[1][2 * i + 1]);
        gst128(hv, ws, tv, hb);
#pragma unroll
        for (int i = 0; i < 4; ++i) hv[i] = pk(acc[0][8 + 2 * i] * r2 * acc[1][8 + 2 * i], acc[0][9 + 2 * i] * r2 * acc[1][9 + 2 * i]);
        gst128(hv, ws, tv + 512, hb);
#pragma unroll
        for (int i = 0; i < 4; ++i) hv[i] = pk(acc[2][2 * i] * r2 * acc[2][8 + 2 * i], acc[2][2 * i + 1] * r2 * acc[2][9 + 2 * i]);
        gst128(hv, ws, tv + 1024, hb);
      }
      STAMP(9 + L * 12);
      ctr_arrive(wsp, 4 * L + 2, t32i);
      PP((PHPRIO >> 8) & 3);
      gemm16<48, WD_, 3, 25, 26, TabD>(rg, ws, OFF_HA + L * HA_SZ + t16i * HA_T, res, OFF_XA + 2 * XA_SZ + (t16i >> 1) * XA_T,
                                       OFF_ST + 2 * ST_SZ + (t16i >> 1) * 4096, L == 1, out, t16i, ct,
                                       [&]() INL { ctr_wait(wsp, 4 * L + 2, t16i >> 1, rep, rg.bud); STAMP(10 + L * 12); });
      STAMP(11 + L * 12);
      if (L == 0) ctr_arrive(wsp, 3, t16i >> 1);
    }
  } else {
    const int lane = fresh_lane();
    const int half = 2 - wave;
    __builtin_amdgcn_s_setprio(3);
    unsigned bud = 0;
    unsigned tag = 0;
    if (wave == 3) { lds_wait_gt(L_TAGV, 0u, bud); tag = ufl(lds_ld(L_TAG)); }
    if (wave < 3) {
      const int hx = wave - 1;
      {
        const unsigned xa = OFF_XA + (band * 8 + (j >> 2)) * XA_T + ((j & 3) * 8 + (lane & 7)) * 16 + (hx * 40 + (lane >> 3)) * 512;
        unsigned la = STG_X + hx * 5120 + (lane & 7) * 128 + (lane >> 3) * 16;
        unsigned sa = OFF_ST + (band * 8 + (j >> 2)) * 4096 + st_line(hx * 8 + (lane >> 3), (j >> 1) & 1) + (lane & 7) * 16 + (j & 1) * 8;
        unsigned fa = lane < 32 ? OFF_IN + (band * 32 + lane) * 512 + 256 + (j * 2 + hx) * 4 : OOR;
#pragma unroll 1
        for (int pass = DRYPASS ? 0 : 1; pass < 2; ++pass) {
          const unsigned off = pass ? 0u : OOR;
          if (pass) DRAIN();
          v4u xv[5];
#pragma unroll
          for (int i = 0; i < 5; ++i) xv[i] = lds128(la + i * 1024);
#pragma unroll
          for (int i = 0; i < 5; ++i) gst128(xv[i], ws, xa + off + i * 4096, 0);
          float s = 0.f, qq = 0.f;
#pragma unroll
          for (int i = 0; i < 5; ++i) {
            s = sum8(xv[i], s);
#pragma unroll
            for (int k = 0; k < 4; ++k) { const unsigned u = xv[i][k]; qq = dot2(u, u, qq); }
          }
          gst64(v2u{asu(s), asu(qq)}, ws, sa + off, 0);
          if (pass) lds_wait_gt(L_TAGV, 0u, bud);
          tag = ufl(lds_ld(L_TAG));
          if (pass) DRAIN();
          gst32(tag, ws, fa + off);
        }
      }
      lds_add1(L_CRIT);
    } else {
      for (;;) {
        const unsigned m = (unsigned)w_ballot(rmw_poll_pairs((GAS unsigned*)(wsp + OFF_IN + (band * 32 + j) * 512 + 256), lane * 8, 0xffffffffu, tag));
        lds_st(L_XRDY, m);
        if (m == 0xffffffffu) break;
        SPEND(bud);
      }
      lds_wait_gt(L_WQOK, 0u, bud);
      v4u pc[10];
      sfor<0, 10>([&](auto I) INL { pc[I.v] = lda<I.v>(ws, lane * 16, OFF_CSQ + j * 10240); });
      f32x4 c = __builtin_bit_cast(f32x4, pc[0]);
#pragma unroll
      for (int i = 1; i < 10; ++i) c += __builtin_bit_cast(f32x4, pc[i]);
#pragma unroll
      for (int i = 0; i < 4; ++i) c[i] = sum_halves(sum_rows16(c[i]));
      if (lane < 16) csum_rows(c, L_CQ + lane * 32);
      CBAR();
      lds_st(L_CSUM, 1u);
    }
    __builtin_amdgcn_s_setprio(PRIO_H);
    if (wave == 3) {
      lds_wait_gt(L_PROG, F2GATE, bud);
      for (;;) {
        const v4u f0 = gld128(ws, lane * 16, OFF_F2);
        const bool ok = f0[0] == tag && f0[1] == tag && f0[2] == tag && f0[3] == tag;
        if (w_all(ok)) break;
        SPEND(bud);
        __builtin_amdgcn_s_sleep(16);
        CBAR();
      }
      CBAR();
      lds_st(L_W2OK, 1u);
      if (blockIdx.x == 0) gst32(tag, ws, lane == 0 ? OFF_EP : WS_USED);
      f32x4 c = {0.f, 0.f, 0.f, 0.f};
#pragma unroll 1
      for (int r = 0; r < 2; ++r) {
        v4u pc[10];
        sfor<0, 10>([&](auto I) INL { pc[I.v] = lda<I.v>(ws, lane * 16, OFF_CSG + j * 20480 + r * 10240); });
#pragma unroll
        for (int i = 0; i < 10; ++i) c += __builtin_bit_cast(f32x4, pc[i]);
      }
#pragma unroll
      for (int i = 0; i < 4; ++i) c[i] = sum_halves(c[i]);
      if (lane < 24) csum_rows(c, L_CG + lane * 32);
      CBAR();
      lds_st(L_CSUM, 2u);
      if (!HOLD) return;
    }
    auto halo = [&](const int L) INL {
      if (wave == 3) return;
      if (band == 0 || (ABL & 1)) {
        if (L > 0) prog_wait<SL_F>(L * NSTAGE, bud); else lds_wait_gt(L_STG, 7u, bud);
        const v4u z = {0u, 0u, 0u, 0u};
#pragma unroll 1
        for (int sl = 0; sl < 4; ++sl) kv_write(KV_OFF + (8 * half + sl) * KV_T, lane, z, v4u{lane < 32 ? 0xf149u : 0u, 0u, 0u, 0u}, z);
        CBAR();
        lds_or1(L_RDY + L * 4, 0xfu << (8 * half));
        return;
      }
#pragma unroll 1
      for (int hf = 1; hf >= 0; --hf) {
        __builtin_amdgcn_s_setprio(PRIO_H);
        lds_wait_gt<SL_F>(L_PROG + (1 - hf) * 16, L * NSTAGE + FGATE, bud);
        if (L > 0) prog_wait(L * NSTAGE, bud); else lds_wait_gt(L_STG, 7u, bud);
        __builtin_amdgcn_s_setprio(PRIO_F);
        unsigned pend = 3u;
        GAS unsigned* fl = (GAS unsigned*)(wsp + OFF_HALOF + (((L * 8 + band - 1) * 32 + j) * 2 + hf) * 256 + half * 8);
        const unsigned gb = OFF_KVH + L * KVH_SZ + (((band - 1) * 32 + j) * 8 + 4 * hf + 2 * half) * KVH_T;
        while (pend) {
          const unsigned nw = (unsigned)w_ballot(rmw_poll_lanes(fl, lane * 4, 3u, 0u, 0u) == tag) & pend;
          __builtin_amdgcn_fence(__ATOMIC_ACQUIRE, "wavefront");
          CBAR();
          if (!nw) {
            SPEND(bud);
            continue;
          }
          pend &= ~nw;
          v4u x[2][3];
#pragma unroll
          for (int sl = 1; sl >= 0; --sl) {
            const unsigned vo = lane * 16 + ((nw >> sl & 1) ? 0u : WS_USED);
#pragma unroll
            for (int i = 0; i < 3; ++i) x[sl][i] = gld128(ws, vo + i * 1024, gb + sl * KVH_T);
          }
#pragma unroll
          for (int sl = 1; sl >= 0; --sl)
            if (nw >> sl & 1) {
              kv_write(KV_OFF + (8 * hf + 2 * half + sl) * KV_T, lane, x[sl][0], x[sl][1], x[sl][2]);
              CBAR();
              lds_or1(L_RDY + L * 4, 1u << (8 * hf + 2 * half + sl));
            }
        }
      }
      __builtin_amdgcn_s_setprio(PRIO_H);
    };
    halo(0);
    if (!HOLD) { halo(1); return; }
    v4u hold[3][10];
    lds_wait_gt<SL_F>(L_W2OK, 0u, bud);
    prog_wait<SL_F>(HGATE, bud);
    sfor<0, 30>([&](auto I) INL { hold[I.v / 10][I.v % 10] = lda<I.v % 10>(ws, lane * 16, OFF_WD + ct * 122880 + (2 + 3 * (I.v / 10) + wave) * UNIT); });
    auto restore = [&](const int L) INL {
      sfor<0, 3>([&](auto K) INL {
        const int f = 2 + 3 * K.v + wave;
        const unsigned la = ARENA + (unsigned)((0xCBA9876ED543ull >> (4 * f)) & 15ull) * UNIT + lane * 16;
        prog_wait<SL_L>(L * NSTAGE + (f < 5 ? 12 : 13) + f, bud);
        sfor<0, 10>([&](auto I) INL { *(LAS v4u*)(la + I.v * 1024) = hold[K.v][I.v]; });
        CBAR();
        lds_wait_gt(L_LANDED, L * NFILL + 25 + f - 1, bud);
        lds_st(L_LANDED, L * NFILL + 25 + f + 1);
      });
    };
    restore(0);
    halo(1);
    restore(1);
  }
}

extern "C" void kernel_launch(void* const* d_in, const int* in_sizes, int n_in, void* d_out, int out_size, void* d_ws, size_t ws_size,
                              hipStream_t stream) {
  typedef const unsigned char* cp;
  mega_kernel<<<256, 768, 0, stream>>>((unsigned char*)d_ws, d_out, (cp)d_in[0], (cp)d_in[1], (cp)d_in[2], (cp)d_in[4], (cp)d_in[5], (cp)d_in[6]);
}
```

```cpp
#include <hip/hip_runtime.h>
#include <hip/hip_bf16.h>
#include <stdint.h>
#pragma clang diagnostic ignored "-Wint-to-pointer-cast"

typedef unsigned v4u __attribute__((ext_vector_type(4)));
typedef unsigned v2u __attribute__((ext_vector_type(2)));
typedef short s16x8 __attribute__((ext_vector_type(8)));
typedef short s16x4 __attribute__((ext_vector_type(4)));
typedef float f32x16 __attribute__((ext_vector_type(16)));
typedef float f32x4 __attribute__((ext_vector_type(4)));
typedef float f32x2 __attribute__((ext_vector_type(2)));
typedef __bf16 bf2 __attribute__((ext_vector_type(2)));
typedef unsigned short u16;
#define LAS __attribute__((address_space(3)))
#define GAS __attribute__((address_space(1)))
#define DI __device__ __forceinline__
#define INL __attribute__((always_inline))
#define RLX_AGENT __ATOMIC_RELAXED, __HIP_MEMORY_SCOPE_AGENT
#define RLX_WG __ATOMIC_RELAXED, __HIP_MEMORY_SCOPE_WORKGROUP
#define CBAR() asm volatile("" ::: "memory")
#define SCHED() __builtin_amdgcn_sched_barrier(0)

constexpr unsigned OFF_CTR = 0;
constexpr unsigned OFF_HALOF = 7 * 64 * 1024;
constexpr unsigned OFF_IN = OFF_HALOF + 2 * 8 * 32 * 512;
constexpr unsigned OFF_F2 = OFF_IN + 256 * 512;
constexpr unsigned OFF_EP = OFF_F2 + 1024;
constexpr unsigned OFF_STAMP = 64u << 20;
constexpr unsigned OFF_WQ = 1u << 20;
constexpr unsigned OFF_WO = OFF_WQ + 32 * 81920;
constexpr unsigned OFF_WG = OFF_WO + 16 * 51200;
constexpr unsigned OFF_WD = OFF_WG + 32 * 122880;
constexpr unsigned XA_T = 40960, XA_SZ = 64 * XA_T;
constexpr unsigned OFF_XA = OFF_WD + 16 * 122880;
constexpr unsigned ST_SZ = 64 * 4096;
constexpr unsigned OFF_ST = OFF_XA + 4 * XA_SZ;
constexpr unsigned OA_T = 20480, OA_SZ = 128 * OA_T;
constexpr unsigned OFF_OA = OFF_ST + 4 * ST_SZ;
constexpr unsigned HA_T = 49152, HA_SZ = 128 * HA_T;
constexpr unsigned OFF_HA = OFF_OA + 2 * OA_SZ;
constexpr unsigned KVH_T = 3072, KVH_SZ = 8 * 32 * 8 * KVH_T;
constexpr unsigned OFF_KVH = OFF_HA + 2 * HA_SZ;
constexpr unsigned OFF_CSQ = OFF_KVH + 2 * KVH_SZ;
constexpr unsigned OFF_CSG = OFF_CSQ + 32 * 40 * 256;
constexpr unsigned WS_USED = OFF_CSG + 32 * 40 * 512;
static_assert(OFF_EP + 256 <= OFF_WQ, "");

constexpr int UNIT = 10240;
constexpr int MISC = 0, ARENA = 2048;
constexpr int KV_OFF = ARENA + 8 * UNIT, KV_T = 3072;
constexpr int L_PROG = MISC, L_LANDED = MISC + 64, L_RDY = MISC + 68, L_CSUM = MISC + 76;
constexpr int L_TAG = MISC + 80, L_TAGV = MISC + 84, L_XRDY = MISC + 88, L_STG = MISC + 92, L_WQOK = MISC + 96, L_W2OK = MISC + 100, L_CRIT = MISC + 104;
constexpr int STG = ARENA + 8 * UNIT;
constexpr int STG_X = STG + 65536;
constexpr int L_CQ = MISC + 256, L_CG = MISC + 768, L_STAMP = ARENA + 15 * UNIT + 4096;
constexpr int LDS_BYTES = ARENA + 15 * UNIT + 8192;
#define LDEPTH 2
#define WQ_ 12
#define WG_ 12
#define WO_ 10
#define WD_ 10
#define PRIO_H 0
#define PHPRIO 438
#define PP(p) do { if (PHPRIO) __builtin_amdgcn_s_setprio(p); } while (0)
#define PRETOUCH 0
#define L2GATE 0
#define DRYPASS 0
#define HINT 0
#define ABL 0
#define PRIO_F 3
#define FGATE 6
#define DQ_ 4
#define DG_ 4
#define NOPK 1
#define W0_ 4
#define SL_L 1
#define SL_F 1
#define TURNQ 0
#define HOLD 1
#define HGATE 8
#define POLL2 0
#define PRESLEEP 0
#define PAIRS 0
#define QREV 0
#define W0Q_ 4
#define TKMAP 1
#define S2GATE (TKMAP ? 2 : 4)
#define F2GATE 3u
#define ANN 0
#define TURN0 0
constexpr int NFILL = 37, NSTAGE = 38;
constexpr unsigned SPIN_CAP = 1u << 18;

struct TabQ { static constexpr int u(int i) { return i; } };
struct TabO { static constexpr int u(int i) { return i + 1; } };
struct TabG { static constexpr int u(int i) { constexpr int t[12] = {13, 14, 0, 6, 7, 8, 9, 10, 11, 12, 1, 2}; return t[i]; } };
struct TabD { static constexpr int u(int i) { constexpr int t[12] = {3, 4, 5, 13, 14, 6, 7, 8, 9, 10, 11, 12}; return t[i]; } };

template <int V> struct IC { static constexpr int v = V; };
template <int I, int N, class F> DI void sfor(F&& f) { if constexpr (I < N) { f(IC<I>{}); sfor<I + 1, N>(f); } }
DI int fresh_lane() { int l; asm volatile("v_mbcnt_lo_u32_b32 %0, -1, 0\n\tv_mbcnt_hi_u32_b32 %0, -1, %0" : "=v"(l)); return l; }
DI int opq(int v) { asm volatile("" : "+s"(v)); return v; }
DI unsigned long long w_ballot(bool p) { return __builtin_amdgcn_ballot_w64(p); }
DI bool w_any(bool p) { return w_ballot(p) != 0ull; }
DI bool w_all(bool p) { return w_ballot(!p) == 0ull; }
DI float asf(unsigned u) { return __uint_as_float(u); }
DI unsigned asu(float f) { return __float_as_uint(f); }
DI unsigned pk(float a, float b) { f32x2 t = {a, b}; bf2 p = __builtin_convertvector(t, bf2); return __builtin_bit_cast(unsigned, p); }
DI float rbf(float a) { return asf(pk(a, a) << 16); }
DI float ex2(float a) { return __builtin_amdgcn_exp2f(a); }
DI float dot2(unsigned a, unsigned b, float c) { return __builtin_amdgcn_fdot2_f32_bf16(__builtin_bit_cast(bf2, a), __builtin_bit_cast(bf2, b), c, false); }
DI f32x16 mfma32(v4u a, v4u b, f32x16 c) { return __builtin_amdgcn_mfma_f32_32x32x16_bf16(__builtin_bit_cast(s16x8, a), __builtin_bit_cast(s16x8, b), c, 0, 0, 0); }
DI f32x16 mfma32k8(v2u a, v2u b, f32x16 c) { return __builtin_amdgcn_mfma_f32_32x32x8bf16_1k(__builtin_bit_cast(s16x4, a), __builtin_bit_cast(s16x4, b), c, 0, 0, 0); }
DI f32x4 mfma16(v4u a, v4u b, f32x4 c) { return __builtin_amdgcn_mfma_f32_16x16x32_bf16(__builtin_bit_cast(s16x8, a), __builtin_bit_cast(s16x8, b), c, 0, 0, 0); }
DI void mfma16i(f32x4& c, v4u a, v4u b) { asm volatile("v_mfma_f32_16x16x32_bf16 %0, %1, %2, %0" : "+v"(c) : "v"(a), "v"(b)); }
DI void mfma16z(f32x4& c, v4u a, v4u b) { asm volatile("v_mfma_f32_16x16x32_bf16 %0, %1, %2, 0" : "=&v"(c) : "v"(a), "v"(b)); }
DI void mfma16k16i(f32x4& c, v2u a, v2u b) { asm volatile("v_mfma_f32_16x16x16_bf16 %0, %1, %2, %0" : "+v"(c) : "v"(a), "v"(b)); }
DI void mfma_fence(f32x4& a, f32x4& b, f32x4& c) { asm volatile("s_nop 7" : "+v"(a), "+v"(b), "+v"(c)); }
DI unsigned ufl(unsigned v) { return __builtin_amdgcn_readfirstlane(v); }
DI unsigned lds_ld(unsigned a) { return __hip_atomic_load((LAS unsigned*)a, RLX_WG); }
DI void lds_st(unsigned a, unsigned v) { __hip_atomic_store((LAS unsigned*)a, v, RLX_WG); }
DI void lds_or1(unsigned a, unsigned v) { asm volatile("s_mov_b64 exec, 1\n\tds_or_b32 %0, %1\n\ts_mov_b64 exec, -1" :: "v"(a), "v"(v) : "memory"); }
DI v4u lds128(unsigned a) { return *(LAS v4u*)a; }
DI v2u lds64(unsigned a) { return *(LAS v2u*)a; }
DI v2u ldstr(unsigned a) { return __builtin_bit_cast(v2u, __builtin_amdgcn_ds_read_tr16_b64_v4i16((LAS s16x4*)a)); }
DI float sum_halves(float x) { auto r = __builtin_amdgcn_permlane32_swap(asu(x), asu(x), false, false); unsigned a = r[0], b = r[1]; return asf(a) + asf(b); }
DI float max_halves(float x) { auto r = __builtin_amdgcn_permlane32_swap(asu(x), asu(x), false, false); unsigned a = r[0], b = r[1]; return fmaxf(asf(a), asf(b)); }
DI float sum_rows16(float x) { auto r = __builtin_amdgcn_permlane16_swap(asu(x), asu(x), false, false); unsigned a = r[0], b = r[1]; return asf(a) + asf(b); }
DI float ror8(float x) { return asf(__builtin_amdgcn_update_dpp(0u, asu(x), 0x128, 0xf, 0xf, false)); }
DI unsigned st_line(int c, int hf) { return ((c >> 2) * 8 + ((c >> 1) & 1) * 4 + hf * 2 + (c & 1)) * 128; }
DI float max3(float a, float b, float c) { return __builtin_elementwise_maximum(__builtin_elementwise_maximum(a, b), c); }

typedef __amdgpu_buffer_rsrc_t rsrc_t;
constexpr int SC1 = 16;
DI v4u gld128(rsrc_t r, unsigned v, unsigned s) { return __builtin_amdgcn_raw_buffer_load_b128(r, v, s, SC1); }
DI v2u gld64(rsrc_t r, unsigned v, unsigned s) { return __builtin_amdgcn_raw_buffer_load_b64(r, v, s, SC1); }
DI void gst128(v4u x, rsrc_t r, unsigned v, unsigned s) { __builtin_amdgcn_raw_buffer_store_b128(x, r, v + s, 0, SC1); }
DI void gst64(v2u x, rsrc_t r, unsigned v, unsigned s) { __builtin_amdgcn_raw_buffer_store_b64(x, r, v, s, SC1); }
template <int S> DI v4u lda(rsrc_t r, unsigned voff, unsigned base) { return gld128(r, voff + (S & 3) * 1024, base + (S >> 2) * 4096); }

#define STAMP_HI (SPIN_CAP - rg.bud)
#define STAMP(i) do {} while (0)
#define INNER 1
#define STAMP1(i) do { if (INNER == 1) STAMP(i); } while (0)
#define STAMP2(i) do { if (INNER == 2) STAMP(i); } while (0)
#define STAMP4(i) do { if (INNER == 4) STAMP(i); } while (0)
#define STAMP5(i) do { if (INNER == 5 && rg.ls == 0) STAMP(i); } while (0)
#define HSTAMP(w, i) do {} while (0)
#define STAMP3(i) do { if (INNER == 3 && rg.ls == 0) STAMP(i); } while (0)

#define SPEND(bud) { if ((bud) >= SPIN_CAP) break; ++(bud); }
template <int SL = 1> DI unsigned lds_wait_gt(unsigned a, unsigned f, unsigned& bud) {
  unsigned v;
  for (;;) {
    v = lds_ld(a);
    if (ufl(v) > f) break;
    SPEND(bud);
    __builtin_amdgcn_s_sleep(SL);
  }
  CBAR();
  return v;
}
DI void lds_wait_bits(unsigned a, unsigned bits, unsigned& cache, unsigned& bud) {
  if (__builtin_expect((cache & bits) == bits, 1)) return;
  for (;;) {
    cache = ufl(lds_ld(a));
    if ((cache & bits) == bits) break;
    SPEND(bud);
    __builtin_amdgcn_s_sleep(1);
  }
  CBAR();
}
template <int SL = 1> DI void prog_wait(unsigned need, unsigned& bud) {
  const int lane = fresh_lane();
  for (;;) {
    unsigned v = lds_ld(L_PROG + (lane & 7) * 4);
    if (w_all(v >= need)) break;
    SPEND(bud);
    __builtin_amdgcn_s_sleep(SL);
  }
  CBAR();
}
DI unsigned rmw_poll(GAS unsigned* p) {
  unsigned f;
  asm volatile("s_mov_b64 exec, 1\n\ts_nop 3\n\tglobal_atomic_or %0, %1, %1, %2 sc0\n\ts_waitcnt vmcnt(0)\n\ts_mov_b64 exec, -1" : "=&v"(f) : "v"(0u), "s"(p) : "memory");
  return ufl(f);
}
DI unsigned rmw_poll_lanes(GAS unsigned* p, unsigned voff, unsigned mlo, unsigned mhi, unsigned init) {
  asm volatile("s_mov_b32 exec_lo, %3\n\ts_mov_b32 exec_hi, %4\n\ts_nop 3\n\tglobal_atomic_or %0, %1, %5, %2 sc0\n\ts_waitcnt vmcnt(0)\n\ts_mov_b64 exec, -1"
               : "+v"(init) : "v"(voff), "s"(p), "s"(mlo), "s"(mhi), "v"(0u) : "memory");
  return init;
}
DI bool rmw_poll_pairs(GAS unsigned* p, unsigned voff, unsigned mlo, unsigned tag) {
  v2u f = {0u, 0u};
  asm volatile("s_mov_b32 exec_lo, %3\n\ts_mov_b32 exec_hi, 0\n\ts_nop 3\n\tglobal_atomic_or_x2 %0, %1, %4, %2 sc0\n\ts_waitcnt vmcnt(0)\n\ts_mov_b64 exec, -1"
               : "+v"(f) : "v"(voff), "s"(p), "s"(mlo), "v"(v2u{0u, 0u}) : "memory");
  return f[0] == tag && f[1] == tag;
}
DI void gst32(unsigned x, rsrc_t r, unsigned v) { __builtin_amdgcn_raw_buffer_store_b32(x, r, v, 0, SC1); }
#define DRAIN() asm volatile("s_waitcnt vmcnt(0)" ::: "memory")
#define WG_BARRIER() do { asm volatile("s_waitcnt lgkmcnt(0)" ::: "memory"); __builtin_amdgcn_s_barrier(); CBAR(); } while (0)
DI void ctr_wait(unsigned char* ws, int kind, int t32, int rep, unsigned& bud, unsigned (&kp)[2]) {
  GAS unsigned* p = (GAS unsigned*)(ws + OFF_CTR + (kind * 64 + t32) * 1024 + rep * 256);
  if (ABL & 8) return;
  if (PRESLEEP) __builtin_amdgcn_s_sleep(PRESLEEP);
  for (;;) {
    if (rmw_poll(p) >= 32u) break;
    SPEND(bud);
  }
  __builtin_amdgcn_fence(__ATOMIC_ACQUIRE, "wavefront");
  CBAR();
}
DI void ctr_arrive(unsigned char* ws, int kind, int t32) {
  const int lane = fresh_lane();
  GAS unsigned* p = (GAS unsigned*)(ws + OFF_CTR + (kind * 64 + t32) * 1024);
  if (ABL & 8) return;
  if (!HINT) DRAIN();
  asm volatile("s_mov_b64 exec, 15\n\ts_nop 3\n\tglobal_atomic_add %0, %1, %2\n\ts_mov_b64 exec, -1" :: "v"(lane * 256), "v"(1u), "s"(p) : "memory");
}

DI void dma_fill(v4u srd, unsigned voff, unsigned soff, unsigned ldst) {
  unsigned keep, so;
  asm volatile(
      "s_mov_b32 %0, m0\n\ts_mov_b32 m0, %5\n\ts_add_u32 %1, %4, 0x1000\n\t"
      "buffer_load_dwordx4 %2, %3, %4 offen sc1 lds\n\tbuffer_load_dwordx4 %2, %3, %4 offen offset:1024 sc1 lds\n\t"
      "buffer_load_dwordx4 %2, %3, %4 offen offset:2048 sc1 lds\n\tbuffer_load_dwordx4 %2, %3, %4 offen offset:3072 sc1 lds\n\t"
      "s_add_u32 m0, m0, 0x1000\n\ts_nop 0\n\t"
      "buffer_load_dwordx4 %2, %3, %1 offen sc1 lds\n\tbuffer_load_dwordx4 %2, %3, %1 offen offset:1024 sc1 lds\n\t"
      "buffer_load_dwordx4 %2, %3, %1 offen offset:2048 sc1 lds\n\tbuffer_load_dwordx4 %2, %3, %1 offen offset:3072 sc1 lds\n\t"
      "s_add_u32 m0, m0, 0x1000\n\ts_add_u32 %1, %1, 0x1000\n\t"
      "buffer_load_dwordx4 %2, %3, %1 offen sc1 lds\n\tbuffer_load_dwordx4 %2, %3, %1 offen offset:1024 sc1 lds\n\t"
      "s_mov_b32 m0, %0"
      : "=&s"(keep), "=&s"(so) : "v"(voff), "s"(srd), "s"(soff), "s"(ldst) : "memory", "scc");
}

DI void dma_raw(const unsigned char* base, unsigned bytes, unsigned voff, unsigned soff, unsigned sstep, unsigned ldst, int n) {
  const v4u srd = {(unsigned)(uintptr_t)base, (unsigned)((uintptr_t)base >> 32) & 0xffffu, bytes, 0x00020000u};
#pragma unroll 1
  for (int i = 0; i < n; ++i) {
    unsigned keep;
    asm volatile("s_mov_b32 %0, m0\n\ts_mov_b32 m0, %4\n\ts_nop 0\n\tbuffer_load_dwordx4 %1, %2, %3 offen lds\n\ts_mov_b32 m0, %0"
                 : "=&s"(keep) : "v"(voff), "s"(srd), "s"(soff + i * sstep), "s"(ldst + i * 1024) : "memory");
  }
}
DI unsigned lds_inc1(unsigned a) {
  unsigned r;
  asm volatile("s_mov_b64 exec, 1\n\tds_add_rtn_u32 %0, %1, %2\n\ts_waitcnt lgkmcnt(0)\n\ts_mov_b64 exec, -1" : "=&v"(r) : "v"(a), "v"(1u) : "memory");
  return ufl(r);
}
DI void lds_add1(unsigned a) { asm volatile("s_mov_b64 exec, 1\n\tds_add_u32 %0, %1\n\ts_mov_b64 exec, -1" :: "v"(a), "v"(1u) : "memory"); }

constexpr unsigned SENT = 0xffffffffu;
typedef unsigned long long u64;
DI u64 stale2(v4u x, v4u y) {
  const unsigned a = x[0], b = x[2], c = y[0], d = y[2];
  u64 m = w_ballot(max(max(max(a, b), c), d) == SENT);
  asm volatile("" : "+s"(m));
  return m;
}
template <int F> DI void fresh2(u64 bad, v4u& x, v4u& y, __amdgpu_buffer_rsrc_t ws, unsigned voff, unsigned base, unsigned& bud) {
  if (__builtin_expect(bad != 0, 0)) {
    for (;;) {
      CBAR();
      x = lda<F>(ws, voff, base); y = lda<F + 1>(ws, voff, base);
      if (!stale2(x, y)) break;
      SPEND(bud);
    }
  }
}

struct Ring {
  unsigned lv, lvs;
  unsigned lf, ls;
  unsigned prog;
  unsigned bud;
  unsigned kp[2];
};
DI void ring_check(Ring& r, int f) {
  if (__builtin_expect(r.lvs <= r.lf + f, 0)) lds_wait_gt(L_LANDED, r.lf + f, r.bud);
  CBAR();
}
DI void ring_keep(Ring& r) { if (POLL2) asm volatile("" :: "v"(r.kp[0]), "v"(r.kp[1])); }
DI void ring_latch(Ring& r) { r.lvs = opq(ufl(r.lv)); }
DI void ring_peek(Ring& r) { r.lv = lds_ld(L_LANDED); }
constexpr int BSTEP = 61440;

template <int NB, int W, int DIST, int F0, int S0, class Tab, int CS, class Wait, class Hook>
DI void gemm32(f32x16 (&acc)[NB], float& rs, Ring& rg, rsrc_t ws, unsigned abase, unsigned stbase, Wait&& wait, Hook&& hook) {
  const int lane = fresh_lane();
  const unsigned voff = lane * 16;
  v4u a[W], b[DIST + 1];
  unsigned lb[3];
#pragma unroll
  for (int i = 0; i < 3; ++i) { lb[i] = voff + i * BSTEP; asm volatile("" : "+v"(lb[i])); }
  unsigned pa = rg.prog, pv = rg.ls + S0;
  asm volatile("" : "+v"(pa), "+v"(pv));
  v2u cf[NB], xm;
  v4u st[4];
  float s_ = 0.f, q_ = 0.f, s1_ = 0.f, q1_ = 0.f;
  unsigned csv = 0;
  u64 bs0 = 0, bs1 = 0;
  auto readB = [&](auto P) INL {
    constexpr int ph = P.v;
    if constexpr (ph < 40 * NB) {
      constexpr int ad = ARENA + Tab::u(ph / 10) * UNIT + (ph % 10) * 1024;
      b[ph % (DIST + 1)] = lds128(lb[ad / BSTEP] + ad % BSTEP);
    }
    if constexpr (ph % 10 == 3 && ph + 7 < 40 * NB) ring_peek(rg);
    if constexpr (ph % 10 == 7 && ph + 3 < 40 * NB) ring_latch(rg);
    if constexpr (ph % 10 == 9 && ph + 1 < 40 * NB) ring_check(rg, F0 + ph / 10 + 1);
    if constexpr (ph % 10 == 0 && ph > 0 && ph <= 40 * NB) { CBAR(); ++pv; lds_st(pa, pv); CBAR(); }
  };
  wait();
  constexpr int W0r = NB == 2 ? W0Q_ : W0_;
  constexpr int W0 = W0r < W ? W0r : W;
  sfor<0, W0>([&](auto S) INL { a[S.v] = lda<S.v>(ws, voff, abase); });
  SCHED();
  ring_peek(rg);
  ring_latch(rg);
  ring_check(rg, F0);
  sfor<0, DIST>(readB);
  u64 bad = 0;
  if (HINT == 1) fresh2<0>(stale2(a[0], a[1]), a[0], a[1], ws, voff, abase, rg.bud);
  SCHED();
  constexpr int X1 = 38 * NB - 12;
  constexpr int X0 = X1 - 28;
  sfor<0, 40 * NB>([&](auto SL) INL {
    constexpr int sl = SL.v, s = sl / NB, t = sl % NB;
    if constexpr (NB == 3 && t == 0 && (s == 1 || s == 11 || s == 21 || s == 31)) STAMP5(24 + s / 10);
    if constexpr (s == 0) { f32x16 z; _Pragma("unroll") for (int r = 0; r < 16; ++r) z[r] = 0.f; acc[t] = mfma32(b[sl % (DIST + 1)], a[0], z); }
    else acc[t] = mfma32(b[sl % (DIST + 1)], a[s % W], acc[t]);
    SCHED();
    if constexpr (sl == 1) ring_keep(rg);
    readB(IC<sl + DIST>{});
    if constexpr (s < W - W0) {
      if constexpr (t == 0) a[(W0 + 2 * s) % W] = lda<W0 + 2 * s>(ws, voff, abase);
      if constexpr (t == NB - 1) a[(W0 + 2 * s + 1) % W] = lda<W0 + 2 * s + 1>(ws, voff, abase);
    } else if constexpr (t == NB - 1 && s + W < 40) a[s % W] = lda<s + W>(ws, voff, abase);
    if constexpr (HINT == 1 && t == NB - 1 && (s & 1) && s + 2 < 40) fresh2<s + 1>(bad, a[(s + 1) % W], a[(s + 2) % W], ws, voff, abase, rg.bud);
    if constexpr (HINT == 1 && t == NB - 2 && (s & 1) && s + 2 < 40) bad = stale2(a[(s + 1) % W], a[(s + 2) % W]);
    if constexpr (HINT == 1 && sl == X1 - 5) bs0 = stale2(st[0], st[1]);
    if constexpr (HINT == 1 && sl == X1 - 4) bs1 = stale2(st[2], st[3]);
    if constexpr (HINT == 1 && sl == X1 - 2) fresh2<0>(bs0, st[0], st[1], ws, voff, stbase, rg.bud);
    if constexpr (HINT == 1 && sl == X1 - 1) fresh2<2>(bs1, st[2], st[3], ws, voff, stbase, rg.bud);
    if constexpr (sl >= X0 && sl < X0 + 8 && (sl - X0) % 2 == 0) st[(sl - X0) / 2] = (ABL & 2) ? v4u{0u, 0u, 0u, 0u} : lda<(sl - X0) / 2>(ws, voff, stbase);
    if constexpr (sl == X1 - 10) csv = lds_ld(L_CSUM);
    if constexpr (sl == X1 - 6) csv = opq(ufl(csv));
    if constexpr (sl == X1 - 3) { if (__builtin_expect(ufl(csv) < NB - 1, 0)) lds_wait_gt(L_CSUM, NB - 2, rg.bud); CBAR(); }
    if constexpr (sl >= X1 && sl < X1 + 3) {
      constexpr int i = sl - X1 + 1;
      const unsigned u0 = st[i][0], u1 = st[i][1], u2 = st[i][2], u3 = st[i][3];
      if constexpr (i == 1) { const unsigned w0 = st[0][0], w1 = st[0][1], w2 = st[0][2], w3 = st[0][3]; s_ = asf(w0); q_ = asf(w1); s1_ = asf(w2); q1_ = asf(w3); }
      s_ += asf(u0); q_ += asf(u1); s1_ += asf(u2); q1_ += asf(u3);
    }
    if constexpr (sl == X1 + 3) { const bool up = lane & 8; s_ = (up ? s1_ : s_) + ror8(up ? s_ : s1_); }
    if constexpr (sl == X1 + 4) { const bool up = lane & 8; q_ = (up ? q1_ : q_) + ror8(up ? q_ : q1_); }
    if constexpr (sl == X1 + 5) s_ = sum_halves(s_) * (1.f / 640.f);
    if constexpr (sl == X1 + 6) q_ = sum_halves(q_) * (1.f / 640.f);
    if constexpr (sl == X1 + 7) {
      q_ = fmaxf(q_ - s_ * s_, 0.f);
      const unsigned nm = pk(-s_, -s_);
      xm[0] = lane < 32 ? nm : 0u; xm[1] = lane < 32 ? (nm & 0xffffu) : 0u;
    }
    if constexpr (sl == X1 + 8) {
      q_ = rbf(rbf(q_) + 1e-5f);
      sfor<0, NB>([&](auto T) INL { cf[T.v] = lds64(CS + T.v * 256 + (lane & 31) * 8); });
    }
    if constexpr (sl == X1 + 9) rs = __builtin_amdgcn_rcpf(rbf(__builtin_amdgcn_sqrtf(q_)));
    if constexpr (sl == X1 + 10) hook();
    SCHED();
    if constexpr (s == 38) { acc[t] = mfma32k8(cf[t], xm, acc[t]); SCHED(); }
  });
}

template <int W, int DIST, int F0, int S0, class Tab, int CS, class Wait>
DI void gemmG(f32x4 (&acc)[6][2], float (&rs)[2], Ring& rg, rsrc_t ws, unsigned abase, unsigned stbase, Wait&& wait) {
  constexpr int NSL = 120;
  const int lane = fresh_lane();
  const unsigned voff = lane * 16;
  v4u a[W], b[DIST + 1];
  unsigned lb[3];
#pragma unroll
  for (int i = 0; i < 3; ++i) { lb[i] = voff + i * BSTEP; asm volatile("" : "+v"(lb[i])); }
  unsigned pa = rg.prog, pv = rg.ls + S0;
  asm volatile("" : "+v"(pa), "+v"(pv));
  v2u cf[6], xm[2];
  v4u st[4];
  float s_ = 0.f, q_ = 0.f, s1_ = 0.f, q1_ = 0.f;
  unsigned csv = 0;
  auto readB = [&](auto P) INL {
    constexpr int ph = P.v;
    if constexpr (ph < NSL) {
      constexpr int ad = ARENA + Tab::u(ph / 10) * UNIT + (ph % 10) * 1024;
      b[ph % (DIST + 1)] = lds128(lb[ad / BSTEP] + ad % BSTEP);
    }
    if constexpr (ph % 10 == 3 && ph + 7 < NSL) ring_peek(rg);
    if constexpr (ph % 10 == 7 && ph + 3 < NSL) ring_latch(rg);
    if constexpr (ph % 10 == 9 && ph + 1 < NSL) ring_check(rg, F0 + ph / 10 + 1);
    if constexpr (ph % 10 == 0 && ph > 0 && ph <= NSL) { CBAR(); ++pv; lds_st(pa, pv); CBAR(); }
  };
  wait();
  constexpr int W0 = 4;
  static_assert(W % 2 == 0 && W >= W0, "");
  sfor<0, W0>([&](auto S) INL { a[S.v] = lda<S.v>(ws, voff, abase); });
  SCHED();
  ring_peek(rg);
  ring_latch(rg);
  ring_check(rg, F0);
  sfor<0, DIST>(readB);
  SCHED();
  constexpr int X1 = 108 - 13, X0 = X1 - 28;
  sfor<0, NSL>([&](auto SL) INL {
    constexpr int sl = SL.v, s = sl / 6, t = sl % 6;
    if constexpr (s == 0) mfma16z(acc[t][0], b[sl % (DIST + 1)], a[0]); else mfma16i(acc[t][0], b[sl % (DIST + 1)], a[(2 * s) % W]);
    SCHED();
    if constexpr (sl < W - W0) a[W0 + sl] = lda<W0 + sl>(ws, voff, abase);
    if constexpr (s > 0 && (t == 1 || t == 4) && 2 * (s - 1) + W + t / 4 < 40) a[(2 * (s - 1) + t / 4) % W] = lda<2 * (s - 1) + W + t / 4>(ws, voff, abase);
    if constexpr (sl >= X0 && sl < X0 + 8 && (sl - X0) % 2 == 0) st[(sl - X0) / 2] = lda<(sl - X0) / 2>(ws, voff, stbase);
    if constexpr (sl == X1 - 10) csv = lds_ld(L_CSUM);
    if constexpr (sl == X1 - 6) csv = opq(ufl(csv));
    if constexpr (sl == X1 - 3) { if (__builtin_expect(ufl(csv) < 2, 0)) lds_wait_gt(L_CSUM, 1, rg.bud); CBAR(); }
    SCHED();
    if constexpr (s == 0) mfma16z(acc[t][1], b[sl % (DIST + 1)], a[1]); else mfma16i(acc[t][1], b[sl % (DIST + 1)], a[(2 * s + 1) % W]);
    SCHED();
    readB(IC<sl + DIST>{});
    if constexpr (sl >= X1 && sl < X1 + 3) {
      constexpr int i = sl - X1 + 1;
      const unsigned u0 = st[i][0], u1 = st[i][1], u2 = st[i][2], u3 = st[i][3];
      if constexpr (i == 1) { const unsigned w0 = st[0][0], w1 = st[0][1], w2 = st[0][2], w3 = st[0][3]; s_ = asf(w0); q_ = asf(w1); s1_ = asf(w2); q1_ = asf(w3); }
      s_ += asf(u0); q_ += asf(u1); s1_ += asf(u2); q1_ += asf(u3);
    }
    if constexpr (sl == X1 + 3) { const bool up = lane & 8; s_ = (up ? s1_ : s_) + ror8(up ? s_ : s1_); }
    if constexpr (sl == X1 + 4) { const bool up = lane & 8; q_ = (up ? q1_ : q_) + ror8(up ? q_ : q1_); }
    if constexpr (sl == X1 + 5) s_ = sum_halves(s_) * (1.f / 640.f);
    if constexpr (sl == X1 + 6) q_ = sum_halves(q_) * (1.f / 640.f);
    if constexpr (sl == X1 + 7) {
      q_ = fmaxf(q_ - s_ * s_, 0.f);
      const unsigned nm = pk(-s_, -s_);
      auto r = __builtin_amdgcn_permlane16_swap(nm, nm, false, false);
      xm[0][0] = r[0]; xm[1][0] = r[1];
    }
    if constexpr (sl == X1 + 8) {
      q_ = rbf(rbf(q_) + 1e-5f);
      sfor<0, 6>([&](auto T) INL { cf[T.v] = lds64(CS + T.v * 128 + (lane & 15) * 8); });
    }
    if constexpr (sl == X1 + 9) {
#pragma unroll
      for (int i = 0; i < 2; ++i) { const unsigned m = lane < 16 ? xm[i][0] : 0u; xm[i][0] = m; xm[i][1] = m & 0xffffu; }
    }
    if constexpr (sl == X1 + 10) q_ = __builtin_amdgcn_rcpf(rbf(__builtin_amdgcn_sqrtf(q_)));
    if constexpr (sl == X1 + 11) {
      auto r = __builtin_amdgcn_permlane16_swap(asu(q_), asu(q_), false, false);
      const unsigned r0 = r[0], r1 = r[1];
      rs[0] = asf(r0); rs[1] = asf(r1);
    }
    SCHED();
    if constexpr (s == 18) {
      constexpr int tc = (t + 3) % 6;
      mfma16k16i(acc[tc][0], cf[tc], xm[0]); mfma16k16i(acc[tc][1], cf[tc], xm[1]);
      SCHED();
    }
  });
  mfma_fence(acc[5][0], acc[5][1], acc[4][1]);
  SCHED();
}

template <int NS, int W, int DIST, int F0, int S0, class Tab, class Wait>
DI void gemm16(Ring& rg, rsrc_t ws, unsigned abase, unsigned (&res)[6], unsigned xbase, unsigned stbase, bool last, void* out, int t16i, int ct, Wait&& wait) {
  const int lane = fresh_lane();
  const unsigned voff = lane * 16;
  v4u a[W], b[DIST + 1][3];
  unsigned lb[3], lc[3];
  const int g4 = lane >> 4;
#pragma unroll
  for (int i = 0; i < 3; ++i) {
    lb[i] = voff + i * BSTEP; asm volatile("" : "+v"(lb[i]));
    lc[i] = g4 * 128 + (lane & 7) * 16 + i * BSTEP + ((lane & 8) ? 0x40000 : 0); asm volatile("" : "+v"(lc[i]));
  }
  unsigned pa = rg.prog, pv = rg.ls + S0;
  asm volatile("" : "+v"(pa), "+v"(pv));
  f32x4 acc[3];
#pragma unroll
  for (int t = 0; t < 3; ++t) acc[t] = f32x4{asf(res[2 * t] << 16), asf(res[2 * t] & 0xffff0000u), asf(res[2 * t + 1] << 16), asf(res[2 * t + 1] & 0xffff0000u)};
  auto readB = [&](auto S, auto T) INL {
    constexpr int s = S.v, t = T.v;
    if constexpr (s < NS) {
      constexpr int ad = ARENA + Tab::u(s / 4) * UNIT + (s % 4) * 2560;
      b[s % (DIST + 1)][t] = lds128((t < 2 ? lb : lc)[ad / BSTEP] + ad % BSTEP + t * 1024);
    }
    if constexpr (t == 0 && s % 4 == 1 && s + 3 < NS) ring_peek(rg);
    if constexpr (t == 0 && s % 4 == 3 && s + 1 < NS) ring_latch(rg);
    if constexpr (t == 2 && s % 4 == 3 && s + 1 < NS) ring_check(rg, F0 + s / 4 + 1);
    if constexpr (t == 0 && s % 4 == 0 && s > 0 && s <= NS) { CBAR(); ++pv; lds_st(pa, pv); CBAR(); }
  };
  const unsigned t32 = (t16i & 1) * 16 + (lane & 15);
  const unsigned pi = 2 * (g4 & 1) + (g4 >> 1);
  unsigned so1, so2;
  if constexpr (NS == 20) {
    const unsigned p1 = 5 * ct + pi, p2 = 5 * ct + 4, tb = xbase + (t16i & 1) * 1024 + (lane & 15) * 16;
    so1 = tb + (p1 >> 2) * 2048 + (p1 & 3) * 256; so2 = tb + (p2 >> 2) * 2048 + (p2 & 3) * 256 + (lane < 16 ? 0u : WS_USED);
  } else {
    so1 = xbase + (5 * ct) * 512 + pi * 512 + t32 * 16; so2 = xbase + (5 * ct + 4) * 512 + t32 * 16 + (lane < 16 ? 0u : WS_USED);
  }
  unsigned so3 = stbase + st_line(ct, t16i & 1) + (lane & 7) * 16 + (lane & 8) + (lane < 16 ? 0u : WS_USED);
  asm volatile("" : "+v"(so1), "+v"(so2), "+v"(so3), "+v"(acc[0]), "+v"(acc[1]), "+v"(acc[2]));
  if (HINT && HINT < 3 && !last) { const v4u sv = {SENT, SENT, SENT, SENT}; gst128(sv, ws, so1, 0); gst128(sv, ws, so2, 0); gst64(v2u{SENT, SENT}, ws, so3, 0); }
  wait();
  sfor<0, W>([&](auto S) INL { a[S.v] = lda<S.v>(ws, voff, abase); });
  SCHED();
  ring_peek(rg);
  ring_latch(rg);
  ring_check(rg, F0);
  sfor<0, DIST * 3>([&](auto I) INL { readB(IC<I.v / 3>{}, IC<I.v % 3>{}); });
  u64 bad = 0;
  if (HINT == 1) fresh2<0>(stale2(a[0], a[1]), a[0], a[1], ws, voff, abase, rg.bud);
  SCHED();
  if constexpr (NS == 48) { STAMP1(27); }
  sfor<0, NS>([&](auto S) INL {
    constexpr int s = S.v;
    if constexpr (NS == 48 && s % 12 == 1) STAMP3(24 + s / 12);
    sfor<0, 3>([&](auto T) INL {
      constexpr int t = 2 - T.v;
      mfma16i(acc[t], b[s % (DIST + 1)][t], a[s % W]);
      SCHED();
      if constexpr (s == 0 && T.v == 1) ring_keep(rg);
      readB(IC<s + DIST>{}, T);
      if constexpr (T.v == 2 && s + W < NS) a[s % W] = lda<s + W>(ws, voff, abase);
      if constexpr (HINT == 1 && T.v == 0 && (s & 1) && s + 2 < NS) bad = stale2(a[(s + 1) % W], a[(s + 2) % W]);
      if constexpr (HINT == 1 && T.v == 2 && (s & 1) && s + 2 < NS) fresh2<s + 1>(bad, a[(s + 1) % W], a[(s + 2) % W], ws, voff, abase, rg.bud);
      SCHED();
    });
  });
  mfma_fence(acc[0], acc[1], acc[2]);
  if constexpr (NS == 48) { STAMP1(28); STAMP3(28); }
#pragma unroll
  for (int t = 0; t < 3; ++t) { res[2 * t] = pk(acc[t][0], acc[t][1]); res[2 * t + 1] = pk(acc[t][2], acc[t][3]); }
  auto s0 = __builtin_amdgcn_permlane16_swap(res[0], res[2], false, false);
  auto s1 = __builtin_amdgcn_permlane16_swap(res[1], res[3], false, false);
  auto s2 = __builtin_amdgcn_permlane16_swap(res[4], res[4], false, false);
  auto s3 = __builtin_amdgcn_permlane16_swap(res[5], res[5], false, false);
  const v4u o01 = {s0[0], s1[0], s0[1], s1[1]};
  const v4u o2 = {s2[0], s3[0], s2[1], s3[1]};
  if (last) {
    GAS char* ob = (GAS char*)out + (size_t)(t16i * 16 + (lane & 15)) * 1280 + ct * 80;
    *(GAS v4u*)(ob + pi * 16) = o01;
    if (lane < 16) *(GAS v4u*)(ob + 64) = o2;
  } else {
    gst128(o01, ws, so1, 0);
    gst128(o2, ws, so2, 0);
    const f32x4 sv = acc[0] + acc[1] + acc[2], qv = acc[0] * acc[0] + acc[1] * acc[1] + acc[2] * acc[2];
    float s = (sv[0] + sv[1]) + (sv[2] + sv[3]), q = (qv[0] + qv[1]) + (qv[2] + qv[3]);
    s = sum_halves(sum_rows16(s));
    q = sum_halves(sum_rows16(q));
    if (!(ABL & 2)) gst64(v2u{asu(s), asu(q)}, ws, so3, 0);
  }
}

constexpr float QSCALE = 0.22360679774997896f * 1.4426950408889634f;
constexpr float NEG = -1e30f;

DI float max16(const f32x16& s) {
  const float a = max3(s[0], s[1], s[2]), b = max3(s[3], s[4], s[5]), c = max3(s[6], s[7], s[8]);
  const float d = max3(s[9], s[10], s[11]), e = max3(s[12], s[13], s[14]);
  return __builtin_elementwise_maximum(max3(a, b, c), max3(d, e, s[15]));
}
DI void kv_write(unsigned kb, int lane, v4u k1, v4u ta, v4u tb) {
  *(LAS v4u*)(kb + lane * 16) = k1;
  *(LAS v2u*)(kb + 1024 + (lane ^ 32) * 8) = v2u{ta[0], ta[1]};
  const unsigned vb = kb + 1536 + (lane & 31) * 48 + (lane >> 5) * 8;
  *(LAS v2u*)(vb) = v2u{ta[2], ta[3]};
  *(LAS v2u*)(vb + 16) = v2u{tb[0], tb[1]};
  *(LAS v2u*)(vb + 32) = v2u{tb[2], tb[3]};
}

DI int kmapO4(int P, int t) {
  const int p = P / 5, r = P - 5 * p;
  if (r == 2) return 20 * (2 * p + t) + 16;
  return 20 * (2 * p + (r > 2)) + 8 * t + 4 * (r > 2 ? r - 3 : r);
}
DI int kmapD4(int P, int t) {
  const int jj = P / 6, r = P - 6 * jj;
  return 48 * jj + (r < 4 ? 16 * t + 4 * r : 32 + 8 * (r - 4) + 4 * t);
}
DI v4u tr_frag(unsigned a0, unsigned a1) { const v2u x = ldstr(a0), y = ldstr(a1); return v4u{x[0], x[1], y[0], y[1]}; }
DI float sum8(v4u f, float s) {
#pragma unroll
  for (int i = 0; i < 4; ++i) { const unsigned u = f[i]; s = dot2(u, 0x3f803f80u, s); }
  return s;
}

DI void csum_rows(f32x4 c, unsigned la) {
  unsigned o[8];
#pragma unroll
  for (int i = 0; i < 4; ++i) {
    const float v = c[i], hi = rbf(v), mid = rbf(v - hi), lo = rbf(v - hi - mid);
    o[2 * i] = pk(hi, mid); o[2 * i + 1] = pk(lo, 0.f);
  }
  *(LAS v4u*)la = v4u{o[0], o[1], o[2], o[3]};
  *(LAS v4u*)(la + 16) = v4u{o[4], o[5], o[6], o[7]};
}

#if defined(__HIP_DEVICE_COMPILE__) && NOPK
#define NO_PK __attribute__((target("no-packed-fp32-ops")))
#else
#define NO_PK
#endif
__global__ void NO_PK __launch_bounds__(768) mega_kernel(unsigned char* wsp, void* out, const unsigned char* xin, const unsigned char* wqkv, const unsigned char* wo,
                                                   const unsigned char* wg, const unsigned char* wu, const unsigned char* wd) {
  __shared__ __attribute__((aligned(16))) char smem[LDS_BYTES];
  const int wave = ufl(threadIdx.x >> 6);
  asm volatile("" :: "v"((LAS char*)smem) : "memory");
  const int bx = blockIdx.x & 7, br = blockIdx.x >> 3;
#define GMAP 5
  const int band = (bx & 4) + (br >> 3), j = (bx & 3) * 8 + (br & 7);
  const int ct = (j >> 4) * 8 + (j & 7), rsel = (j >> 3) & 1;
  const rsrc_t ws = __builtin_amdgcn_make_buffer_rsrc(wsp, 0, WS_USED, 0x00020000);
  constexpr unsigned OOR = 0x40000000u;
  if (wave == 0) lds_st(MISC + (fresh_lane() & 31) * 4, 0u);
  WG_BARRIER();
  const int tk = blockIdx.x * 8 + ((wave - 4) & 7);
  const unsigned sb = wave < 4 ? STG_X + (wave - 1) * 5120 : STG + (wave - 4) * 8192;
  {
    const int lane = fresh_lane();
    const bool isx = wave < 4;
    dma_raw(isx ? xin : wqkv, isx ? 2048 * 1280 : 640 * 3840, isx || (lane & 7) < 5 ? (lane >> 3) * 1280 + (lane & 7) * 16 : OOR,
            isx ? (band * 256 + j * 8) * 1280 + (wave - 1) * 640 : (tk >> 4) * (16 * 3840) + (tk & 15) * 80, isx ? 128 : 10240, sb,
            opq(isx ? (wave == 1 || wave == 2 ? 5 : 0) : tk < 640 ? 6 : 0));
  }

  if (wave == 0) {
    const int lane = fresh_lane();
    __builtin_amdgcn_s_setprio(3);
    const v4u wsd = {(unsigned)(uintptr_t)wsp, (unsigned)((uintptr_t)wsp >> 32) & 0xffffu, WS_USED, 0x00020000u};
    unsigned bud = 0;
    const unsigned tag = ufl(__builtin_amdgcn_raw_buffer_load_b32(ws, 0, OFF_EP, SC1)) + 1u;
    lds_st(L_TAG, tag);
    CBAR();
    lds_st(L_TAGV, 1u);
    HSTAMP(0, 24);
    for (;;) {
      if (((unsigned)w_ballot(rmw_poll_pairs((GAS unsigned*)(wsp + OFF_IN + (band * 32 + j) * 512), lane * 8, 0xfffffu, tag)) & 0xfffffu) == 0xfffffu) break;
      SPEND(bud);
    }
    lds_st(L_WQOK, 1u);
    HSTAMP(0, 25);
#pragma unroll 1
    for (int f = 0; f < 6; ++f) dma_fill(wsd, lane * 16, OFF_WQ + j * 81920 + f * UNIT, ARENA + f * UNIT);
    asm volatile("s_waitcnt vmcnt(50)" ::: "memory"); lds_st(L_LANDED, 1u); HSTAMP(0, 26);
    dma_fill(wsd, lane * 16, OFF_WQ + j * 81920 + 6 * UNIT, ARENA + 6 * UNIT);
    asm volatile("s_waitcnt vmcnt(50)" ::: "memory"); lds_st(L_LANDED, 2u);
    dma_fill(wsd, lane * 16, OFF_WQ + j * 81920 + 7 * UNIT, ARENA + 7 * UNIT);
    asm volatile("s_waitcnt vmcnt(50)" ::: "memory"); lds_st(L_LANDED, 3u);
    asm volatile("s_waitcnt vmcnt(40)" ::: "memory"); lds_st(L_LANDED, 4u);
    asm volatile("s_waitcnt vmcnt(30)" ::: "memory"); lds_st(L_LANDED, 5u);
    asm volatile("s_waitcnt vmcnt(20)" ::: "memory"); lds_st(L_LANDED, 6u);
    asm volatile("s_waitcnt vmcnt(10)" ::: "memory"); lds_st(L_LANDED, 7u);
    asm volatile("s_waitcnt vmcnt(0)" ::: "memory"); lds_st(L_LANDED, 8u); HSTAMP(0, 27);
    __builtin_amdgcn_s_setprio(PRIO_H);
    lds_wait_gt(L_W2OK, 0u, bud);
    HSTAMP(0, 28);
    unsigned issued = 8, published = 8;
#pragma unroll 1
    for (int lp = 1; lp < 8; ++lp) {
      const int L = lp >> 2, ph = lp & 3;
      unsigned long long units, needs_lo, needs_hi; int n; unsigned src;
      if (ph == 0) { units = 0x76543210ull; n = 8; src = OFF_WQ + j * 81920;
        needs_lo = 17ull | 25ull << 8 | 26ull << 16 | 27ull << 24 | 28ull << 32 | 29ull << 40 | 32ull << 48 | 33ull << 56; needs_hi = 0; }
      else if (ph == 1) { units = 0x54321ull; n = 5; src = OFF_WO + ct * 51200;
        needs_lo = 40ull | 41ull << 8 | 42ull << 16 | 43ull << 24 | 44ull << 32; needs_hi = 0; }
      else if (ph == 2) { units = 0x21CBA98760EDull; n = 12; src = OFF_WG + j * 122880;
        needs_lo = 30ull | 31ull << 8 | 39ull << 16 | 45ull << 24 | 46ull << 32 | 47ull << 40 | 47ull << 48 | 47ull << 56;
        needs_hi = 47ull | 47ull << 8 | 48ull << 16 | 49ull << 24; }
      else { units = 0xCBA9876ED543ull; n = HOLD ? 3 : 12; src = OFF_WD + ct * 122880;
        needs_lo = 50ull | 51ull << 8 | 52ull << 16 | 53ull << 24 | 54ull << 32 | 56ull << 40 | 57ull << 48 | 58ull << 56;
        needs_hi = 59ull | 60ull << 8 | 61ull << 16 | 62ull << 24; }
#pragma unroll 1
      for (int f = 0; f < n; ++f) {
        const int unit = (int)((units >> (4 * f)) & 15ull);
        const int nd = (int)(((f < 8 ? needs_lo >> (8 * f) : needs_hi >> (8 * (f - 8)))) & 255ull) + (L - 1) * NSTAGE;
        if (nd > 0) {
          unsigned v = lds_ld(L_PROG + (lane & 7) * 4);
          if (!w_all((int)v >= nd)) {
            if (published != issued) {
              if (issued - published >= 2) { asm volatile("s_waitcnt vmcnt(10)" ::: "memory"); lds_st(L_LANDED, issued - 1); }
              asm volatile("s_waitcnt vmcnt(0)" ::: "memory");
              published = issued; lds_st(L_LANDED, published);
            }
            prog_wait<SL_L>((unsigned)nd, bud);
          }
        }
        dma_fill(wsd, lane * 16, src + f * UNIT, ARENA + unit * UNIT);
        ++issued;
        if (issued - published > LDEPTH) {
          asm volatile("s_waitcnt vmcnt(%0)" :: "i"(10 * LDEPTH) : "memory");
          published = issued - LDEPTH; lds_st(L_LANDED, published);
        }
      }
      if (HOLD && ph == 3) {
        asm volatile("s_waitcnt vmcnt(10)" ::: "memory"); lds_st(L_LANDED, issued - 1);
        asm volatile("s_waitcnt vmcnt(0)" ::: "memory"); lds_st(L_LANDED, issued);
        if (L == 1) return;
        issued += 9; published = issued;
        lds_wait_gt<SL_L>(L_LANDED, issued - 1, bud);
      }
    }
    asm volatile("s_waitcnt vmcnt(10)" ::: "memory");
    lds_st(L_LANDED, issued - 1);
    asm volatile("s_waitcnt vmcnt(0)" ::: "memory");
    lds_st(L_LANDED, issued);
    return;
  }

  if (wave >= 4) {
    const int cw = wave - 4, grp = cw >> 2, wi = cw & 3;
    const int qt = (1 - grp) * 4 + (QREV ? 3 - wi : wi);
    const int t32_ = band * 8 + qt;
    const int t16_ = band * 16 + (1 - grp) * 8 + rsel * 4 + wi;
    const int rep = j & 3;
    Ring rg;
    rg.prog = L_PROG + cw * 4;
    rg.bud = 0;
    rg.kp[0] = 0; rg.kp[1] = 0;
#define PRIO_E 2
#define PRIO_L 1
    if (grp == 0) __builtin_amdgcn_s_setprio(PRIO_E); else __builtin_amdgcn_s_setprio(PRIO_L);
    const int dslot = qt + (qt < 4 ? 4 : 8);
    STAMP(30);
    STAMP(0);
    unsigned res[6] = {0u, 0u, 0u, 0u, 0u, 0u};
    unsigned tag;
    {
      const int lane = fresh_lane();
      const int gam = lane >> 4, q = (lane & 15) >> 2, p = lane & 3, hh = gam >> 1, g = (gam & 1) * 4 + p;
      const v4u ones = {0x3f803f80u, 0x3f803f80u, 0x3f803f80u, 0x3f803f80u};
      f32x16 z;
#pragma unroll
      for (int i = 0; i < 16; ++i) z[i] = 0.f;
      if (cw == 7) {
        const int ci = j * 7 + lane;
        gst32(0u, ws, lane < 7 ? OFF_CTR + ((ci >> 5) * 64 + band * 8 + ((ci >> 2) & 7)) * 1024 + (ci & 3) * 256 : OOR);
      }
      if (tk < 640) {
        const int hp = tk & 15, ks = tk >> 4;
        const unsigned r = sb + (8 * hh + q) * 384;
        unsigned c0 = r + (g < 4 ? 0 : 128) + (g & 3) * 8, c1 = r + (g == 0 ? 32 : g == 1 ? 128 + 32 : 256 + ((g < 7 ? g : 6) - 2) * 8);
        unsigned dst = OFF_WQ + hp * 163840 + ks * 2048 + lane * 16, cs = OFF_CSQ + (hp * 80 + ks) * 256 + lane * 4;
        unsigned fa = lane < 16 ? OFF_IN + ((lane >> 1) * 32 + 2 * hp + (lane & 1)) * 512 + ks * 4 : OOR;
        STAMP4(24);
#pragma unroll 1
        for (int pass = DRYPASS ? 0 : 1; pass < 2; ++pass) {
          const unsigned off = pass ? 0u : OOR;
          if (pass) DRAIN();
          HSTAMP(2, 24);
          STAMP4(25);
          v4u f[2][2];
#pragma unroll
          for (int h2 = 0; h2 < 2; ++h2) {
            f[h2][0] = tr_frag(c0 + h2 * 40, c0 + h2 * 40 + 1536);
            f[h2][1] = tr_frag(c1 + h2 * 40, c1 + h2 * 40 + 1536);
          }
#pragma unroll
          for (int h2 = 0; h2 < 2; ++h2) {
            if ((lane & 31) >= 28) f[h2][1] = v4u{0u, 0u, 0u, 0u};
            gst128(f[h2][0], ws, dst + off + h2 * 81920, 0); gst128(f[h2][1], ws, dst + off + h2 * 81920 + 1024, 0);
          }
#pragma unroll
          for (int h2 = 0; h2 < 2; ++h2) {
            const float s0 = mfma32(ones, f[h2][0], z)[0], s1 = mfma32(ones, f[h2][1], z)[0];
            gst32(asu(lane < 32 ? s0 : s1), ws, cs + off + h2 * 10240);
          }
          if (pass) lds_wait_gt(L_TAGV, 0u, rg.bud);
          tag = ufl(lds_ld(L_TAG));
          STAMP4(26);
          if (pass) DRAIN();
          STAMP4(27);
          gst32(tag, ws, fa + off);
          HSTAMP(2, 25);
        }
        lds_add1(L_CRIT);
      } else {
        const bool gu = tk < 1280, dn = tk < 1664;
        const int i = gu ? tk - 640 : dn ? tk - 1280 : tk - 1664;
        unsigned a0, a1, a2, a3, a4, dst, o1, o2;
        if (L2GATE) lds_wait_gt(L_CRIT, L2GATE - 1, rg.bud);
        if (gu) {
          const unsigned vo = (lane & 7) < 6 ? (lane >> 3) * 3072 + (lane & 7) * 16 : OOR, so = (i >> 5) * (32 * 3072) + (i & 31) * 96;
          dma_raw(wg, 640 * 3072, vo, so, 8 * 3072, sb, 4);
          dma_raw(wu, 640 * 3072, vo, so, 8 * 3072, sb + 4096, 4);
          a0 = sb + (8 * gam + q) * 128 + p * 8; a1 = a0 + 32; a2 = a0 + 64; a3 = 4096; a4 = 512;
          dst = OFF_WG + (i & 31) * 122880 + (i >> 5) * 6144; o1 = 2048; o2 = 4096 + lane * 16;
        } else {
          const unsigned vo = (lane & 15) < 10 ? gam * 1280 + (lane & 15) * 16 : OOR;
          if (tk < 1824) {
#pragma unroll 1
            for (int u = 0; u < 8; ++u) {
              const int P = 4 * (i >> 3) + (u >> 1);
              const int row = dn ? kmapD4(P, u & 1) : kmapO4(P, u & 1);
              dma_raw(dn ? wd : wo, dn ? 1536 * 1280 : 640 * 1280, vo, row * 1280 + (i & 7) * 160, 0, sb + u * 1024, 1);
            }
          }
          const unsigned r = sb + gam * 2048 + q * 256;
          a0 = r + p * 8; a1 = a0 + 32; a2 = r + 64 + (p & 1) * 8; a3 = 80; a4 = 1024; o1 = 1024;
          dst = tk >= 1824 ? OOR : dn ? OFF_WD + (i & 7) * 245760 + (i >> 3) * 2560 : OFF_WO + (i & 7) * 102400 + (i >> 3) * 2560;
          o2 = (lane & 15) < 8 ? 2048 + gam * 128 + (lane & 7) * 16 : OOR;
        }
        const unsigned dstep = gu ? 1024 : dn ? 122880 : 51200;
        STAMP4(24);
        asm volatile("s_waitcnt vmcnt(0)" : "+v"(a0), "+v"(a1), "+v"(a2), "+v"(o2) :: "memory");
        STAMP4(25);
        v4u f[2][3];
#pragma unroll
        for (int h2 = 0; h2 < 2; ++h2) {
          f[h2][0] = tr_frag(a0 + h2 * a3, a0 + h2 * a3 + a4);
          f[h2][1] = tr_frag(a1 + h2 * a3, a1 + h2 * a3 + a4);
          f[h2][2] = tr_frag(a2 + h2 * a3, a2 + h2 * a3 + a4);
        }
        if (S2GATE) lds_wait_gt(L_CRIT, S2GATE - 1, rg.bud);
#pragma unroll
        for (int h2 = 0; h2 < 2; ++h2) {
          gst128(f[h2][0], ws, dst + h2 * dstep + lane * 16, 0); gst128(f[h2][1], ws, dst + h2 * dstep + lane * 16 + o1, 0);
          gst128(f[h2][2], ws, dst + h2 * dstep + o2, 0);
        }
        if (gu) {
          float c[6];
#pragma unroll
          for (int k = 0; k < 6; ++k) c[k] = mfma16(ones, f[k & 1][k >> 1], f32x4{0.f, 0.f, 0.f, 0.f})[0];
          const unsigned cs = OFF_CSG + ((i & 31) * 20 + (i >> 5)) * 512 + lane * 4;
          gst32(asu(gam == 0 ? c[0] : gam == 1 ? c[1] : gam == 2 ? c[2] : c[3]), ws, cs);
          gst32(gam == 0 ? asu(c[4]) : gam == 1 ? asu(c[5]) : 0u, ws, cs + 256);
        }
        lds_wait_gt(L_TAGV, 0u, rg.bud);
        tag = ufl(lds_ld(L_TAG));
        STAMP4(26);
        DRAIN();
        STAMP4(27);
      }
      if (lds_inc1(L_STG) == 7u) gst32(tag, ws, lane == 0 ? OFF_F2 + blockIdx.x * 4 : OOR);
    }
    STAMP4(28);
    HSTAMP(2, 26);
#pragma unroll 1
    for (int L = 0; L < 2; ++L) {
      rg.lf = L * NFILL; rg.ls = L * NSTAGE;
      const int t32i = opq(t32_), t16i = opq(t16_);
      PP(PHPRIO & 3);
      v4u q1; v2u q2;
      {
        f32x16 acc[2]; float rs;
        gemm32<2, WQ_, DQ_, 0, 0, TabQ, L_CQ>(acc, rs, rg, ws, OFF_XA + (2 * L) * XA_SZ + t32i * XA_T, OFF_ST + (2 * L) * ST_SZ + t32i * 4096,
                                            [&]() INL {
          if (HINT && HINT < 3) {
            const int lane = fresh_lane(), h = lane >> 5;
            const v4u sv = {SENT, SENT, SENT, SENT};
            if (band < 7) {
              const unsigned gb = OFF_KVH + L * KVH_SZ + ((band * 32 + j) * 8 + qt) * KVH_T;
              gst128(sv, ws, lane * 16, gb); gst128(sv, ws, lane * 16 + 1024, gb); gst128(sv, ws, lane * 16 + 2048, gb);
            }
            const unsigned ob = OFF_OA + L * OA_SZ + (2 * t32i) * OA_T + (5 * (j >> 1)) * 256;
            const unsigned tv = ((lane >> 4) & 1) * OA_T + (lane & 15) * 16;
            gst128(sv, ws, tv + h * 256, ob + (3 * (j & 1)) * 256);
            gst64(v2u{SENT, SENT}, ws, tv + (h ? WS_USED : 0u), ob + 2 * 256 + (j & 1) * 8);
          }
          if (L > 0) {
            ctr_wait(wsp, 3, t32i, rep, rg.bud, rg.kp);
            if (TURNQ && grp == 0) lds_wait_gt(rg.prog + 16, 25 + TURNQ, rg.bud);
          } else {
            DRAIN();
            unsigned xr = 0;
            lds_wait_bits(L_XRDY, 0xfu << (4 * qt), xr, rg.bud);
            if (TURN0 > 0 && grp == 1) lds_wait_gt(rg.prog - 16, TURN0 - 1, rg.bud);
          }
          STAMP(1 + L * 12); },
          [&]() INL { if (L > 0) prog_wait(L * NSTAGE, rg.bud); else lds_wait_gt(L_STG, 7u, rg.bud); });
        STAMP(2 + L * 12);
        if (L == 0 && cw == 0) HSTAMP(2, 27);
        if (L == 0 && cw == 4) HSTAMP(2, 28);
        const int lane = fresh_lane(), h = lane >> 5;
        v4u k1, ta, tb;
        const float rq = rs * QSCALE, r0 = h ? rs : rq;
#pragma unroll
        for (int i = 0; i < 4; ++i) k1[i] = pk(acc[0][8 + 2 * i] * rs, acc[0][9 + 2 * i] * rs);
        ta[0] = pk(acc[1][0] * r0, acc[1][1] * r0); ta[1] = pk(acc[1][2] * r0, acc[1][3] * r0);
        ta[2] = pk(acc[1][4] * rs, acc[1][5] * rs); ta[3] = pk(acc[1][6] * rs, acc[1][7] * rs);
#pragma unroll
        for (int i = 0; i < 4; ++i) tb[i] = pk(acc[1][8 + 2 * i] * rs, acc[1][9 + 2 * i] * rs);
        if (h) { tb[2] = 0x3f803f80u; tb[3] = 0x3f803f80u; }
        q2[0] = h ? 0x3f80u : ta[0]; q2[1] = h ? 0u : ta[1];
        if (!h) { ta[0] = 0u; ta[1] = 0u; }
        if (band < 7 && !(ABL & 1)) {
          const unsigned gb = OFF_KVH + L * KVH_SZ + ((band * 32 + j) * 8 + qt) * KVH_T;
          gst128(k1, ws, lane * 16, gb); gst128(ta, ws, lane * 16 + 1024, gb); gst128(tb, ws, lane * 16 + 2048, gb);
          if (HINT) gst32(tag, ws, lane == 0 ? OFF_HALOF + (((L * 8 + band) * 32 + j) * 2 + (qt >> 2)) * 256 + (qt & 3) * 4 : WS_USED);
        }
        kv_write(KV_OFF + dslot * KV_T, lane, k1, ta, tb);
        CBAR();
        lds_or1(L_RDY + L * 4, 1u << dslot);
#pragma unroll
        for (int i = 0; i < 4; ++i) q1[i] = pk(acc[0][2 * i] * rq, acc[0][2 * i + 1] * rq);
      }
      STAMP(3 + L * 12);
      PP((PHPRIO >> 2) & 3);
      {
        const int lane = fresh_lane(), h = lane >> 5;
        f32x16 o, negm, sc[2];
        const int topA = dslot, topB = grp == 0 ? 8 : 12;
        const int top0 = grp == 0 ? topA : topB, top1 = grp == 0 ? topB : topA;
        const unsigned vlo = 1536 + (4 * h + ((lane & 15) >> 2)) * 48 + (((lane >> 4) & 1) == 0 ? 8 * (lane & 3) : ((lane & 3) == 0 ? 32 : 40));
        unsigned ka[2], kb[2], va[2], vd;
        ka[0] = KV_OFF + (top0 - 4) * KV_T + lane * 16; ka[1] = KV_OFF + (top1 - 4) * KV_T + lane * 16;
        kb[0] = KV_OFF + (top0 - 4) * KV_T + 1024 + lane * 8; kb[1] = KV_OFF + (top1 - 4) * KV_T + 1024 + lane * 8;
        va[0] = KV_OFF + (top0 - 4) * KV_T + vlo; va[1] = KV_OFF + (top1 - 4) * KV_T + vlo;
        vd = KV_OFF + dslot * KV_T + vlo;
        asm volatile("" : "+v"(ka[0]), "+v"(ka[1]), "+v"(kb[0]), "+v"(kb[1]), "+v"(va[0]), "+v"(va[1]), "+v"(vd));
        const int ql = (lane & 31) - 4 * h;
        unsigned rdyv = lds_ld(L_RDY + L * 4);
        {
          f32x16 z;
#pragma unroll
          for (int r = 0; r < 16; ++r) { z[r] = 0.f; o[r] = 0.f; }
          sc[0] = mfma32(lds128(KV_OFF + dslot * KV_T + lane * 16), q1, z);
          sc[0] = mfma32k8(lds64(KV_OFF + dslot * KV_T + 1024 + lane * 8), q2, sc[0]);
#pragma unroll
          for (int r = 0; r < 16; ++r) sc[0][r] = ((r & 3) + 8 * (r >> 2)) <= ql ? sc[0][r] : NEG;
          const float m = max_halves(max16(sc[0]));
#pragma unroll
          for (int r = 0; r < 16; ++r) { sc[0][r] -= m; negm[r] = -m; }
        }
        STAMP2(24);
        unsigned rdy = ufl(rdyv);
        sfor<0, 9>([&](auto N) INL {
          if constexpr ((ABL & 4) && N.v > 0 && N.v < 8) return;
          constexpr int n = N.v;
          f32x16& s = sc[n & 1];
          f32x16& sn = sc[(n + 1) & 1];
          v4u k1; v2u k2, v0, v1, v2, v3;
          if constexpr (n == ANN) {
            if (!HINT && band < 7 && !(ABL & 1)) {
              DRAIN();
              gst32(tag, ws, lane == 0 ? OFF_HALOF + (((L * 8 + band) * 32 + j) * 2 + (qt >> 2)) * 256 + (qt & 3) * 4 : WS_USED);
            }
            STAMP2(25);
          }
          {
            const unsigned a = n == 0 ? vd : va[(n - 1) >> 2] + (3 - ((n - 1) & 3)) * KV_T;
            v0 = ldstr(a); v1 = ldstr(a + 384); v2 = ldstr(a + 768); v3 = ldstr(a + 1152);
          }
          if constexpr (n < 8) {
            lds_wait_bits(L_RDY + L * 4, 1u << ((n < 4 ? top0 : top1) - 1 - (n & 3)), rdy, rg.bud);
            k1 = lds128(ka[n >> 2] + (3 - (n & 3)) * KV_T); k2 = lds64(kb[n >> 2] + (3 - (n & 3)) * KV_T);
          }
          if constexpr (n == 4 || n == 8) {
            if (grp == (n == 4 ? 0 : 1)) {
#pragma unroll
              for (int r = 0; r < 16; ++r) s[r] = ((r & 3) + 8 * (r >> 2)) > ql ? s[r] : NEG;
            }
          }
          SCHED();
          v4u p, p2;
          float e[16];
          e[0] = ex2(s[0]); e[1] = ex2(s[1]); e[2] = ex2(s[2]); SCHED();
          p[0] = pk(e[0], e[1]); e[3] = ex2(s[3]); e[4] = ex2(s[4]); SCHED();
          p[1] = pk(e[2], e[3]); e[5] = ex2(s[5]); e[6] = ex2(s[6]); SCHED();
          if constexpr (n < 8) sn = mfma32(k1, q1, negm);
          SCHED();
          p[2] = pk(e[4], e[5]); e[7] = ex2(s[7]); e[8] = ex2(s[8]); SCHED();
          p[3] = pk(e[6], e[7]); SCHED();
          if constexpr (n < 8) sn = mfma32k8(k2, q2, sn);
          SCHED();
          e[9] = ex2(s[9]); e[10] = ex2(s[10]); SCHED();
          p2[0] = pk(e[8], e[9]); e[11] = ex2(s[11]); SCHED();
          o = mfma32(v4u{v0[0], v0[1], v1[0], v1[1]}, p, o);
          SCHED();
          e[12] = ex2(s[12]); SCHED();
          p2[1] = pk(e[10], e[11]); e[13] = ex2(s[13]); e[14] = ex2(s[14]); e[15] = ex2(s[15]); SCHED();
          p2[2] = pk(e[12], e[13]); SCHED();
          p2[3] = pk(e[14], e[15]); SCHED();
          o = mfma32(v4u{v2[0], v2[1], v3[0], v3[1]}, p2, o);
          SCHED();
          if constexpr (n == 3) STAMP2(26);
          if constexpr (n == 5) STAMP2(27);
          if constexpr (n == 7) STAMP2(28);
        });
        if (__builtin_expect(w_any(!(o[12] < 1e18f)), 0)) {
          float m = NEG;
#pragma unroll
          for (int r = 0; r < 16; ++r) { o[r] = 0.f; negm[r] = 0.f; }
#pragma unroll 1
          for (int n = 0; n < 9; ++n) {
            const int sl = n < 5 ? dslot - n : topB + 4 - n;
            const unsigned kt = KV_OFF + sl * KV_T;
            f32x16 s = mfma32(lds128(kt + lane * 16), q1, negm);
            s = mfma32k8(lds64(kt + 1024 + lane * 8), q2, s);
#pragma unroll
            for (int r = 0; r < 16; ++r) {
              const bool lo = ((r & 3) + 8 * (r >> 2)) <= ql;
              s[r] = (n == 0 && !lo) || (n == 4 && lo) ? NEG : s[r];
            }
            const float mn = fmaxf(m, max_halves(max16(s))), f = ex2(m - mn);
            m = mn;
            v4u p, p2;
#pragma unroll
            for (int i = 0; i < 4; ++i) { p[i] = pk(ex2(s[2 * i] - m), ex2(s[2 * i + 1] - m)); p2[i] = pk(ex2(s[8 + 2 * i] - m), ex2(s[9 + 2 * i] - m)); }
#pragma unroll
            for (int r = 0; r < 16; ++r) o[r] *= f;
            const v2u v0 = ldstr(kt + vlo), v1 = ldstr(kt + vlo + 384), v2 = ldstr(kt + vlo + 768), v3 = ldstr(kt + vlo + 1152);
            o = mfma32(v4u{v0[0], v0[1], v1[0], v1[1]}, p, o);
            o = mfma32(v4u{v2[0], v2[1], v3[0], v3[1]}, p2, o);
          }
        }
        CBAR();
        lds_st(rg.prog, rg.ls + 9);
        CBAR();
        const float inv = __builtin_amdgcn_rcpf(o[12]);
        v4u o1;
#pragma unroll
        for (int i = 0; i < 4; ++i) o1[i] = pk(o[2 * i] * inv, o[2 * i + 1] * inv);
        const v2u o2 = {pk(o[8] * inv, o[9] * inv), pk(o[10] * inv, o[11] * inv)};
        const unsigned ob = OFF_OA + L * OA_SZ + (2 * t32i) * OA_T + (5 * (j >> 1)) * 256;
        const unsigned tv = ((lane >> 4) & 1) * OA_T + (lane & 15) * 16;
        gst128(o1, ws, tv + h * 256, ob + (3 * (j & 1)) * 256);
        gst64(o2, ws, tv + (h ? WS_USED : 0u), ob + 2 * 256 + (j & 1) * 8);
        STAMP(4 + L * 12);
        if (L == 0) lds_wait_gt(L_W2OK, 0u, rg.bud);
        ctr_arrive(wsp, 4 * L, t32i);
        if (L == 0) {
          const int g4 = lane >> 4;
          const unsigned ro = ((t16i & 1) * 16 + (lane & 15)) * 16 + (g4 & 1) * 8, rb = OFF_XA + (t16i >> 1) * XA_T + (5 * ct) * 512;
          const v2u r0 = gld64(ws, ro + (g4 >> 1) * 512, rb), r1 = gld64(ws, ro + (g4 >> 1) * 512 + 1024, rb), r2 = gld64(ws, g4 < 2 ? ro + 2048 : WS_USED, rb);
          res[0] = r0[0]; res[1] = r0[1]; res[2] = r1[0]; res[3] = r1[1]; res[4] = r2[0]; res[5] = r2[1];
        }
      }
      STAMP(5 + L * 12);
      PP((PHPRIO >> 4) & 3);
      gemm16<20, WO_, 3, 8, 9, TabO>(rg, ws, OFF_OA + L * OA_SZ + t16i * OA_T, res, OFF_XA + (2 * L + 1) * XA_SZ + (t16i >> 1) * XA_T,
                                     OFF_ST + (2 * L + 1) * ST_SZ + (t16i >> 1) * 4096, false, out, t16i, ct,
                                     [&]() INL { ctr_wait(wsp, 4 * L, t16i >> 1, rep, rg.bud, rg.kp); STAMP(6 + L * 12); });
      STAMP(7 + L * 12);
      ctr_arrive(wsp, 4 * L + 1, t16i >> 1);
      PP((PHPRIO >> 6) & 3);
      {
        f32x4 acc[6][2]; float rs[2];
        gemmG<WG_, DG_, 13, 14, TabG, L_CG>(acc, rs, rg, ws, OFF_XA + (2 * L + 1) * XA_SZ + t32i * XA_T, OFF_ST + (2 * L + 1) * ST_SZ + t32i * 4096,
                                            [&]() INL { ctr_wait(wsp, 4 * L + 1, t32i, rep, rg.bud, rg.kp); STAMP(8 + L * 12); });
        const int lane = fresh_lane(), g4 = lane >> 4;
        const unsigned hb = OFF_HA + L * HA_SZ + (2 * t32i) * HA_T + (6 * j) * 256;
        unsigned e2[2][2];
#pragma unroll
        for (int tt = 0; tt < 2; ++tt) {
          const float r2 = rs[tt] * rs[tt];
          unsigned hp[6];
#pragma unroll
          for (int bk = 0; bk < 3; ++bk)
#pragma unroll
            for (int y = 0; y < 2; ++y)
              hp[2 * bk + y] = pk(acc[2 * bk][tt][2 * y] * r2 * acc[2 * bk + 1][tt][2 * y], acc[2 * bk][tt][2 * y + 1] * r2 * acc[2 * bk + 1][tt][2 * y + 1]);
          gst128(v4u{hp[0], hp[1], hp[2], hp[3]}, ws, lane * 16 + tt * HA_T, hb);
          e2[tt][0] = hp[4]; e2[tt][1] = hp[5];
        }
        auto s0 = __builtin_amdgcn_permlane16_swap(e2[0][0], e2[1][0], false, false);
        auto s1 = __builtin_amdgcn_permlane16_swap(e2[0][1], e2[1][1], false, false);
        gst128(v4u{s0[0], s1[0], s0[1], s1[1]}, ws, (g4 & 1) * HA_T + 1024 + (g4 >> 1) * 256 + (lane & 15) * 16, hb);
      }
      STAMP(9 + L * 12);
      ctr_arrive(wsp, 4 * L + 2, t32i);
      PP((PHPRIO >> 8) & 3);
      gemm16<48, WD_, 3, 25, 26, TabD>(rg, ws, OFF_HA + L * HA_SZ + t16i * HA_T, res, OFF_XA + 2 * XA_SZ + (t16i >> 1) * XA_T,
                                       OFF_ST + 2 * ST_SZ + (t16i >> 1) * 4096, L == 1, out, t16i, ct,
                                       [&]() INL { ctr_wait(wsp, 4 * L + 2, t16i >> 1, rep, rg.bud, rg.kp); STAMP(10 + L * 12); });
      STAMP(11 + L * 12);
      if (L == 0) ctr_arrive(wsp, 3, t16i >> 1);
    }
  } else {
    const int lane = fresh_lane();
    const int half = 2 - wave;
    __builtin_amdgcn_s_setprio(3);
    unsigned bud = 0;
    unsigned tag = 0;
    if (wave == 3) { lds_wait_gt(L_TAGV, 0u, bud); tag = ufl(lds_ld(L_TAG)); }
    if (wave < 3) {
      const int hx = wave - 1;
      {
        const unsigned xa = OFF_XA + (band * 8 + (j >> 2)) * XA_T + ((j & 3) * 8 + (lane & 7)) * 16 + (hx * 40 + (lane >> 3)) * 512;
        unsigned la = STG_X + hx * 5120 + (lane & 7) * 128 + (lane >> 3) * 16;
        unsigned sa = OFF_ST + (band * 8 + (j >> 2)) * 4096 + st_line(hx * 8 + (lane >> 3), (j >> 1) & 1) + (lane & 7) * 16 + (j & 1) * 8;
        unsigned fa = lane < 32 ? OFF_IN + (band * 32 + lane) * 512 + 256 + (j * 2 + hx) * 4 : OOR;
#pragma unroll 1
        for (int pass = DRYPASS ? 0 : 1; pass < 2; ++pass) {
          const unsigned off = pass ? 0u : OOR;
          if (pass) DRAIN();
          if (wave == 1) HSTAMP(1, 24);
          v4u xv[5];
#pragma unroll
          for (int i = 0; i < 5; ++i) xv[i] = lds128(la + i * 1024);
#pragma unroll
          for (int i = 0; i < 5; ++i) gst128(xv[i], ws, xa + off + i * 4096, 0);
          float s = 0.f, qq = 0.f;
#pragma unroll
          for (int i = 0; i < 5; ++i) {
            s = sum8(xv[i], s);
#pragma unroll
            for (int k = 0; k < 4; ++k) { const unsigned u = xv[i][k]; qq = dot2(u, u, qq); }
          }
          gst64(v2u{asu(s), asu(qq)}, ws, sa + off, 0);
          if (pass) lds_wait_gt(L_TAGV, 0u, bud);
          tag = ufl(lds_ld(L_TAG));
          if (pass) DRAIN();
          gst32(tag, ws, fa + off);
          if (wave == 1) HSTAMP(1, 25);
        }
      }
      lds_add1(L_CRIT);
    } else {
      for (;;) {
        const unsigned m = (unsigned)w_ballot(rmw_poll_pairs((GAS unsigned*)(wsp + OFF_IN + (band * 32 + j) * 512 + 256), lane * 8, 0xffffffffu, tag));
        lds_st(L_XRDY, m);
        if (m == 0xffffffffu) break;
        SPEND(bud);
      }
      HSTAMP(1, 26);
      lds_wait_gt(L_WQOK, 0u, bud);
      v4u pc[10];
      sfor<0, 10>([&](auto I) INL { pc[I.v] = lda<I.v>(ws, lane * 16, OFF_CSQ + j * 10240); });
      f32x4 c = __builtin_bit_cast(f32x4, pc[0]);
#pragma unroll
      for (int i = 1; i < 10; ++i) c += __builtin_bit_cast(f32x4, pc[i]);
#pragma unroll
      for (int i = 0; i < 4; ++i) c[i] = sum_halves(sum_rows16(c[i]));
      if (lane < 16) csum_rows(c, L_CQ + lane * 32);
      CBAR();
      lds_st(L_CSUM, 1u);
    }
    __builtin_amdgcn_s_setprio(PRIO_H);
    if (wave == 3) {
      lds_wait_gt(L_PROG, F2GATE, bud);
      for (;;) {
        const v4u f0 = gld128(ws, lane * 16, OFF_F2);
        const bool ok = f0[0] == tag && f0[1] == tag && f0[2] == tag && f0[3] == tag;
        if (w_all(ok)) break;
        SPEND(bud);
        __builtin_amdgcn_s_sleep(16);
        CBAR();
      }
      CBAR();
      lds_st(L_W2OK, 1u);
      if (blockIdx.x == 0) gst32(tag, ws, lane == 0 ? OFF_EP : WS_USED);
      f32x4 c = {0.f, 0.f, 0.f, 0.f};
      {
        v4u pc[10];
        sfor<0, 10>([&](auto I) INL { pc[I.v] = lda<I.v>(ws, lane * 16, OFF_CSG + j * 10240); });
#pragma unroll
        for (int i = 0; i < 10; ++i) c += __builtin_bit_cast(f32x4, pc[i]);
      }
#pragma unroll
      for (int i = 0; i < 4; ++i) c[i] = sum_halves(c[i]);
      if (lane < 24) csum_rows(c, L_CG + lane * 32);
      CBAR();
      lds_st(L_CSUM, 2u);
      if (!HOLD) return;
    }
    auto halo = [&](const int L) INL {
      if (wave == 3) return;
      if (band == 0 || (ABL & 1)) {
        if (L > 0) prog_wait<SL_F>(L * NSTAGE, bud); else lds_wait_gt(L_STG, 7u, bud);
        const v4u z = {0u, 0u, 0u, 0u};
#pragma unroll 1
        for (int sl = 0; sl < 4; ++sl) kv_write(KV_OFF + (8 * half + sl) * KV_T, lane, z, v4u{lane < 32 ? 0xf149u : 0u, 0u, 0u, 0u}, z);
        CBAR();
        lds_or1(L_RDY + L * 4, 0xfu << (8 * half));
        return;
      }
#pragma unroll 1
      for (int hf = 1; hf >= 0; --hf) {
        __builtin_amdgcn_s_setprio(PRIO_H);
        lds_wait_gt<SL_F>(L_PROG + (1 - hf) * 16, L * NSTAGE + FGATE, bud);
        if (L > 0) prog_wait(L * NSTAGE, bud); else lds_wait_gt(L_STG, 7u, bud);
        __builtin_amdgcn_s_setprio(PRIO_F);
        unsigned pend = 3u;
        GAS unsigned* fl = (GAS unsigned*)(wsp + OFF_HALOF + (((L * 8 + band - 1) * 32 + j) * 2 + hf) * 256 + half * 8);
        const unsigned gb = OFF_KVH + L * KVH_SZ + (((band - 1) * 32 + j) * 8 + 4 * hf + 2 * half) * KVH_T;
        while (pend) {
          const unsigned nw = (unsigned)w_ballot(rmw_poll_lanes(fl, lane * 4, 3u, 0u, 0u) == tag) & pend;
          __builtin_amdgcn_fence(__ATOMIC_ACQUIRE, "wavefront");
          CBAR();
          if (!nw) {
            SPEND(bud);
            continue;
          }
          pend &= ~nw;
          v4u x[2][3];
#pragma unroll
          for (int sl = 1; sl >= 0; --sl) {
            const unsigned vo = lane * 16 + ((nw >> sl & 1) ? 0u : WS_USED);
#pragma unroll
            for (int i = 0; i < 3; ++i) x[sl][i] = gld128(ws, vo + i * 1024, gb + sl * KVH_T);
          }
#pragma unroll
          for (int sl = 1; sl >= 0; --sl)
            if (nw >> sl & 1) {
              kv_write(KV_OFF + (8 * hf + 2 * half + sl) * KV_T, lane, x[sl][0], x[sl][1], x[sl][2]);
              CBAR();
              lds_or1(L_RDY + L * 4, 1u << (8 * hf + 2 * half + sl));
            }
        }
      }
      __builtin_amdgcn_s_setprio(PRIO_H);
    };
    halo(0);
    if (!HOLD) { halo(1); return; }
    v4u hold[3][10];
    lds_wait_gt<SL_F>(L_W2OK, 0u, bud);
    prog_wait<SL_F>(HGATE, bud);
    sfor<0, 30>([&](auto I) INL { hold[I.v / 10][I.v % 10] = lda<I.v % 10>(ws, lane * 16, OFF_WD + ct * 122880 + (2 + 3 * (I.v / 10) + wave) * UNIT); });
    auto restore = [&](const int L) INL {
      sfor<0, 3>([&](auto K) INL {
        const int f = 2 + 3 * K.v + wave;
        const unsigned la = ARENA + (unsigned)((0xCBA9876ED543ull >> (4 * f)) & 15ull) * UNIT + lane * 16;
        prog_wait<SL_L>(L * NSTAGE + (f < 5 ? 12 : 13) + f, bud);
        sfor<0, 10>([&](auto I) INL { *(LAS v4u*)(la + I.v * 1024) = hold[K.v][I.v]; });
        CBAR();
        lds_wait_gt(L_LANDED, L * NFILL + 25 + f - 1, bud);
        lds_st(L_LANDED, L * NFILL + 25 + f + 1);
      });
    };
    restore(0);
    halo(1);
    restore(1);
  }
}

extern "C" void kernel_launch(void* const* d_in, const int* in_sizes, int n_in, void* d_out, int out_size, void* d_ws, size_t ws_size,
                              hipStream_t stream) {
  typedef const unsigned char* cp;
  mega_kernel<<<256, 768, 0, stream>>>((unsigned char*)d_ws, d_out, (cp)d_in[0], (cp)d_in[1], (cp)d_in[2], (cp)d_in[4], (cp)d_in[5], (cp)d_in[6]);
}
```

```cpp
#include <hip/hip_runtime.h>
#include <hip/hip_bf16.h>
#include <stdint.h>
#pragma clang diagnostic ignored "-Wint-to-pointer-cast"

typedef unsigned v4u __attribute__((ext_vector_type(4)));
typedef unsigned v2u __attribute__((ext_vector_type(2)));
typedef short s16x8 __attribute__((ext_vector_type(8)));
typedef short s16x4 __attribute__((ext_vector_type(4)));
typedef float f32x16 __attribute__((ext_vector_type(16)));
typedef float f32x4 __attribute__((ext_vector_type(4)));
typedef float f32x2 __attribute__((ext_vector_type(2)));
typedef __bf16 bf2 __attribute__((ext_vector_type(2)));
#define LAS __attribute__((address_space(3)))
#define GAS __attribute__((address_space(1)))
#define DI __device__ __forceinline__
#define INL __attribute__((always_inline))
#define RLX_WG __ATOMIC_RELAXED, __HIP_MEMORY_SCOPE_WORKGROUP
#define CBAR() asm volatile("" ::: "memory")
#define SCHED() __builtin_amdgcn_sched_barrier(0)

constexpr unsigned OFF_CTR = 0;
constexpr unsigned OFF_HALOF = 7 * 64 * 1024;
constexpr unsigned OFF_IN = OFF_HALOF + 2 * 8 * 32 * 512;
constexpr unsigned OFF_F2 = OFF_IN + 256 * 512;
constexpr unsigned OFF_EP = OFF_F2 + 1024;
constexpr unsigned OFF_WQ = 1u << 20;
constexpr unsigned OFF_WO = OFF_WQ + 32 * 81920;
constexpr unsigned OFF_WG = OFF_WO + 16 * 51200;
constexpr unsigned OFF_WD = OFF_WG + 32 * 122880;
constexpr unsigned XA_T = 40960, XA_SZ = 64 * XA_T;
constexpr unsigned OFF_XA = OFF_WD + 16 * 122880;
constexpr unsigned ST_SZ = 64 * 4096;
constexpr unsigned OFF_ST = OFF_XA + 4 * XA_SZ;
constexpr unsigned OA_T = 20480, OA_SZ = 128 * OA_T;
constexpr unsigned OFF_OA = OFF_ST + 4 * ST_SZ;
constexpr unsigned HA_T = 49152, HA_SZ = 128 * HA_T;
constexpr unsigned OFF_HA = OFF_OA + 2 * OA_SZ;
constexpr unsigned KVH_T = 3072, KVH_SZ = 8 * 32 * 8 * KVH_T;
constexpr unsigned OFF_KVH = OFF_HA + 2 * HA_SZ;
constexpr unsigned OFF_CSQ = OFF_KVH + 2 * KVH_SZ;
constexpr unsigned OFF_CSG = OFF_CSQ + 32 * 40 * 256;
constexpr unsigned WS_USED = OFF_CSG + 32 * 20 * 512;
static_assert(OFF_EP + 256 <= OFF_WQ, "");

constexpr int UNIT = 10240;
constexpr int MISC = 0, ARENA = 2048;
constexpr int KV_OFF = ARENA + 8 * UNIT, KV_T = 3072;
constexpr int L_PROG = MISC, L_LANDED = MISC + 64, L_RDY = MISC + 68, L_CSUM = MISC + 76;
constexpr int L_TAG = MISC + 80, L_TAGV = MISC + 84, L_XRDY = MISC + 88, L_STG = MISC + 92, L_WQOK = MISC + 96, L_W2OK = MISC + 100, L_CRIT = MISC + 104;
constexpr int STG = ARENA + 8 * UNIT;
constexpr int STG_X = STG + 65536;
constexpr int L_CQ = MISC + 256, L_CG = MISC + 768;
constexpr int LDS_BYTES = ARENA + 15 * UNIT + 4096;
constexpr int LDEPTH = 2;
constexpr int WQ_ = 12, WG_ = 12, WO_ = 10, WD_ = 10;
constexpr int NFILL = 37, NSTAGE = 38;
constexpr unsigned SPIN_CAP = 1u << 18;

struct TabQ { static constexpr int u(int i) { return i; } };
struct TabO { static constexpr int u(int i) { return i + 1; } };
struct TabG { static constexpr int u(int i) { constexpr int t[12] = {13, 14, 0, 6, 7, 8, 9, 10, 11, 12, 1, 2}; return t[i]; } };
struct TabD { static constexpr int u(int i) { constexpr int t[12] = {3, 4, 5, 13, 14, 6, 7, 8, 9, 10, 11, 12}; return t[i]; } };

template <int V> struct IC { static constexpr int v = V; };
template <int I, int N, class F> DI void sfor(F&& f) { if constexpr (I < N) { f(IC<I>{}); sfor<I + 1, N>(f); } }
DI int fresh_lane() { int l; asm volatile("v_mbcnt_lo_u32_b32 %0, -1, 0\n\tv_mbcnt_hi_u32_b32 %0, -1, %0" : "=v"(l)); return l; }
DI int opq(int v) { asm volatile("" : "+s"(v)); return v; }
DI unsigned long long w_ballot(bool p) { return __builtin_amdgcn_ballot_w64(p); }
DI bool w_any(bool p) { return w_ballot(p) != 0ull; }
DI bool w_all(bool p) { return w_ballot(!p) == 0ull; }
DI float asf(unsigned u) { return __uint_as_float(u); }
DI unsigned asu(float f) { return __float_as_uint(f); }
DI unsigned pk(float a, float b) { f32x2 t = {a, b}; bf2 p = __builtin_convertvector(t, bf2); return __builtin_bit_cast(unsigned, p); }
DI float rbf(float a) { return asf(pk(a, a) << 16); }
DI float ex2(float a) { return __builtin_amdgcn_exp2f(a); }
DI float dot2(unsigned a, unsigned b, float c) { return __builtin_amdgcn_fdot2_f32_bf16(__builtin_bit_cast(bf2, a), __builtin_bit_cast(bf2, b), c, false); }
DI f32x16 mfma32(v4u a, v4u b, f32x16 c) { return __builtin_amdgcn_mfma_f32_32x32x16_bf16(__builtin_bit_cast(s16x8, a), __builtin_bit_cast(s16x8, b), c, 0, 0, 0); }
DI f32x16 mfma32k8(v2u a, v2u b, f32x16 c) { return __builtin_amdgcn_mfma_f32_32x32x8bf16_1k(__builtin_bit_cast(s16x4, a), __builtin_bit_cast(s16x4, b), c, 0, 0, 0); }
DI f32x4 mfma16(v4u a, v4u b, f32x4 c) { return __builtin_amdgcn_mfma_f32_16x16x32_bf16(__builtin_bit_cast(s16x8, a), __builtin_bit_cast(s16x8, b), c, 0, 0, 0); }
DI void mfma16i(f32x4& c, v4u a, v4u b) { asm volatile("v_mfma_f32_16x16x32_bf16 %0, %1, %2, %0" : "+v"(c) : "v"(a), "v"(b)); }
DI void mfma16z(f32x4& c, v4u a, v4u b) { asm volatile("v_mfma_f32_16x16x32_bf16 %0, %1, %2, 0" : "=&v"(c) : "v"(a), "v"(b)); }
DI void mfma_fence(f32x4& a, f32x4& b, f32x4& c) { asm volatile("s_nop 7" : "+v"(a), "+v"(b), "+v"(c)); }
DI unsigned ufl(unsigned v) { return __builtin_amdgcn_readfirstlane(v); }
DI unsigned lds_ld(unsigned a) { return __hip_atomic_load((LAS unsigned*)a, RLX_WG); }
DI void lds_st(unsigned a, unsigned v) { __hip_atomic_store((LAS unsigned*)a, v, RLX_WG); }
DI void lds_or1(unsigned a, unsigned v) { asm volatile("s_mov_b64 exec, 1\n\tds_or_b32 %0, %1\n\ts_mov_b64 exec, -1" :: "v"(a), "v"(v) : "memory"); }
DI void lds_max1(unsigned a, unsigned v) { asm volatile("s_mov_b64 exec, 1\n\tds_max_u32 %0, %1\n\ts_mov_b64 exec, -1" :: "v"(a), "v"(v) : "memory"); }
DI void keep4(v4u& x) { asm volatile("" : "+v"(x)); }
DI v4u lds128(unsigned a) { return *(LAS v4u*)a; }
DI v2u lds64(unsigned a) { return *(LAS v2u*)a; }
DI v2u ldstr(unsigned a) { return __builtin_bit_cast(v2u, __builtin_amdgcn_ds_read_tr16_b64_v4i16((LAS s16x4*)a)); }
DI float sum_halves(float x) { auto r = __builtin_amdgcn_permlane32_swap(asu(x), asu(x), false, false); unsigned a = r[0], b = r[1]; return asf(a) + asf(b); }
DI float max_halves(float x) { auto r = __builtin_amdgcn_permlane32_swap(asu(x), asu(x), false, false); unsigned a = r[0], b = r[1]; return fmaxf(asf(a), asf(b)); }
DI float sum_rows16(float x) { auto r = __builtin_amdgcn_permlane16_swap(asu(x), asu(x), false, false); unsigned a = r[0], b = r[1]; return asf(a) + asf(b); }
DI float ror8(float x) { return asf(__builtin_amdgcn_update_dpp(0u, asu(x), 0x128, 0xf, 0xf, false)); }
DI unsigned st_line(int c, int hf) { return ((c >> 2) * 8 + ((c >> 1) & 1) * 4 + hf * 2 + (c & 1)) * 128; }
DI float max3(float a, float b, float c) { return __builtin_elementwise_maximum(__builtin_elementwise_maximum(a, b), c); }

typedef __amdgpu_buffer_rsrc_t rsrc_t;
constexpr int SC1 = 16;
DI v4u gld128(rsrc_t r, unsigned v, unsigned s) { return __builtin_amdgcn_raw_buffer_load_b128(r, v, s, SC1); }
DI v2u gld64(rsrc_t r, unsigned v, unsigned s) { return __builtin_amdgcn_raw_buffer_load_b64(r, v, s, SC1); }
DI void gst128(v4u x, rsrc_t r, unsigned v, unsigned s) { __builtin_amdgcn_raw_buffer_store_b128(x, r, v + s, 0, SC1); }
DI void gst64(v2u x, rsrc_t r, unsigned v, unsigned s) { __builtin_amdgcn_raw_buffer_store_b64(x, r, v, s, SC1); }
template <int S> DI v4u lda(rsrc_t r, unsigned voff, unsigned base) { return gld128(r, voff + (S & 3) * 1024, base + (S >> 2) * 4096); }

#define SPEND(bud) { if ((bud) >= SPIN_CAP) break; ++(bud); }
DI unsigned lds_wait_gt(unsigned a, unsigned f, unsigned& bud) {
  unsigned v;
  for (;;) {
    v = lds_ld(a);
    if (ufl(v) > f) break;
    SPEND(bud);
    __builtin_amdgcn_s_sleep(1);
  }
  CBAR();
  return v;
}
DI void lds_wait_bits(unsigned a, unsigned bits, unsigned& cache, unsigned& bud) {
  if (__builtin_expect((cache & bits) == bits, 1)) return;
  for (;;) {
    cache = ufl(lds_ld(a));
    if ((cache & bits) == bits) break;
    SPEND(bud);
    __builtin_amdgcn_s_sleep(1);
  }
  CBAR();
}
DI void prog_wait(unsigned need, unsigned& bud) {
  const int lane = fresh_lane();
  for (;;) {
    unsigned v = lds_ld(L_PROG + (lane & 7) * 4);
    if (w_all(v >= need)) break;
    SPEND(bud);
    __builtin_amdgcn_s_sleep(1);
  }
  CBAR();
}
DI unsigned rmw_poll(GAS unsigned* p) {
  unsigned f;
  asm volatile("s_mov_b64 exec, 1\n\ts_nop 3\n\tglobal_atomic_or %0, %1, %1, %2 sc0\n\ts_waitcnt vmcnt(0)\n\ts_mov_b64 exec, -1" : "=&v"(f) : "v"(0u), "s"(p) : "memory");
  return ufl(f);
}
DI unsigned rmw_poll_lanes(GAS unsigned* p, unsigned voff, unsigned mlo, unsigned mhi, unsigned init) {
  asm volatile("s_mov_b32 exec_lo, %3\n\ts_mov_b32 exec_hi, %4\n\ts_nop 3\n\tglobal_atomic_or %0, %1, %5, %2 sc0\n\ts_waitcnt vmcnt(0)\n\ts_mov_b64 exec, -1"
               : "+v"(init) : "v"(voff), "s"(p), "s"(mlo), "s"(mhi), "v"(0u) : "memory");
  return init;
}
DI bool rmw_poll_pairs(GAS unsigned* p, unsigned voff, unsigned mlo, unsigned tag) {
  v2u f = {0u, 0u};
  asm volatile("s_mov_b32 exec_lo, %3\n\ts_mov_b32 exec_hi, 0\n\ts_nop 3\n\tglobal_atomic_or_x2 %0, %1, %4, %2 sc0\n\ts_waitcnt vmcnt(0)\n\ts_mov_b64 exec, -1"
               : "+v"(f) : "v"(voff), "s"(p), "s"(mlo), "v"(v2u{0u, 0u}) : "memory");
  return f[0] == tag && f[1] == tag;
}
DI void gst32(unsigned x, rsrc_t r, unsigned v) { __builtin_amdgcn_raw_buffer_store_b32(x, r, v, 0, SC1); }
#define DRAIN() asm volatile("s_waitcnt vmcnt(0)" ::: "memory")
#define WG_BARRIER() do { asm volatile("s_waitcnt lgkmcnt(0)" ::: "memory"); __builtin_amdgcn_s_barrier(); CBAR(); } while (0)
DI void ctr_wait(unsigned char* ws, int kind, int t32, int rep, unsigned& bud) {
  GAS unsigned* p = (GAS unsigned*)(ws + OFF_CTR + (kind * 64 + t32) * 1024 + rep * 256);
  for (;;) {
    if (rmw_poll(p) >= 32u) break;
    SPEND(bud);
  }
  __builtin_amdgcn_fence(__ATOMIC_ACQUIRE, "wavefront");
  CBAR();
}
DI void ctr_arrive(unsigned char* ws, int kind, int t32) {
  const int lane = fresh_lane();
  GAS unsigned* p = (GAS unsigned*)(ws + OFF_CTR + (kind * 64 + t32) * 1024);
  DRAIN();
  asm volatile("s_mov_b64 exec, 15\n\ts_nop 3\n\tglobal_atomic_add %0, %1, %2\n\ts_mov_b64 exec, -1" :: "v"(lane * 256), "v"(1u), "s"(p) : "memory");
}

DI void dma_fill(v4u srd, unsigned voff, unsigned soff, unsigned ldst) {
  unsigned keep, so;
  asm volatile(
      "s_mov_b32 %0, m0\n\ts_mov_b32 m0, %5\n\ts_add_u32 %1, %4, 0x1000\n\ts_nop 1\n\t"
      "buffer_load_dwordx4 %2, %3, %4 offen sc1 lds\n\tbuffer_load_dwordx4 %2, %3, %4 offen offset:1024 sc1 lds\n\t"
      "buffer_load_dwordx4 %2, %3, %4 offen offset:2048 sc1 lds\n\tbuffer_load_dwordx4 %2, %3, %4 offen offset:3072 sc1 lds\n\t"
      "s_add_u32 m0, m0, 0x1000\n\ts_nop 0\n\t"
      "buffer_load_dwordx4 %2, %3, %1 offen sc1 lds\n\tbuffer_load_dwordx4 %2, %3, %1 offen offset:1024 sc1 lds\n\t"
      "buffer_load_dwordx4 %2, %3, %1 offen offset:2048 sc1 lds\n\tbuffer_load_dwordx4 %2, %3, %1 offen offset:3072 sc1 lds\n\t"
      "s_add_u32 m0, m0, 0x1000\n\ts_add_u32 %1, %1, 0x1000\n\t"
      "buffer_load_dwordx4 %2, %3, %1 offen sc1 lds\n\tbuffer_load_dwordx4 %2, %3, %1 offen offset:1024 sc1 lds\n\t"
      "s_mov_b32 m0, %0"
      : "=&s"(keep), "=&s"(so) : "v"(voff), "s"(srd), "s"(soff), "s"(ldst) : "memory", "scc");
}

DI void dma_raw(const unsigned char* base, unsigned bytes, unsigned voff, unsigned soff, unsigned sstep, unsigned ldst, int n) {
  const v4u srd = {(unsigned)(uintptr_t)base, (unsigned)((uintptr_t)base >> 32) & 0xffffu, bytes, 0x00020000u};
#pragma unroll 1
  for (int i = 0; i < n; ++i) {
    unsigned keep;
    asm volatile("s_mov_b32 %0, m0\n\ts_mov_b32 m0, %4\n\ts_nop 2\n\tbuffer_load_dwordx4 %1, %2, %3 offen lds\n\ts_mov_b32 m0, %0"
                 : "=&s"(keep) : "v"(voff), "s"(srd), "s"(soff + i * sstep), "s"(ldst + i * 1024) : "memory");
  }
}
DI unsigned lds_inc1(unsigned a) {
  unsigned r;
  asm volatile("s_mov_b64 exec, 1\n\tds_add_rtn_u32 %0, %1, %2\n\ts_waitcnt lgkmcnt(0)\n\ts_mov_b64 exec, -1" : "=&v"(r) : "v"(a), "v"(1u) : "memory");
  return ufl(r);
}
DI void lds_add1(unsigned a) { asm volatile("s_mov_b64 exec, 1\n\tds_add_u32 %0, %1\n\ts_mov_b64 exec, -1" :: "v"(a), "v"(1u) : "memory"); }

struct Ring {
  unsigned lv, lvs;
  unsigned lf, ls;
  unsigned prog;
  unsigned bud;
};
DI void ring_check(Ring& r, int f) {
  if (__builtin_expect(r.lvs <= r.lf + f, 0)) lds_wait_gt(L_LANDED, r.lf + f, r.bud);
  CBAR();
}
DI void ring_latch(Ring& r) { r.lvs = opq(ufl(r.lv)); }
DI void ring_peek(Ring& r) { r.lv = lds_ld(L_LANDED); }
constexpr int BSTEP = 61440;

template <int NB, int W, int DIST, int F0, int S0, class Tab, int CS, class Wait, class Hook>
DI void gemm32(f32x16 (&acc)[NB], float& rs, Ring& rg, rsrc_t ws, unsigned abase, unsigned stbase, Wait&& wait, Hook&& hook) {
  const int lane = fresh_lane();
  const unsigned voff = lane * 16;
  v4u a[W], b[DIST + 1];
  unsigned lb[3];
#pragma unroll
  for (int i = 0; i < 3; ++i) { lb[i] = voff + i * BSTEP; asm volatile("" : "+v"(lb[i])); }
  unsigned pa = rg.prog, pv = rg.ls + S0;
  asm volatile("" : "+v"(pa), "+v"(pv));
  v2u cf[NB], xm;
  v4u st[4];
  float s_ = 0.f, q_ = 0.f, s1_ = 0.f, q1_ = 0.f;
  unsigned csv = 0;
  auto readB = [&](auto P) INL {
    constexpr int ph = P.v;
    if constexpr (ph < 40 * NB) {
      constexpr int ad = ARENA + Tab::u(ph / 10) * UNIT + (ph % 10) * 1024;
      b[ph % (DIST + 1)] = lds128(lb[ad / BSTEP] + ad % BSTEP);
    }
    if constexpr (ph % 10 == 3 && ph + 7 < 40 * NB) ring_peek(rg);
    if constexpr (ph % 10 == 7 && ph + 3 < 40 * NB) ring_latch(rg);
    if constexpr (ph % 10 == 9 && ph + 1 < 40 * NB) ring_check(rg, F0 + ph / 10 + 1);
    if constexpr (ph % 10 == 0 && ph > 0 && ph <= 40 * NB) { CBAR(); ++pv; lds_st(pa, pv); CBAR(); }
  };
  wait();
  constexpr int W0 = 4;
  sfor<0, W0>([&](auto S) INL { a[S.v] = lda<S.v>(ws, voff, abase); });
  SCHED();
  ring_peek(rg);
  ring_latch(rg);
  ring_check(rg, F0);
  sfor<0, DIST>(readB);
  SCHED();
  constexpr int X1 = 38 * NB - 12;
  constexpr int X0 = X1 - 28;
  sfor<0, 40 * NB>([&](auto SL) INL {
    constexpr int sl = SL.v, s = sl / NB, t = sl % NB;
    if constexpr (s == 0) { f32x16 z; _Pragma("unroll") for (int r = 0; r < 16; ++r) z[r] = 0.f; acc[t] = mfma32(b[sl % (DIST + 1)], a[0], z); }
    else acc[t] = mfma32(b[sl % (DIST + 1)], a[s % W], acc[t]);
    SCHED();
    readB(IC<sl + DIST>{});
    if constexpr (s < W - W0) {
      if constexpr (t == 0) a[(W0 + 2 * s) % W] = lda<W0 + 2 * s>(ws, voff, abase);
      if constexpr (t == NB - 1) a[(W0 + 2 * s + 1) % W] = lda<W0 + 2 * s + 1>(ws, voff, abase);
    } else if constexpr (t == NB - 1 && s + W < 40) a[s % W] = lda<s + W>(ws, voff, abase);
    if constexpr (sl >= X0 && sl < X0 + 8 && (sl - X0) % 2 == 0) st[(sl - X0) / 2] = lda<(sl - X0) / 2>(ws, voff, stbase);
    if constexpr (sl == X1 - 10) csv = lds_ld(L_CSUM);
    if constexpr (sl == X1 - 6) csv = opq(ufl(csv));
    if constexpr (sl == X1 - 3) { if (__builtin_expect(ufl(csv) < NB - 1, 0)) lds_wait_gt(L_CSUM, NB - 2, rg.bud); CBAR(); }
    if constexpr (sl >= X1 && sl < X1 + 3) {
      constexpr int i = sl - X1 + 1;
      const unsigned u0 = st[i][0], u1 = st[i][1], u2 = st[i][2], u3 = st[i][3];
      if constexpr (i == 1) { const unsigned w0 = st[0][0], w1 = st[0][1], w2 = st[0][2], w3 = st[0][3]; s_ = asf(w0); q_ = asf(w1); s1_ = asf(w2); q1_ = asf(w3); }
      s_ += asf(u0); q_ += asf(u1); s1_ += asf(u2); q1_ += asf(u3);
    }
    if constexpr (sl == X1 + 3) { const bool up = lane & 8; s_ = (up ? s1_ : s_) + ror8(up ? s_ : s1_); }
    if constexpr (sl == X1 + 4) { const bool up = lane & 8; q_ = (up ? q1_ : q_) + ror8(up ? q_ : q1_); }
    if constexpr (sl == X1 + 5) s_ = sum_halves(s_) * (1.f / 640.f);
    if constexpr (sl == X1 + 6) q_ = sum_halves(q_) * (1.f / 640.f);
    if constexpr (sl == X1 + 7) {
      q_ = fmaxf(q_ - s_ * s_, 0.f);
      const unsigned nm = pk(-s_, -s_);
      xm[0] = lane < 32 ? nm : 0u; xm[1] = lane < 32 ? (nm & 0xffffu) : 0u;
    }
    if constexpr (sl == X1 + 8) {
      q_ = rbf(rbf(q_) + 1e-5f);
      sfor<0, NB>([&](auto T) INL { cf[T.v] = lds64(CS + T.v * 256 + (lane & 31) * 8); });
    }
    if constexpr (sl == X1 + 9) rs = __builtin_amdgcn_rcpf(rbf(__builtin_amdgcn_sqrtf(q_)));
    if constexpr (sl == X1 + 10) hook();
    SCHED();
    if constexpr (s == 38) { acc[t] = mfma32k8(cf[t], xm, acc[t]); SCHED(); }
  });
}

template <int W, int DIST, int F0, int S0, class Tab, int CS, class Wait>
DI void gemmG(f32x4 (&acc)[6][2], float (&rs)[2], float (&nm)[2], v4u (&cf)[6], Ring& rg, rsrc_t ws, unsigned abase, unsigned stbase, Wait&& wait) {
  constexpr int NG = 60, R = DIST + 1;
  const int lane = fresh_lane();
  const unsigned voff = lane * 16;
  v4u a[W], b[R][2];
  unsigned lb[3];
#pragma unroll
  for (int i = 0; i < 3; ++i) { lb[i] = voff + i * BSTEP; asm volatile("" : "+v"(lb[i])); }
  unsigned pa = rg.prog, pv = rg.ls + S0;
  asm volatile("" : "+v"(pa), "+v"(pv));
  v4u st[4];
  float s_ = 0.f, q_ = 0.f, s1_ = 0.f, q1_ = 0.f, t_ = 0.f;
  unsigned csv = 0;
  auto readB = [&](auto G, auto H) INL {
    constexpr int g = G.v, ph = 2 * g + H.v;
    if constexpr (g < NG) {
      constexpr int ad = ARENA + Tab::u(ph / 10) * UNIT + (ph % 10) * 1024;
      b[g % R][H.v] = lds128(lb[ad / BSTEP] + ad % BSTEP);
    }
  };
  wait();
  constexpr int W0 = 6;
  static_assert(W % 2 == 0 && W >= W0 && DIST <= 3, "");
  sfor<0, W0>([&](auto S) INL { a[S.v] = lda<S.v>(ws, voff, abase); });
  SCHED();
  ring_peek(rg);
  ring_latch(rg);
  ring_check(rg, F0 + 1);
  sfor<0, 2 * DIST>([&](auto I) INL { readB(IC<I.v / 2>{}, IC<I.v % 2>{}); });
  SCHED();
  constexpr int X1 = 41, X0 = X1 - 20;
  sfor<0, NG>([&](auto G) INL {
    constexpr int g = G.v, s = g / 3, k = g % 3, gr = g + DIST, u = gr % 5;
    auto mm = [&](auto H, auto T) INL {
      if constexpr (s == 0) mfma16z(acc[2 * k + H.v][T.v], b[g % R][H.v], a[T.v]); else mfma16i(acc[2 * k + H.v][T.v], b[g % R][H.v], a[(2 * s + T.v) % W]);
      SCHED();
    };
    mm(IC<1>{}, IC<1>{});
    readB(IC<gr>{}, IC<0>{});
    if constexpr (gr % 10 == 8 && gr + 2 < NG) ring_latch(rg);
    if constexpr (u == 0 && gr <= NG) { CBAR(); ++pv; }
    if constexpr (g == X1 - 4) csv = opq(ufl(csv));
    SCHED();
    mm(IC<1>{}, IC<0>{});
    readB(IC<gr>{}, IC<1>{});
    if constexpr (u == 0 && gr <= NG) { lds_st(pa, pv); CBAR(); }
    if constexpr (gr % 10 == 6 && gr + 4 < NG) ring_peek(rg);
    if constexpr (g == X1 - 7) csv = lds_ld(L_CSUM);
    SCHED();
    mm(IC<0>{}, IC<1>{});
    if constexpr (2 * g < W - W0) a[W0 + 2 * g] = lda<W0 + 2 * g>(ws, voff, abase);
    if constexpr (s > 0 && k < 2 && 2 * (s - 1) + W + k < 40) a[(2 * (s - 1) + k) % W] = lda<2 * (s - 1) + W + k>(ws, voff, abase);
    if constexpr (g >= X0 && g < X0 + 12 && k == 2) st[(g - X0) / 3] = lda<(g - X0) / 3>(ws, voff, stbase);
    if constexpr (g >= X1 && g < X1 + 3) {
      constexpr int i = g - X1 + 1;
      const unsigned u0 = st[i][0], u1 = st[i][1];
      if constexpr (i == 1) { const unsigned w0 = st[0][0], w1 = st[0][1]; s_ = asf(w0); q_ = asf(w1); }
      s_ += asf(u0); q_ += asf(u1);
    }
    if constexpr (g == X1 + 3) { const bool up = lane & 8; t_ = up ? s_ : s1_; s_ = up ? s1_ : s_; }
    if constexpr (g == X1 + 4) { const bool up = lane & 8; t_ = up ? q_ : q1_; q_ = up ? q1_ : q_; }
    if constexpr (g == X1 + 5) s_ = sum_halves(s_);
    if constexpr (g == X1 + 6) q_ = sum_halves(q_);
    if constexpr (g == X1 + 7) { q_ = fmaxf(q_ * (1.f / 640.f) - s_ * s_, 0.f); }
    if constexpr (g == X1 + 8) s_ = rbf(-s_);
    if constexpr (g == X1 + 9) { auto r = __builtin_amdgcn_permlane16_swap(asu(s_), asu(s_), false, false); const unsigned r0 = r[0], r1 = r[1]; nm[0] = asf(r0); nm[1] = asf(r1); }
    if constexpr (g == X1 + 10) q_ = rbf(q_ + 1e-5f);
    if constexpr (g == X1 + 11) q_ = rbf(q_);
    if constexpr (g == X1 + 12) { auto r = __builtin_amdgcn_permlane16_swap(asu(q_), asu(q_), false, false); const unsigned r0 = r[0], r1 = r[1]; rs[0] = asf(r0); rs[1] = asf(r1); }
    SCHED();
    mm(IC<0>{}, IC<0>{});
    if constexpr (2 * g + 1 < W - W0) a[W0 + 2 * g + 1] = lda<W0 + 2 * g + 1>(ws, voff, abase);
    if constexpr (gr % 10 == 9 && gr + 1 < NG) ring_check(rg, F0 + gr / 5 + 2);
    if constexpr (g == X1 - 2) { if (__builtin_expect(ufl(csv) < 2, 0)) lds_wait_gt(L_CSUM, 1, rg.bud); CBAR(); }
    if constexpr (g >= X1 && g < X1 + 3) {
      constexpr int i = g - X1 + 1;
      const unsigned u2 = st[i][2], u3 = st[i][3];
      if constexpr (i == 1) { const unsigned w2 = st[0][2], w3 = st[0][3]; s1_ = asf(w2); q1_ = asf(w3); }
      s1_ += asf(u2); q1_ += asf(u3);
    }
    if constexpr (g == X1 + 3) s_ += ror8(t_);
    if constexpr (g == X1 + 4) q_ += ror8(t_);
    if constexpr (g == X1 + 5) s_ *= 1.f / 640.f;
    if constexpr (g == X1 + 7) q_ = rbf(q_);
    if constexpr (g >= NG - 7 && g < NG - 1) cf[g - (NG - 7)] = lds128(CS + (g - (NG - 7)) * 64 + (lane >> 4) * 16);
    if constexpr (g == X1 + 10) q_ = __builtin_amdgcn_sqrtf(q_);
    if constexpr (g == X1 + 11) q_ = __builtin_amdgcn_rcpf(q_);
    SCHED();
  });
  mfma_fence(acc[4][0], acc[4][1], acc[5][0]);
  SCHED();
}

template <int NS, int W, int DIST, int F0, int S0, class Tab, class Wait>
DI void gemm16(Ring& rg, rsrc_t ws, unsigned abase, unsigned (&res)[6], unsigned xbase, unsigned stbase, bool last, void* out, int t16i, int ct, Wait&& wait) {
  const int lane = fresh_lane();
  const unsigned voff = lane * 16;
  v4u a[W], b[DIST + 1][3];
  unsigned lb[3], lc[3];
  const int g4 = lane >> 4;
#pragma unroll
  for (int i = 0; i < 3; ++i) {
    lb[i] = voff + i * BSTEP; asm volatile("" : "+v"(lb[i]));
    lc[i] = g4 * 128 + (lane & 7) * 16 + i * BSTEP + ((lane & 8) ? 0x40000 : 0); asm volatile("" : "+v"(lc[i]));
  }
  unsigned pa = rg.prog, pv = rg.ls + S0;
  asm volatile("" : "+v"(pa), "+v"(pv));
  f32x4 acc[3];
#pragma unroll
  for (int t = 0; t < 3; ++t) acc[t] = f32x4{asf(res[2 * t] << 16), asf(res[2 * t] & 0xffff0000u), asf(res[2 * t + 1] << 16), asf(res[2 * t + 1] & 0xffff0000u)};
  auto readB = [&](auto S, auto T) INL {
    constexpr int s = S.v, t = T.v;
    if constexpr (s < NS) {
      constexpr int ad = ARENA + Tab::u(s / 4) * UNIT + (s % 4) * 2560;
      b[s % (DIST + 1)][t] = lds128((t < 2 ? lb : lc)[ad / BSTEP] + ad % BSTEP + t * 1024);
    }
    if constexpr (t == 0 && s % 4 == 1 && s + 3 < NS) ring_peek(rg);
    if constexpr (t == 0 && s % 4 == 3 && s + 1 < NS) ring_latch(rg);
    if constexpr (t == 2 && s % 4 == 3 && s + 1 < NS) ring_check(rg, F0 + s / 4 + 1);
    if constexpr (t == 0 && s % 4 == 0 && s > 0 && s <= NS) { CBAR(); ++pv; lds_st(pa, pv); CBAR(); }
  };
  const unsigned t32 = (t16i & 1) * 16 + (lane & 15);
  const unsigned pi = 2 * (g4 & 1) + (g4 >> 1);
  unsigned so1, so2;
  if constexpr (NS == 20) {
    const unsigned p1 = 5 * ct + pi, p2 = 5 * ct + 4, tb = xbase + (t16i & 1) * 1024 + (lane & 15) * 16;
    so1 = tb + (p1 >> 2) * 2048 + (p1 & 3) * 256; so2 = tb + (p2 >> 2) * 2048 + (p2 & 3) * 256 + (lane < 16 ? 0u : WS_USED);
  } else {
    so1 = xbase + (5 * ct) * 512 + pi * 512 + t32 * 16; so2 = xbase + (5 * ct + 4) * 512 + t32 * 16 + (lane < 16 ? 0u : WS_USED);
  }
  unsigned so3 = stbase + st_line(ct, t16i & 1) + (lane & 7) * 16 + (lane & 8) + (lane < 16 ? 0u : WS_USED);
  asm volatile("" : "+v"(so1), "+v"(so2), "+v"(so3), "+v"(acc[0]), "+v"(acc[1]), "+v"(acc[2]));
  wait();
  sfor<0, W>([&](auto S) INL { a[S.v] = lda<S.v>(ws, voff, abase); });
  SCHED();
  ring_peek(rg);
  ring_latch(rg);
  ring_check(rg, F0);
  sfor<0, DIST * 3>([&](auto I) INL { readB(IC<I.v / 3>{}, IC<I.v % 3>{}); });
  SCHED();
  sfor<0, NS>([&](auto S) INL {
    constexpr int s = S.v;
    sfor<0, 3>([&](auto T) INL {
      constexpr int t = 2 - T.v;
      mfma16i(acc[t], b[s % (DIST + 1)][t], a[s % W]);
      SCHED();
      readB(IC<s + DIST>{}, T);
      if constexpr (T.v == 2 && s + W < NS) a[s % W] = lda<s + W>(ws, voff, abase);
      SCHED();
    });
  });
  mfma_fence(acc[0], acc[1], acc[2]);
#pragma unroll
  for (int t = 0; t < 3; ++t) { res[2 * t] = pk(acc[t][0], acc[t][1]); res[2 * t + 1] = pk(acc[t][2], acc[t][3]); }
  auto s0 = __builtin_amdgcn_permlane16_swap(res[0], res[2], false, false);
  auto s1 = __builtin_amdgcn_permlane16_swap(res[1], res[3], false, false);
  auto s2 = __builtin_amdgcn_permlane16_swap(res[4], res[4], false, false);
  auto s3 = __builtin_amdgcn_permlane16_swap(res[5], res[5], false, false);
  const v4u o01 = {s0[0], s1[0], s0[1], s1[1]};
  const v4u o2 = {s2[0], s3[0], s2[1], s3[1]};
  if (last) {
    GAS char* ob = (GAS char*)out + (size_t)(t16i * 16 + (lane & 15)) * 1280 + ct * 80;
    *(GAS v4u*)(ob + pi * 16) = o01;
    if (lane < 16) *(GAS v4u*)(ob + 64) = o2;
  } else {
    gst128(o01, ws, so1, 0);
    gst128(o2, ws, so2, 0);
    const f32x4 sv = acc[0] + acc[1] + acc[2], qv = acc[0] * acc[0] + acc[1] * acc[1] + acc[2] * acc[2];
    float s = (sv[0] + sv[1]) + (sv[2] + sv[3]), q = (qv[0] + qv[1]) + (qv[2] + qv[3]);
    s = sum_halves(sum_rows16(s));
    q = sum_halves(sum_rows16(q));
    gst64(v2u{asu(s), asu(q)}, ws, so3, 0);
  }
}

constexpr float QSCALE = 0.22360679774997896f * 1.4426950408889634f;
constexpr float NEG = -1e30f;

DI float max16(const f32x16& s) {
  const float a = max3(s[0], s[1], s[2]), b = max3(s[3], s[4], s[5]), c = max3(s[6], s[7], s[8]);
  const float d = max3(s[9], s[10], s[11]), e = max3(s[12], s[13], s[14]);
  return __builtin_elementwise_maximum(max3(a, b, c), max3(d, e, s[15]));
}
DI void kv_write(unsigned kb, int lane, v4u k1, v4u ta, v4u tb) {
  *(LAS v4u*)(kb + lane * 16) = k1;
  *(LAS v2u*)(kb + 1024 + (lane ^ 32) * 8) = v2u{ta[0], ta[1]};
  const unsigned vb = kb + 1536 + (lane & 31) * 48 + (lane >> 5) * 8;
  *(LAS v2u*)(vb) = v2u{ta[2], ta[3]};
  *(LAS v2u*)(vb + 16) = v2u{tb[0], tb[1]};
  *(LAS v2u*)(vb + 32) = v2u{tb[2], tb[3]};
}

DI int kmapO4(int P, int t) {
  const int p = P / 5, r = P - 5 * p;
  if (r == 2) return 20 * (2 * p + t) + 16;
  return 20 * (2 * p + (r > 2)) + 8 * t + 4 * (r > 2 ? r - 3 : r);
}
DI int kmapD4(int P, int t) {
  const int jj = P / 6, r = P - 6 * jj;
  return 48 * jj + (r < 4 ? 16 * t + 4 * r : 32 + 8 * (r - 4) + 4 * t);
}
DI v4u tr_frag(unsigned a0, unsigned a1) { const v2u x = ldstr(a0), y = ldstr(a1); return v4u{x[0], x[1], y[0], y[1]}; }
DI float sum8(v4u f, float s) {
#pragma unroll
  for (int i = 0; i < 4; ++i) { const unsigned u = f[i]; s = dot2(u, 0x3f803f80u, s); }
  return s;
}

DI void csum_rows(f32x4 c, unsigned la) {
  unsigned o[8];
#pragma unroll
  for (int i = 0; i < 4; ++i) {
    const float v = c[i], hi = rbf(v), mid = rbf(v - hi), lo = rbf(v - hi - mid);
    o[2 * i] = pk(hi, mid); o[2 * i + 1] = pk(lo, 0.f);
  }
  *(LAS v4u*)la = v4u{o[0], o[1], o[2], o[3]};
  *(LAS v4u*)(la + 16) = v4u{o[4], o[5], o[6], o[7]};
}

#if defined(__HIP_DEVICE_COMPILE__)
#define NO_PK __attribute__((target("no-packed-fp32-ops")))
#else
#define NO_PK
#endif
__global__ void NO_PK __launch_bounds__(768, 3) mega_kernel(unsigned char* wsp, void* out, const unsigned char* xin, const unsigned char* wqkv, const unsigned char* wo,
                                                   const unsigned char* wg, const unsigned char* wu, const unsigned char* wd) {
  __shared__ __attribute__((aligned(16))) char smem[LDS_BYTES];
  const int wave = ufl(threadIdx.x >> 6);
  asm volatile("" :: "v"((LAS char*)smem) : "memory");
  const int bx = blockIdx.x & 7, br = blockIdx.x >> 3;
  const int band = (bx & 4) + (br >> 3), j = (bx & 3) * 8 + (br & 7);
  const int ct = (j >> 4) * 8 + (j & 7), rsel = (j >> 3) & 1;
  const rsrc_t ws = __builtin_amdgcn_make_buffer_rsrc(wsp, 0, WS_USED, 0x00020000);
  constexpr unsigned OOR = 0x40000000u;
  const int tk = blockIdx.x * 8 + ((wave - 4) & 7);
  const unsigned sb = wave < 4 ? STG_X + (wave - 1) * 5120 : STG + (wave - 4) * 8192;
  {
    const int lane = fresh_lane();
    const bool isx = wave < 4;
    dma_raw(isx ? xin : wqkv, isx ? 2048 * 1280 : 640 * 3840, isx || (lane & 7) < 5 ? (lane >> 3) * 1280 + (lane & 7) * 16 : OOR,
            isx ? (band * 256 + j * 8) * 1280 + (wave - 1) * 640 : (tk >> 4) * (16 * 3840) + (tk & 15) * 80, isx ? 128 : 10240, sb,
            opq(isx ? (wave == 1 || wave == 2 ? 5 : 0) : tk < 640 ? 6 : 0));
  }

  if (wave == 0) {
    const int lane = fresh_lane();
    __builtin_amdgcn_s_setprio(3);
    const v4u wsd = {(unsigned)(uintptr_t)wsp, (unsigned)((uintptr_t)wsp >> 32) & 0xffffu, WS_USED, 0x00020000u};
    unsigned bud = 0;
    unsigned ep = __builtin_amdgcn_raw_buffer_load_b32(ws, 0, OFF_EP, SC1);
    lds_st(MISC + (lane & 31) * 4, 0u);
    WG_BARRIER();
    asm volatile("" : "+v"(ep));
    const unsigned tag = ufl(ep) + 1u;
    lds_st(L_TAG, tag);
    CBAR();
    lds_st(L_TAGV, 1u);
    {
      int nf = 0;
      for (;;) {
        const unsigned m = (unsigned)w_ballot(rmw_poll_pairs((GAS unsigned*)(wsp + OFF_IN + (band * 32 + j) * 512), lane * 8, 0xfffffu, tag));
        lds_st(L_LANDED, nf);
        while (nf < 8 && ((m >> (5 * (nf >> 1))) & 31u) == 31u) {
          if (nf >= 6) { asm volatile("s_waitcnt vmcnt(50)" ::: "memory"); lds_max1(L_LANDED, nf - 5); }
          dma_fill(wsd, lane * 16, OFF_WQ + j * 81920 + nf * UNIT, ARENA + nf * UNIT);
          ++nf;
        }
        if (nf == 8) break;
        SPEND(bud);
      }
    }
    lds_st(L_WQOK, 1u);
    asm volatile("s_waitcnt vmcnt(50)" ::: "memory"); lds_max1(L_LANDED, 3u);
    asm volatile("s_waitcnt vmcnt(40)" ::: "memory"); lds_max1(L_LANDED, 4u);
    asm volatile("s_waitcnt vmcnt(30)" ::: "memory"); lds_max1(L_LANDED, 5u);
    asm volatile("s_waitcnt vmcnt(20)" ::: "memory"); lds_max1(L_LANDED, 6u);
    asm volatile("s_waitcnt vmcnt(10)" ::: "memory"); lds_st(L_LANDED, 7u);
    asm volatile("s_waitcnt vmcnt(0)" ::: "memory"); lds_st(L_LANDED, 8u);
    __builtin_amdgcn_s_setprio(0);
    v4u hq[4][10];
    sfor<0, 40>([&](auto I) INL { hq[I.v / 10][I.v % 10] = lds128(ARENA + 4 * UNIT + I.v * 1024 + lane * 16); });
    sfor<0, 40>([&](auto I) INL { keep4(hq[I.v / 10][I.v % 10]); });
    lds_wait_gt(L_W2OK, 0u, bud);
    unsigned issued = 8, published = 8;
#pragma unroll 1
    for (int lp = 1; lp < 8; ++lp) {
      const int L = lp >> 2, ph = lp & 3;
      unsigned long long units, needs_lo, needs_hi; int n; unsigned src;
      if (ph == 0) { units = 0x76543210ull; n = 4; src = OFF_WQ + j * 81920;
        needs_lo = 17ull | 25ull << 8 | 26ull << 16 | 27ull << 24 | 28ull << 32 | 29ull << 40 | 32ull << 48 | 33ull << 56; needs_hi = 0; }
      else if (ph == 1) { units = 0x54321ull; n = 5; src = OFF_WO + ct * 51200;
        needs_lo = 40ull | 41ull << 8 | 42ull << 16 | 43ull << 24 | 44ull << 32; needs_hi = 0; }
      else if (ph == 2) { units = 0x21CBA98760EDull; n = 12; src = OFF_WG + j * 122880;
        needs_lo = 30ull | 31ull << 8 | 39ull << 16 | 45ull << 24 | 46ull << 32 | 47ull << 40 | 47ull << 48 | 47ull << 56;
        needs_hi = 47ull | 47ull << 8 | 48ull << 16 | 49ull << 24; }
      else { units = 0x543ull; n = 3; src = OFF_WD + ct * 122880;
        needs_lo = 50ull | 51ull << 8 | 52ull << 16; needs_hi = 0; }
#pragma unroll 1
      for (int f = 0; f < n; ++f) {
        const int unit = (int)((units >> (4 * f)) & 15ull);
        const int nd = (int)(((f < 8 ? needs_lo >> (8 * f) : needs_hi >> (8 * (f - 8)))) & 255ull) + (L - 1) * NSTAGE;
        if (nd > 0) {
          unsigned v = lds_ld(L_PROG + (lane & 7) * 4);
          if (!w_all((int)v >= nd)) {
            if (published != issued) {
              if (issued - published >= 2) { asm volatile("s_waitcnt vmcnt(10)" ::: "memory"); lds_st(L_LANDED, issued - 1); }
              asm volatile("s_waitcnt vmcnt(0)" ::: "memory");
              published = issued; lds_st(L_LANDED, published);
            }
            prog_wait((unsigned)nd, bud);
          }
        }
        dma_fill(wsd, lane * 16, src + f * UNIT, ARENA + unit * UNIT);
        ++issued;
        if (issued - published > LDEPTH) {
          asm volatile("s_waitcnt vmcnt(%0)" :: "i"(10 * LDEPTH) : "memory");
          published = issued - LDEPTH; lds_st(L_LANDED, published);
        }
      }
      if (ph == 0) {
        asm volatile("s_waitcnt vmcnt(10)" ::: "memory"); lds_st(L_LANDED, issued - 1);
        asm volatile("s_waitcnt vmcnt(0)" ::: "memory"); lds_st(L_LANDED, issued);
        sfor<0, 4>([&](auto K) INL {
          prog_wait(K.v < 2 ? 28u + K.v : 30u + K.v, bud);
          sfor<0, 10>([&](auto I) INL { *(LAS v4u*)(unsigned)(ARENA + (4 + K.v) * UNIT + I.v * 1024 + lane * 16) = hq[K.v][I.v]; });
          CBAR();
          lds_st(L_LANDED, ++issued);
        });
        published = issued;
      }
      if (ph == 3) {
        asm volatile("s_waitcnt vmcnt(10)" ::: "memory"); lds_st(L_LANDED, issued - 1);
        asm volatile("s_waitcnt vmcnt(0)" ::: "memory"); lds_st(L_LANDED, issued);
        if (L == 1) return;
        issued += 9; published = issued;
        lds_wait_gt(L_LANDED, issued - 1, bud);
      }
    }
    return;
  }

  __builtin_amdgcn_s_barrier();
  CBAR();
  if (wave >= 4) {
    const int cw = wave - 4, grp = cw >> 2, wi = cw & 3;
    const int qt = (1 - grp) * 4 + wi;
    const int t32_ = band * 8 + qt;
    const int t16_ = band * 16 + (1 - grp) * 8 + rsel * 4 + wi;
    const int rep = j & 3;
    Ring rg;
    rg.prog = L_PROG + cw * 4;
    rg.bud = 0;
    if (grp == 0) __builtin_amdgcn_s_setprio(2); else __builtin_amdgcn_s_setprio(1);
    const int dslot = qt + (qt < 4 ? 4 : 8);
    unsigned res[6] = {0u, 0u, 0u, 0u, 0u, 0u};
    unsigned tag;
    {
      const int lane = fresh_lane();
      const int gam = lane >> 4, q = (lane & 15) >> 2, p = lane & 3, hh = gam >> 1, g = (gam & 1) * 4 + p;
      const v4u ones = {0x3f803f80u, 0x3f803f80u, 0x3f803f80u, 0x3f803f80u};
      f32x16 z;
#pragma unroll
      for (int i = 0; i < 16; ++i) z[i] = 0.f;
      if (cw == 7) {
        const int ci = j * 7 + lane;
        gst32(0u, ws, lane < 7 ? OFF_CTR + ((ci >> 5) * 64 + band * 8 + ((ci >> 2) & 7)) * 1024 + (ci & 3) * 256 : OOR);
      }
      if (tk < 640) {
        const int hp = tk & 15, ks = tk >> 4;
        const unsigned r = sb + (8 * hh + q) * 384;
        unsigned c0 = r + (g < 4 ? 0 : 128) + (g & 3) * 8, c1 = r + (g == 0 ? 32 : g == 1 ? 128 + 32 : 256 + ((g < 7 ? g : 6) - 2) * 8);
        unsigned dst = OFF_WQ + hp * 163840 + ks * 2048 + lane * 16, cs = OFF_CSQ + (hp * 80 + ks) * 256 + lane * 4;
        unsigned fa = lane < 16 ? OFF_IN + ((lane >> 1) * 32 + 2 * hp + (lane & 1)) * 512 + ks * 4 : OOR;
        asm volatile("s_waitcnt vmcnt(0)" : "+v"(c0), "+v"(c1), "+v"(dst), "+v"(cs), "+v"(fa) :: "memory");
        v4u f[2][2];
#pragma unroll
        for (int h2 = 0; h2 < 2; ++h2) {
          f[h2][0] = tr_frag(c0 + h2 * 40, c0 + h2 * 40 + 1536);
          f[h2][1] = tr_frag(c1 + h2 * 40, c1 + h2 * 40 + 1536);
        }
#pragma unroll
        for (int h2 = 0; h2 < 2; ++h2) {
          if ((lane & 31) >= 28) f[h2][1] = v4u{0u, 0u, 0u, 0u};
          gst128(f[h2][0], ws, dst + h2 * 81920, 0); gst128(f[h2][1], ws, dst + h2 * 81920 + 1024, 0);
        }
#pragma unroll
        for (int h2 = 0; h2 < 2; ++h2) {
          const float s0 = mfma32(ones, f[h2][0], z)[0], s1 = mfma32(ones, f[h2][1], z)[0];
          gst32(asu(lane < 32 ? s0 : s1), ws, cs + h2 * 10240);
        }
        lds_wait_gt(L_TAGV, 0u, rg.bud);
        tag = ufl(lds_ld(L_TAG));
        DRAIN();
        gst32(tag, ws, fa);
      } else {
        const bool gu = tk < 1280, dn = tk < 1664;
        const int i = gu ? tk - 640 : dn ? tk - 1280 : tk - 1664;
        unsigned a0, a1, a2, a3, a4, dst, o1, o2;
        if (gu) {
          const unsigned vo = (lane & 7) < 6 ? (lane >> 3) * 3072 + (lane & 7) * 16 : OOR, so = (i >> 5) * (32 * 3072) + (i & 31) * 96;
          dma_raw(wg, 640 * 3072, vo, so, 8 * 3072, sb, 4);
          dma_raw(wu, 640 * 3072, vo, so, 8 * 3072, sb + 4096, 4);
          a0 = sb + (8 * gam + q) * 128 + p * 8; a1 = a0 + 32; a2 = a0 + 64; a3 = 4096; a4 = 512;
          dst = OFF_WG + (i & 31) * 122880 + (i >> 5) * 6144; o1 = 2048; o2 = 4096 + lane * 16;
        } else {
          const unsigned vo = (lane & 15) < 10 ? gam * 1280 + (lane & 15) * 16 : OOR;
          if (tk < 1824) {
#pragma unroll 1
            for (int u = 0; u < 8; ++u) {
              const int P = 4 * (i >> 3) + (u >> 1);
              const int row = dn ? kmapD4(P, u & 1) : kmapO4(P, u & 1);
              dma_raw(dn ? wd : wo, dn ? 1536 * 1280 : 640 * 1280, vo, row * 1280 + (i & 7) * 160, 0, sb + u * 1024, 1);
            }
          }
          const unsigned r = sb + gam * 2048 + q * 256;
          a0 = r + p * 8; a1 = a0 + 32; a2 = r + 64 + (p & 1) * 8; a3 = 80; a4 = 1024; o1 = 1024;
          dst = tk >= 1824 ? OOR : dn ? OFF_WD + (i & 7) * 245760 + (i >> 3) * 2560 : OFF_WO + (i & 7) * 102400 + (i >> 3) * 2560;
          o2 = (lane & 15) < 8 ? 2048 + gam * 128 + (lane & 7) * 16 : OOR;
        }
        const unsigned dstep = gu ? 1024 : dn ? 122880 : 51200;
        asm volatile("s_waitcnt vmcnt(0)" : "+v"(a0), "+v"(a1), "+v"(a2), "+v"(o2) :: "memory");
        v4u f[2][3];
#pragma unroll
        for (int h2 = 0; h2 < 2; ++h2) {
          f[h2][0] = tr_frag(a0 + h2 * a3, a0 + h2 * a3 + a4);
          f[h2][1] = tr_frag(a1 + h2 * a3, a1 + h2 * a3 + a4);
          f[h2][2] = tr_frag(a2 + h2 * a3, a2 + h2 * a3 + a4);
        }
        lds_wait_gt(L_CRIT, 1u, rg.bud);
#pragma unroll
        for (int h2 = 0; h2 < 2; ++h2) {
          gst128(f[h2][0], ws, dst + h2 * dstep + lane * 16, 0); gst128(f[h2][1], ws, dst + h2 * dstep + lane * 16 + o1, 0);
          gst128(f[h2][2], ws, dst + h2 * dstep + o2, 0);
        }
        if (gu) {
          float c[6];
#pragma unroll
          for (int k = 0; k < 6; ++k) c[k] = mfma16(ones, f[k & 1][k >> 1], f32x4{0.f, 0.f, 0.f, 0.f})[0];
          const unsigned cs = OFF_CSG + ((i & 31) * 20 + (i >> 5)) * 512 + lane * 4;
          gst32(asu(gam == 0 ? c[0] : gam == 1 ? c[1] : gam == 2 ? c[2] : c[3]), ws, cs);
          gst32(gam == 0 ? asu(c[4]) : gam == 1 ? asu(c[5]) : 0u, ws, cs + 256);
        }
        lds_wait_gt(L_TAGV, 0u, rg.bud);
        tag = ufl(lds_ld(L_TAG));
        DRAIN();
      }
      if (lds_inc1(L_STG) == 7u) gst32(tag, ws, lane == 0 ? OFF_F2 + blockIdx.x * 4 : OOR);
    }
#pragma unroll 1
    for (int L = 0; L < 2; ++L) {
      rg.lf = L * NFILL; rg.ls = L * NSTAGE;
      const int t32i = opq(t32_), t16i = opq(t16_);
      __builtin_amdgcn_s_setprio(2);
      v4u q1; v2u q2;
      {
        f32x16 acc[2]; float rs;
        gemm32<2, WQ_, 4, 0, 0, TabQ, L_CQ>(acc, rs, rg, ws, OFF_XA + (2 * L) * XA_SZ + t32i * XA_T, OFF_ST + (2 * L) * ST_SZ + t32i * 4096,
          [&]() INL {
            if (L > 0) ctr_wait(wsp, 3, t32i, rep, rg.bud);
            else { unsigned xr = 0; lds_wait_bits(L_XRDY, 0xfu << (4 * qt), xr, rg.bud); }
          },
          [&]() INL { if (L > 0) prog_wait(L * NSTAGE, rg.bud); else lds_wait_gt(L_STG, 7u, rg.bud); });
        const int lane = fresh_lane(), h = lane >> 5;
        v4u k1, ta, tb;
        const float rq = rs * QSCALE, r0 = h ? rs : rq;
#pragma unroll
        for (int i = 0; i < 4; ++i) k1[i] = pk(acc[0][8 + 2 * i] * rs, acc[0][9 + 2 * i] * rs);
        ta[0] = pk(acc[1][0] * r0, acc[1][1] * r0); ta[1] = pk(acc[1][2] * r0, acc[1][3] * r0);
        ta[2] = pk(acc[1][4] * rs, acc[1][5] * rs); ta[3] = pk(acc[1][6] * rs, acc[1][7] * rs);
#pragma unroll
        for (int i = 0; i < 4; ++i) tb[i] = pk(acc[1][8 + 2 * i] * rs, acc[1][9 + 2 * i] * rs);
        if (h) { tb[2] = 0x3f803f80u; tb[3] = 0x3f803f80u; }
        q2[0] = h ? 0x3f80u : ta[0]; q2[1] = h ? 0u : ta[1];
        if (!h) { ta[0] = 0u; ta[1] = 0u; }
        if (band < 7) {
          const unsigned gb = OFF_KVH + L * KVH_SZ + ((band * 32 + j) * 8 + qt) * KVH_T;
          gst128(k1, ws, lane * 16, gb); gst128(ta, ws, lane * 16 + 1024, gb); gst128(tb, ws, lane * 16 + 2048, gb);
        }
        kv_write(KV_OFF + dslot * KV_T, lane, k1, ta, tb);
        CBAR();
        lds_or1(L_RDY + L * 4, 1u << dslot);
#pragma unroll
        for (int i = 0; i < 4; ++i) q1[i] = pk(acc[0][2 * i] * rq, acc[0][2 * i + 1] * rq);
      }
      __builtin_amdgcn_s_setprio(1);
      {
        const int lane = fresh_lane(), h = lane >> 5;
        f32x16 o, negm, sc[2];
        const int topA = dslot, topB = grp == 0 ? 8 : 12;
        const int top0 = grp == 0 ? topA : topB, top1 = grp == 0 ? topB : topA;
        const unsigned vlo = 1536 + (4 * h + ((lane & 15) >> 2)) * 48 + (((lane >> 4) & 1) == 0 ? 8 * (lane & 3) : ((lane & 3) == 0 ? 32 : 40));
        unsigned ka[2], kb[2], va[2], vd;
        ka[0] = KV_OFF + (top0 - 4) * KV_T + lane * 16; ka[1] = KV_OFF + (top1 - 4) * KV_T + lane * 16;
        kb[0] = KV_OFF + (top0 - 4) * KV_T + 1024 + lane * 8; kb[1] = KV_OFF + (top1 - 4) * KV_T + 1024 + lane * 8;
        va[0] = KV_OFF + (top0 - 4) * KV_T + vlo; va[1] = KV_OFF + (top1 - 4) * KV_T + vlo;
        vd = KV_OFF + dslot * KV_T + vlo;
        asm volatile("" : "+v"(ka[0]), "+v"(ka[1]), "+v"(kb[0]), "+v"(kb[1]), "+v"(va[0]), "+v"(va[1]), "+v"(vd));
        const int ql = (lane & 31) - 4 * h;
        unsigned rdyv = lds_ld(L_RDY + L * 4);
        {
          f32x16 z;
#pragma unroll
          for (int r = 0; r < 16; ++r) { z[r] = 0.f; o[r] = 0.f; }
          sc[0] = mfma32(lds128(KV_OFF + dslot * KV_T + lane * 16), q1, z);
          sc[0] = mfma32k8(lds64(KV_OFF + dslot * KV_T + 1024 + lane * 8), q2, sc[0]);
#pragma unroll
          for (int r = 0; r < 16; ++r) sc[0][r] = ((r & 3) + 8 * (r >> 2)) <= ql ? sc[0][r] : NEG;
          const float m = max_halves(max16(sc[0]));
#pragma unroll
          for (int r = 0; r < 16; ++r) { sc[0][r] -= m; negm[r] = -m; }
        }
        unsigned rdy = ufl(rdyv);
        sfor<0, 9>([&](auto N) INL {
          constexpr int n = N.v;
          f32x16& s = sc[n & 1];
          f32x16& sn = sc[(n + 1) & 1];
          v4u k1; v2u k2, v0, v1, v2, v3;
          if constexpr (n == 0) {
            if (band < 7) {
              DRAIN();
              gst32(tag, ws, lane == 0 ? OFF_HALOF + (((L * 8 + band) * 32 + j) * 2 + (qt >> 2)) * 256 + (qt & 3) * 4 : WS_USED);
            }
          }
          {
            const unsigned a = n == 0 ? vd : va[(n - 1) >> 2] + (3 - ((n - 1) & 3)) * KV_T;
            v0 = ldstr(a); v1 = ldstr(a + 384); v2 = ldstr(a + 768); v3 = ldstr(a + 1152);
          }
          if constexpr (n < 8) {
            lds_wait_bits(L_RDY + L * 4, 1u << ((n < 4 ? top0 : top1) - 1 - (n & 3)), rdy, rg.bud);
            k1 = lds128(ka[n >> 2] + (3 - (n & 3)) * KV_T); k2 = lds64(kb[n >> 2] + (3 - (n & 3)) * KV_T);
          }
          if constexpr (n == 4 || n == 8) {
            if (grp == (n == 4 ? 0 : 1)) {
#pragma unroll
              for (int r = 0; r < 16; ++r) s[r] = ((r & 3) + 8 * (r >> 2)) > ql ? s[r] : NEG;
            }
          }
          SCHED();
          v4u p, p2;
          float e[16];
          e[0] = ex2(s[0]); e[1] = ex2(s[1]); e[2] = ex2(s[2]); SCHED();
          p[0] = pk(e[0], e[1]); e[3] = ex2(s[3]); e[4] = ex2(s[4]); SCHED();
          p[1] = pk(e[2], e[3]); e[5] = ex2(s[5]); e[6] = ex2(s[6]); SCHED();
          if constexpr (n < 8) sn = mfma32(k1, q1, negm);
          SCHED();
          p[2] = pk(e[4], e[5]); e[7] = ex2(s[7]); e[8] = ex2(s[8]); SCHED();
          p[3] = pk(e[6], e[7]); SCHED();
          if constexpr (n < 8) sn = mfma32k8(k2, q2, sn);
          SCHED();
          e[9] = ex2(s[9]); e[10] = ex2(s[10]); SCHED();
          p2[0] = pk(e[8], e[9]); e[11] = ex2(s[11]); SCHED();
          o = mfma32(v4u{v0[0], v0[1], v1[0], v1[1]}, p, o);
          SCHED();
          e[12] = ex2(s[12]); SCHED();
          p2[1] = pk(e[10], e[11]); e[13] = ex2(s[13]); e[14] = ex2(s[14]); e[15] = ex2(s[15]); SCHED();
          p2[2] = pk(e[12], e[13]); SCHED();
          p2[3] = pk(e[14], e[15]); SCHED();
          o = mfma32(v4u{v2[0], v2[1], v3[0], v3[1]}, p2, o);
          SCHED();
        });
        if (__builtin_expect(w_any(!(o[12] < 1e18f)), 0)) {
          float m = NEG;
#pragma unroll
          for (int r = 0; r < 16; ++r) { o[r] = 0.f; negm[r] = 0.f; }
#pragma unroll 1
          for (int n = 0; n < 9; ++n) {
            const int sl = n < 5 ? dslot - n : topB + 4 - n;
            const unsigned kt = KV_OFF + sl * KV_T;
            f32x16 s = mfma32(lds128(kt + lane * 16), q1, negm);
            s = mfma32k8(lds64(kt + 1024 + lane * 8), q2, s);
#pragma unroll
            for (int r = 0; r < 16; ++r) {
              const bool lo = ((r & 3) + 8 * (r >> 2)) <= ql;
              s[r] = (n == 0 && !lo) || (n == 4 && lo) ? NEG : s[r];
            }
            const float mn = fmaxf(m, max_halves(max16(s))), f = ex2(m - mn);
            m = mn;
            v4u p, p2;
#pragma unroll
            for (int i = 0; i < 4; ++i) { p[i] = pk(ex2(s[2 * i] - m), ex2(s[2 * i + 1] - m)); p2[i] = pk(ex2(s[8 + 2 * i] - m), ex2(s[9 + 2 * i] - m)); }
#pragma unroll
            for (int r = 0; r < 16; ++r) o[r] *= f;
            const v2u v0 = ldstr(kt + vlo), v1 = ldstr(kt + vlo + 384), v2 = ldstr(kt + vlo + 768), v3 = ldstr(kt + vlo + 1152);
            o = mfma32(v4u{v0[0], v0[1], v1[0], v1[1]}, p, o);
            o = mfma32(v4u{v2[0], v2[1], v3[0], v3[1]}, p2, o);
          }
        }
        CBAR();
        lds_st(rg.prog, rg.ls + 9);
        CBAR();
        const float inv = __builtin_amdgcn_rcpf(o[12]);
        v4u o1;
#pragma unroll
        for (int i = 0; i < 4; ++i) o1[i] = pk(o[2 * i] * inv, o[2 * i + 1] * inv);
        const v2u o2 = {pk(o[8] * inv, o[9] * inv), pk(o[10] * inv, o[11] * inv)};
        const unsigned ob = OFF_OA + L * OA_SZ + (2 * t32i) * OA_T + (5 * (j >> 1)) * 256;
        const unsigned tv = ((lane >> 4) & 1) * OA_T + (lane & 15) * 16;
        gst128(o1, ws, tv + h * 256, ob + (3 * (j & 1)) * 256);
        gst64(o2, ws, tv + (h ? WS_USED : 0u), ob + 2 * 256 + (j & 1) * 8);
        if (L == 0) lds_wait_gt(L_W2OK, 0u, rg.bud);
        ctr_arrive(wsp, 4 * L, t32i);
        if (L == 0) {
          const int g4 = lane >> 4;
          const unsigned ro = ((t16i & 1) * 16 + (lane & 15)) * 16 + (g4 & 1) * 8, rb = OFF_XA + (t16i >> 1) * XA_T + (5 * ct) * 512;
          const v2u r0 = gld64(ws, ro + (g4 >> 1) * 512, rb), r1 = gld64(ws, ro + (g4 >> 1) * 512 + 1024, rb), r2 = gld64(ws, g4 < 2 ? ro + 2048 : WS_USED, rb);
          res[0] = r0[0]; res[1] = r0[1]; res[2] = r1[0]; res[3] = r1[1]; res[4] = r2[0]; res[5] = r2[1];
        }
      }
      __builtin_amdgcn_s_setprio(3);
      gemm16<20, WO_, 3, 8, 9, TabO>(rg, ws, OFF_OA + L * OA_SZ + t16i * OA_T, res, OFF_XA + (2 * L + 1) * XA_SZ + (t16i >> 1) * XA_T,
                                     OFF_ST + (2 * L + 1) * ST_SZ + (t16i >> 1) * 4096, false, out, t16i, ct,
                                     [&]() INL { ctr_wait(wsp, 4 * L, t16i >> 1, rep, rg.bud); });
      ctr_arrive(wsp, 4 * L + 1, t16i >> 1);
      __builtin_amdgcn_s_setprio(2);
      {
        f32x4 acc[6][2]; float rs[2], nm[2]; v4u cf[6];
        gemmG<WG_, 2, 13, 14, TabG, L_CG>(acc, rs, nm, cf, rg, ws, OFF_XA + (2 * L + 1) * XA_SZ + t32i * XA_T, OFF_ST + (2 * L + 1) * ST_SZ + t32i * 4096,
                                            [&]() INL { ctr_wait(wsp, 4 * L + 1, t32i, rep, rg.bud); });
        const int lane = fresh_lane(), g4 = lane >> 4;
        const unsigned hb = OFF_HA + L * HA_SZ + (2 * t32i) * HA_T + (6 * j) * 256;
        unsigned e2[2][2];
#pragma unroll
        for (int tt = 0; tt < 2; ++tt) {
          const float r2 = rs[tt] * rs[tt];
          unsigned hp[6];
#pragma unroll
          for (int bk = 0; bk < 3; ++bk) {
            float hv[4];
#pragma unroll
            for (int r = 0; r < 4; ++r) {
              const unsigned cg = cf[2 * bk][r], cu = cf[2 * bk + 1][r];
              hv[r] = fmaf(nm[tt], asf(cg), acc[2 * bk][tt][r]) * r2 * fmaf(nm[tt], asf(cu), acc[2 * bk + 1][tt][r]);
            }
            hp[2 * bk] = pk(hv[0], hv[1]); hp[2 * bk + 1] = pk(hv[2], hv[3]);
          }
          gst128(v4u{hp[0], hp[1], hp[2], hp[3]}, ws, lane * 16 + tt * HA_T, hb);
          e2[tt][0] = hp[4]; e2[tt][1] = hp[5];
        }
        auto s0 = __builtin_amdgcn_permlane16_swap(e2[0][0], e2[1][0], false, false);
        auto s1 = __builtin_amdgcn_permlane16_swap(e2[0][1], e2[1][1], false, false);
        gst128(v4u{s0[0], s1[0], s0[1], s1[1]}, ws, (g4 & 1) * HA_T + 1024 + (g4 >> 1) * 256 + (lane & 15) * 16, hb);
      }
      ctr_arrive(wsp, 4 * L + 2, t32i);
      __builtin_amdgcn_s_setprio(1);
      gemm16<48, WD_, 3, 25, 26, TabD>(rg, ws, OFF_HA + L * HA_SZ + t16i * HA_T, res, OFF_XA + 2 * XA_SZ + (t16i >> 1) * XA_T,
                                       OFF_ST + 2 * ST_SZ + (t16i >> 1) * 4096, L == 1, out, t16i, ct,
                                       [&]() INL { ctr_wait(wsp, 4 * L + 2, t16i >> 1, rep, rg.bud);});
      if (L == 0) ctr_arrive(wsp, 3, t16i >> 1);
    }
  } else {
    const int lane = fresh_lane();
    const int half = 2 - wave;
    __builtin_amdgcn_s_setprio(3);
    unsigned bud = 0;
    unsigned tag = 0;
    if (wave == 3) { lds_wait_gt(L_TAGV, 0u, bud); tag = ufl(lds_ld(L_TAG)); }
    if (wave < 3) {
      const int hx = wave - 1;
      {
        const unsigned xa = OFF_XA + (band * 8 + (j >> 2)) * XA_T + ((j & 3) * 8 + (lane & 7)) * 16 + (hx * 40 + (lane >> 3)) * 512;
        unsigned la = STG_X + hx * 5120 + (lane & 7) * 128 + (lane >> 3) * 16;
        unsigned sa = OFF_ST + (band * 8 + (j >> 2)) * 4096 + st_line(hx * 8 + (lane >> 3), (j >> 1) & 1) + (lane & 7) * 16 + (j & 1) * 8;
        unsigned fa = lane < 32 ? OFF_IN + (band * 32 + lane) * 512 + 256 + (j * 2 + hx) * 4 : OOR;
        asm volatile("s_waitcnt vmcnt(0)" : "+v"(la), "+v"(sa), "+v"(fa) :: "memory");
        v4u xv[5];
#pragma unroll
        for (int i = 0; i < 5; ++i) xv[i] = lds128(la + i * 1024);
#pragma unroll
        for (int i = 0; i < 5; ++i) gst128(xv[i], ws, xa + i * 4096, 0);
        float s = 0.f, qq = 0.f;
#pragma unroll
        for (int i = 0; i < 5; ++i) {
          s = sum8(xv[i], s);
#pragma unroll
          for (int k = 0; k < 4; ++k) { const unsigned u = xv[i][k]; qq = dot2(u, u, qq); }
        }
        gst64(v2u{asu(s), asu(qq)}, ws, sa, 0);
        lds_wait_gt(L_TAGV, 0u, bud);
        tag = ufl(lds_ld(L_TAG));
        DRAIN();
        gst32(tag, ws, fa);
      }
      lds_add1(L_CRIT);
    } else {
      for (;;) {
        const unsigned m = (unsigned)w_ballot(rmw_poll_pairs((GAS unsigned*)(wsp + OFF_IN + (band * 32 + j) * 512 + 256), lane * 8, 0xffffffffu, tag));
        lds_st(L_XRDY, m);
        if (m == 0xffffffffu) break;
        SPEND(bud);
      }
      lds_wait_gt(L_WQOK, 0u, bud);
      v4u pc[10];
      sfor<0, 10>([&](auto I) INL { pc[I.v] = lda<I.v>(ws, lane * 16, OFF_CSQ + j * 10240); });
      f32x4 c = __builtin_bit_cast(f32x4, pc[0]);
#pragma unroll
      for (int i = 1; i < 10; ++i) c += __builtin_bit_cast(f32x4, pc[i]);
#pragma unroll
      for (int i = 0; i < 4; ++i) c[i] = sum_halves(sum_rows16(c[i]));
      if (lane < 16) csum_rows(c, L_CQ + lane * 32);
      CBAR();
      lds_st(L_CSUM, 1u);
    }
    __builtin_amdgcn_s_setprio(0);
    if (wave == 3) {
      lds_wait_gt(L_PROG, 3u, bud);
      for (;;) {
        const v4u f0 = gld128(ws, lane * 16, OFF_F2);
        const bool ok = f0[0] == tag && f0[1] == tag && f0[2] == tag && f0[3] == tag;
        if (w_all(ok)) break;
        SPEND(bud);
        __builtin_amdgcn_s_sleep(16);
        CBAR();
      }
      CBAR();
      lds_st(L_W2OK, 1u);
      if (blockIdx.x == 0) gst32(tag, ws, lane == 0 ? OFF_EP : WS_USED);
      f32x4 c = {0.f, 0.f, 0.f, 0.f};
      {
        v4u pc[10];
        sfor<0, 10>([&](auto I) INL { pc[I.v] = lda<I.v>(ws, lane * 16, OFF_CSG + j * 10240); });
#pragma unroll
        for (int i = 0; i < 10; ++i) c += __builtin_bit_cast(f32x4, pc[i]);
      }
#pragma unroll
      for (int i = 0; i < 4; ++i) c[i] = sum_halves(c[i]);
      if (lane < 24) *(LAS f32x4*)(unsigned)(L_CG + lane * 16) = c;
      CBAR();
      lds_st(L_CSUM, 2u);
    }
    auto halo = [&](const int L) INL {
      if (wave == 3) return;
      if (band == 0) {
        if (L > 0) prog_wait(L * NSTAGE, bud); else lds_wait_gt(L_STG, 7u, bud);
        const v4u z = {0u, 0u, 0u, 0u};
#pragma unroll 1
        for (int sl = 0; sl < 4; ++sl) kv_write(KV_OFF + (8 * half + sl) * KV_T, lane, z, v4u{lane < 32 ? 0xf149u : 0u, 0u, 0u, 0u}, z);
        CBAR();
        lds_or1(L_RDY + L * 4, 0xfu << (8 * half));
        return;
      }
#pragma unroll 1
      for (int hf = 1; hf >= 0; --hf) {
        __builtin_amdgcn_s_setprio(0);
        lds_wait_gt(L_PROG + (1 - hf) * 16, L * NSTAGE + 6, bud);
        if (L > 0) prog_wait(L * NSTAGE, bud); else lds_wait_gt(L_STG, 7u, bud);
        __builtin_amdgcn_s_setprio(3);
        unsigned pend = 3u;
        GAS unsigned* fl = (GAS unsigned*)(wsp + OFF_HALOF + (((L * 8 + band - 1) * 32 + j) * 2 + hf) * 256 + half * 8);
        const unsigned gb = OFF_KVH + L * KVH_SZ + (((band - 1) * 32 + j) * 8 + 4 * hf + 2 * half) * KVH_T;
        while (pend) {
          const unsigned nw = (unsigned)w_ballot(rmw_poll_lanes(fl, lane * 4, 3u, 0u, 0u) == tag) & pend;
          __builtin_amdgcn_fence(__ATOMIC_ACQUIRE, "wavefront");
          CBAR();
          if (!nw) {
            SPEND(bud);
            continue;
          }
          pend &= ~nw;
          v4u x[2][3];
#pragma unroll
          for (int sl = 1; sl >= 0; --sl) {
            const unsigned vo = lane * 16 + ((nw >> sl & 1) ? 0u : WS_USED);
#pragma unroll
            for (int i = 0; i < 3; ++i) x[sl][i] = gld128(ws, vo + i * 1024, gb + sl * KVH_T);
          }
#pragma unroll
          for (int sl = 1; sl >= 0; --sl)
            if (nw >> sl & 1) {
              kv_write(KV_OFF + (8 * hf + 2 * half + sl) * KV_T, lane, x[sl][0], x[sl][1], x[sl][2]);
              CBAR();
              lds_or1(L_RDY + L * 4, 1u << (8 * hf + 2 * half + sl));
            }
        }
      }
      __builtin_amdgcn_s_setprio(0);
    };
    halo(0);
    v4u hold[3][10];
    lds_wait_gt(L_W2OK, 0u, bud);
    prog_wait(8, bud);
    sfor<0, 30>([&](auto I) INL { hold[I.v / 10][I.v % 10] = lda<I.v % 10>(ws, lane * 16, OFF_WD + ct * 122880 + (2 + 3 * (I.v / 10) + wave) * UNIT); });
    auto restore = [&](const int L) INL {
      sfor<0, 3>([&](auto K) INL {
        const int f = 2 + 3 * K.v + wave;
        const unsigned la = ARENA + (unsigned)((0xCBA9876ED543ull >> (4 * f)) & 15ull) * UNIT + lane * 16;
        prog_wait(L * NSTAGE + (f < 5 ? 12 : 13) + f, bud);
        sfor<0, 10>([&](auto I) INL { *(LAS v4u*)(la + I.v * 1024) = hold[K.v][I.v]; });
        CBAR();
        lds_wait_gt(L_LANDED, L * NFILL + 25 + f - 1, bud);
        lds_st(L_LANDED, L * NFILL + 25 + f + 1);
      });
    };
    restore(0);
    halo(1);
    restore(1);
  }
}

extern "C" void kernel_launch(void* const* d_in, const int* in_sizes, int n_in, void* d_out, int out_size, void* d_ws, size_t ws_size,
                              hipStream_t stream) {
  typedef const unsigned char* cp;
  mega_kernel<<<256, 768, 0, stream>>>((unsigned char*)d_ws, d_out, (cp)d_in[0], (cp)d_in[1], (cp)d_in[2], (cp)d_in[4], (cp)d_in[5], (cp)d_in[6]);
}
```
